```python
import torch

def _softmax(x, axis=-1):
    return torch.softmax(x, dim=axis)

def _gelu(x):
    return torch.nn.functional.gelu(x, approximate='tanh')

def _silu(x):
    return torch.nn.functional.silu(x)

def _sigmoid(x):
    return torch.sigmoid(x)

def _layernorm(x, axis=-1, eps=1e-05):
    m = x.mean(dim=axis, keepdim=True)
    v = x.var(dim=axis, keepdim=True, unbiased=False)
    return (x - m) / torch.sqrt(v + eps)

def _scatter_add(tgt, idx, src, axis=0):
    return tgt.clone().index_add_(axis, idx, src)

def _segment_sum(src, seg, n):
    out = torch.zeros((n,) + src.shape[1:], dtype=src.dtype)
    return out.index_add_(0, seg, src)

def _pad(x, pw):
    flat = tuple((v for pair in reversed(pw) for v in pair))
    return torch.nn.functional.pad(x, flat)
kernel_name = 'adaLN_cond__gqa_block_cached__glu_gated_geglu__decode'

def setup_inputs():

    _w = lambda a: (lambda t: t.to(torch.bfloat16) if t.is_floating_point() else t)(torch.as_tensor(a))

    def _A_setup():
        B, Sq, D = (1, 1, 1024)
        Hq, Hkv, Sk, Dh = (32, 4, 65536, 128)
        assert Hq % Hkv == 0, (Hq, Hkv)
        return {'x': _w((lambda *s: torch.randn(*s, dtype=torch.bfloat16) * 0.05)(B, Sq, D)), 'k_cache': _w((lambda *s: torch.randn(*s, dtype=torch.bfloat16) * 3.2)(B, Hkv, Sk, Dh)), 'v_cache': _w((lambda *s: torch.randn(*s, dtype=torch.bfloat16) * 3.2)(B, Hkv, Sk, Dh)), 'wq': _w((lambda *s: torch.randn(*s, dtype=torch.bfloat16) * 3.2)(D, Hq * Dh) * D ** (-0.5)), 'wo': _w((lambda *s: torch.randn(*s, dtype=torch.bfloat16) * 3.2)(Hq * Dh, D) * (Hq * Dh) ** (-0.5))}
    _A_in = _A_setup()

    def _B_setup():
        B, S, Dm, Df = (1, 1, 1024, 3584)
        return {'x': _w((lambda *s: torch.randn(*s, dtype=torch.bfloat16) * 0.05)(B, S, Dm)), 'w_gate': _w((lambda *s: torch.randn(*s, dtype=torch.bfloat16) * 3.2)(Dm, Df) * Dm ** (-0.5)), 'w_up': _w((lambda *s: torch.randn(*s, dtype=torch.bfloat16) * 3.2)(Dm, Df) * Dm ** (-0.5)), 'w_down': _w((lambda *s: torch.randn(*s, dtype=torch.bfloat16) * 3.2)(Df, Dm) * Df ** (-0.5))}
    _B_in = _B_setup()
    return {'A_x': _A_in['x'], 'A_k_cache': _A_in['k_cache'], 'A_v_cache': _A_in['v_cache'], 'A_wq': _A_in['wq'], 'A_wo': _A_in['wo'], 'B_x': _B_in['x'], 'B_w_gate': _B_in['w_gate'], 'B_w_up': _B_in['w_up'], 'B_w_down': _B_in['w_down'], 'c': _w((lambda *s: torch.randn(*s, dtype=torch.bfloat16) * 0.4)(1, 1024) * 8.0), 'w_mod': _w((lambda *s: torch.randn(*s, dtype=torch.bfloat16) * 0.4)(1024, 6 * 1024) * 1024 ** (-0.5) * 8.0)}

def reference(A_x, A_k_cache, A_v_cache, A_wq, A_wo, B_x, B_w_gate, B_w_up, B_w_down, c, w_mod):

    def _A_ref(x, k_cache, v_cache, wq, wo):
        B, Sq, D = x.shape
        Hq, Dh = (32, 128)
        Hkv = k_cache.shape[1]
        rep = Hq // Hkv
        q = (lambda x, *d: x.permute(*d))((x @ wq).reshape(B, Sq, Hq, Dh), 0, 2, 1, 3)
        k = (lambda x, n, axis: torch.repeat_interleave(x, n, dim=axis))(k_cache, rep, axis=1)
        v = (lambda x, n, axis: torch.repeat_interleave(x, n, dim=axis))(v_cache, rep, axis=1)
        scores = torch.einsum('bhqd,bhkd->bhqk', q, k) * Dh ** (-0.5)
        p = _softmax(scores, axis=-1)
        o = torch.einsum('bhqk,bhkd->bhqd', p, v)
        return (lambda x, *d: x.permute(*d))(o, 0, 2, 1, 3).reshape(B, Sq, Hq * Dh) @ wo

    def _B_ref(x, w_gate, w_up, w_down):
        g = torch.einsum('bsd,df->bsf', x, w_gate)
        u = torch.einsum('bsd,df->bsf', x, w_up)
        h = _gelu(g) * u
        return torch.einsum('bsf,fd->bsd', h, w_down)
    x = A_x
    mod = (_silu(c) @ w_mod).reshape(c.shape[0], 6, 1, -1)
    sa, ca, ga, sb, cb, gb = (mod[:, 0].clone(), mod[:, 1].clone(), mod[:, 2].clone(), mod[:, 3].clone(), mod[:, 4].clone(), mod[:, 5].clone())
    h = _layernorm(x, axis=-1) * (1 + ca) + sa
    x = x + ga * _A_ref(h, A_k_cache, A_v_cache, A_wq, A_wo)
    h = _layernorm(x, axis=-1) * (1 + cb) + sb
    x = x + gb * _B_ref(h, B_w_gate, B_w_up, B_w_down)
    return x
```

```cpp
#include <hip/hip_runtime.h>
#include <hip/hip_bf16.h>
#include <stdint.h>
#define MIMIC 1
typedef unsigned short u16;
typedef unsigned v4u __attribute__((ext_vector_type(4)));
__device__ __forceinline__ float bf2f(u16 b){ return __uint_as_float(((unsigned)b)<<16); }
__device__ __forceinline__ float rbf(float f){
  unsigned u=__float_as_uint(f); u += 0x7FFFu + ((u>>16)&1u); return __uint_as_float(u & 0xFFFF0000u); }
__device__ __forceinline__ float R(float f){ return MIMIC ? rbf(f) : f; }
__device__ __forceinline__ u16 f2bf(float f){ unsigned u=__float_as_uint(f); u += 0x7FFFu + ((u>>16)&1u); return (u16)(u>>16); }

__device__ void gemv64(const float* xs  , const u16* W, int K, int N, int col0, float* red  , float* out64  ){
  int t=threadIdx.x; int cg=t&7, ks=t>>3;
  float acc[8]={0,0,0,0,0,0,0,0};
  for(int k=ks;k<K;k+=32){
    v4u w=*(const v4u*)(W+(size_t)k*N+col0+cg*8);
    float xv=xs[k];
    for(int j=0;j<4;j++){ acc[2*j]+=xv*__uint_as_float(w[j]<<16); acc[2*j+1]+=xv*__uint_as_float(w[j]&0xFFFF0000u);} }
  for(int j=0;j<8;j++) red[ks*64+cg*8+j]=acc[j];
  __syncthreads();
  if(t<64){ float s=0; for(int i=0;i<32;i++) s+=red[i*64+t]; out64[t]=s; }
  __syncthreads();
}
__device__ float blocksum(float v, float* tmp){
  for(int o=32;o>0;o>>=1) v+=__shfl_xor(v,o);
  __syncthreads();
  if((threadIdx.x&63)==0) tmp[threadIdx.x>>6]=v;
  __syncthreads();
  float s=0; for(int i=0;i<(int)(blockDim.x>>6);i++) s+=tmp[i];
  return s;
}
__device__ void lnmod(const float* xin  , const float* shift, const float* scale, float* xs, float* tmp){
  int t=threadIdx.x; float s=0; for(int i=t;i<1024;i+=256) s+=xin[i];
  float mean=blocksum(s,tmp)/1024.f;
  float q=0; for(int i=t;i<1024;i+=256){ float d=xin[i]-mean; q+=d*d; }
  float var=blocksum(q,tmp)/1024.f;
  float m=R(mean), v=R(var); float den=R(sqrtf(R(v+1e-5f)));
  for(int i=t;i<1024;i+=256){ float ln=R(R(xin[i]-m)/den); xs[i]=R(R(ln*R(1.f+scale[i]))+shift[i]); }
  __syncthreads();
}
__global__ void k_mod(const u16* c, const u16* wmod, float* mod){
  __shared__ float xs[1024], red[2048], o[64];
  for(int i=threadIdx.x;i<1024;i+=256){ float v=bf2f(c[i]); xs[i]=R(v/(1.f+expf(-v))); }
  __syncthreads();
  gemv64(xs,wmod,1024,6144,blockIdx.x*64,red,o);
  if(threadIdx.x<64) mod[blockIdx.x*64+threadIdx.x]=R(o[threadIdx.x]);
}
__global__ void k_q(const u16* x, const float* mod, const u16* wq, float* q){
  __shared__ float xi[1024], xs[1024], red[2048], o[64], tmp[8];
  for(int i=threadIdx.x;i<1024;i+=256) xi[i]=bf2f(x[i]);
  __syncthreads();
  lnmod(xi,mod,mod+1024,xs,tmp);
  gemv64(xs,wq,1024,4096,blockIdx.x*64,red,o);
  if(threadIdx.x<64) q[blockIdx.x*64+threadIdx.x]=R(o[threadIdx.x]);
}
__global__ void k_attn(const float* q, const u16* K, const u16* V, float* pm, float* pl, float* po){
  __shared__ float qs[1024], ps[8*256], tmp[8];
  int sp=blockIdx.x, g=blockIdx.y, t=threadIdx.x;
  for(int i=t;i<1024;i+=256) qs[i]=q[g*1024+i];
  __syncthreads();
  const u16* kr=K+((size_t)g*65536+sp*256+t)*128;
  float s[8]={0,0,0,0,0,0,0,0};
  for(int c=0;c<16;c++){ v4u w=*(const v4u*)(kr+c*8);
    for(int j=0;j<4;j++){ float a=__uint_as_float(w[j]<<16), b=__uint_as_float(w[j]&0xFFFF0000u);
      for(int h=0;h<8;h++) s[h]+=a*qs[h*128+c*8+2*j]+b*qs[h*128+c*8+2*j+1]; } }
  for(int h=0;h<8;h++){
    float sc=R(R(s[h])*0.08838834764831845f);
    float m=sc; for(int o=32;o>0;o>>=1) m=fmaxf(m,__shfl_xor(m,o));
    __syncthreads(); if((t&63)==0) tmp[t>>6]=m; __syncthreads();
    m=fmaxf(fmaxf(tmp[0],tmp[1]),fmaxf(tmp[2],tmp[3]));
    float p=expf(sc-m); ps[h*256+t]=p;
    float l=blocksum(p,tmp);
    if(t==0){ pm[(g*8+h)*256+sp]=m; pl[(g*8+h)*256+sp]=l; }
  }
  __syncthreads();
  int d=t&127, hh=t>>7; float acc[4]={0,0,0,0};
  const u16* vr=V+((size_t)g*65536+sp*256)*128+d;
  for(int k=0;k<256;k++){ float v=bf2f(vr[(size_t)k*128]); for(int j=0;j<4;j++) acc[j]+=ps[(hh*4+j)*256+k]*v; }
  for(int j=0;j<4;j++) po[((size_t)(g*8+hh*4+j)*256+sp)*128+d]=acc[j];
}
__global__ void k_comb(const float* pm,const float* pl,const float* po,float* o){
  int h=blockIdx.x, d=threadIdx.x; float M=-INFINITY; for(int s=0;s<256;s++) M=fmaxf(M,pm[h*256+s]);
  float L=0,a=0; for(int s=0;s<256;s++){ float w=expf(pm[h*256+s]-M); L+=w*pl[h*256+s]; a+=w*po[((size_t)h*256+s)*128+d]; }
  o[h*128+d]=R(a/L);
}
__global__ void k_wo(const float* o,const u16* wo,const u16* x,const float* mod,float* x2){
  __shared__ float xs[4096], red[2048], oo[64];
  for(int i=threadIdx.x;i<4096;i+=256) xs[i]=o[i];
  __syncthreads();
  gemv64(xs,wo,4096,1024,blockIdx.x*64,red,oo);
  if(threadIdx.x<64){ int j=blockIdx.x*64+threadIdx.x; x2[j]=R(bf2f(x[j])+R(mod[2048+j]*R(oo[threadIdx.x]))); }
}
__global__ void k_gu(const float* x2,const float* mod,const u16* wg,const u16* wu,float* h2){
  __shared__ float xi[1024], xs[1024], red[2048], g[64], u[64], tmp[8];
  for(int i=threadIdx.x;i<1024;i+=256) xi[i]=x2[i];
  __syncthreads();
  lnmod(xi,mod+3072,mod+4096,xs,tmp);
  gemv64(xs,wg,1024,3584,blockIdx.x*64,red,g);
  gemv64(xs,wu,1024,3584,blockIdx.x*64,red,u);
  if(threadIdx.x<64){ float gv=R(g[threadIdx.x]), uv=R(u[threadIdx.x]);
    float ge=R(0.5f*gv*(1.f+tanhf(0.7978845608028654f*(gv+0.044715f*gv*gv*gv))));
    h2[blockIdx.x*64+threadIdx.x]=R(ge*uv); }
}
__global__ void k_down(const float* h2,const u16* wd,const float* x2,const float* mod,u16* out){
  __shared__ float xs[3584], red[2048], oo[64];
  for(int i=threadIdx.x;i<3584;i+=256) xs[i]=h2[i];
  __syncthreads();
  gemv64(xs,wd,3584,1024,blockIdx.x*64,red,oo);
  if(threadIdx.x<64){ int j=blockIdx.x*64+threadIdx.x; out[j]=f2bf(x2[j]+R(mod[5120+j]*R(oo[threadIdx.x]))); }
}
extern "C" void kernel_launch(void* const* d_in, const int* in_sizes, int n_in, void* d_out, int out_size, void* d_ws, size_t ws_size, hipStream_t stream){
  const u16* x=(const u16*)d_in[0]; const u16* K=(const u16*)d_in[1]; const u16* V=(const u16*)d_in[2];
  const u16* wq=(const u16*)d_in[3]; const u16* wo=(const u16*)d_in[4];
  const u16* wg=(const u16*)d_in[6]; const u16* wu=(const u16*)d_in[7]; const u16* wd=(const u16*)d_in[8];
  const u16* c=(const u16*)d_in[9]; const u16* wmod=(const u16*)d_in[10];
  float* ws=(float*)d_ws; float* mod=ws; float* q=ws+8192; float* o=ws+16384; float* x2=ws+24576; float* h2=ws+32768;
  float* pm=ws+65536; float* pl=pm+8192; float* po=ws+131072;
  k_mod<<<96,256,0,stream>>>(c,wmod,mod);
  k_q<<<64,256,0,stream>>>(x,mod,wq,q);
  k_attn<<<dim3(256,4),256,0,stream>>>(q,K,V,pm,pl,po);
  k_comb<<<32,128,0,stream>>>(pm,pl,po,o);
  k_wo<<<16,256,0,stream>>>(o,wo,x,mod,x2);
  k_gu<<<56,256,0,stream>>>(x2,mod,wg,wu,h2);
  k_down<<<16,256,0,stream>>>(h2,wd,x2,mod,(u16*)d_out);
}
```

```cpp
#include <hip/hip_runtime.h>
#include <stdint.h>

typedef unsigned short u16;
typedef unsigned u32;
typedef unsigned long long u64;
typedef u32 v4u __attribute__((ext_vector_type(4)));
typedef u32 v2u __attribute__((ext_vector_type(2)));
typedef float v4f __attribute__((ext_vector_type(4)));
typedef float v2f __attribute__((ext_vector_type(2)));
typedef __bf16 v8bf __attribute__((ext_vector_type(8)));
typedef __bf16 v2bf __attribute__((ext_vector_type(2)));
typedef short v4s __attribute__((ext_vector_type(4)));
#define LAS __attribute__((address_space(3)))
#define GAS __attribute__((address_space(1)))
#define DI __device__ __forceinline__
typedef __amdgpu_buffer_rsrc_t rsrc_t;

#define STAMPS 0
#define ROT 37
#define NAP0A 10
#define NAP0B 10
#define NAP1 8
#define NAP2 10
#define NAP5 64
#define PACE0 4
#define PUBNAP 0
#define PACE 0
#define DEPTH 24
#define DEPTH2 16

constexpr u32 W_EPOCH = 0;
constexpr u32 W_MOD1 = 4096;
constexpr u32 W_Q = W_MOD1 + 256 * 512;
constexpr u32 W_MOD2 = W_Q + 256 * 512;
constexpr u32 W_CS = W_MOD2 + 256 * 512;
constexpr u32 W_X = W_CS + 64 * 128;
constexpr u32 W_ML = W_X + 64 * 128;
constexpr u32 W_GU = W_ML + 32 * 64 * 16;
constexpr u32 W_A = W_GU + 56 * 16 * 512;
constexpr u32 W_D = W_A + 64 * 64 * 128;
constexpr u32 W_P = W_D + 64 * 64 * 128;
constexpr u32 W_END = W_P + 64 * 64 * 512;
constexpr u32 W_STAMP = W_END;

constexpr u32 NH = 18;
constexpr u32 L_CTRL = NH * 8192;
constexpr u32 L_MRG = L_CTRL + 256;
constexpr u32 L_QS = L_MRG + 4608;
constexpr u32 L_HS = L_QS + 2048;
constexpr u32 L_CS = L_HS + 512;
constexpr u32 L_PRIV = L_CS + 4096;
constexpr u32 L_END = L_PRIV + 4096;
static_assert(L_END <= 163840, "LDS");
constexpr u32 C_FULL = 0, C_H = 2, C_Q = 3, C_M = 4, C_CS = 5, C_TAG = 6, C_W = 7, C_FREE = 16;
constexpr float LOG2E = 1.4426950408889634f;

struct Params {
  const u16 *x, *K, *V, *wq, *wo, *wg, *wu, *wd, *c, *wmod;
  u16* out;
  char* ws;
};

template <class T> DI T ldl(u32 a) { return *(LAS T*)a; }
template <class T> DI void stl(u32 a, T v) { *(LAS T*)a = v; }
DI u32 rfl(u32 v) { return __builtin_amdgcn_readfirstlane(v); }
DI float asf(u32 v) { return __builtin_bit_cast(float, v); }
DI u32 asu(float v) { return __builtin_bit_cast(u32, v); }
DI float lo16(u32 v) { return asf(v << 16); }
DI float hi16(u32 v) { return asf(v & 0xffff0000u); }
DI u32 pk(float a, float b) { v2f t = {a, b}; return __builtin_bit_cast(u32, __builtin_convertvector(t, v2bf)); }
DI float rbf(float a) { return hi16(pk(a, a)); }
#define CBAR() asm volatile("" ::: "memory")
#define LGKM0() asm volatile("s_waitcnt lgkmcnt(0)" ::: "memory")
DI v4f mfma(v4u a, v4u b, v4f c) {
  return __builtin_amdgcn_mfma_f32_16x16x32_bf16(__builtin_bit_cast(v8bf, a), __builtin_bit_cast(v8bf, b), c, 0, 0, 0);
}
DI v2u trd(u32 addr) { return __builtin_bit_cast(v2u, __builtin_amdgcn_ds_read_tr16_b64_v4i16((LAS v4s*)addr)); }
DI v4u cat(v2u a, v2u b) { return v4u{a.x, a.y, b.x, b.y}; }
template <int CTRL> DI float dpp(float x) { return asf(__builtin_amdgcn_update_dpp(0, asu(x), CTRL, 0xf, 0xf, true)); }
constexpr int XOR1 = 0xB1, XOR2 = 0x4E, HMIR = 0x141, ROR8 = 0x128;
DI float fmx(float a, float b) { return __builtin_amdgcn_fmed3f(a, b, INFINITY); }
DI float sw16(float& a, float b) { auto s = __builtin_amdgcn_permlane16_swap(asu(a), asu(b), false, false); a = asf(s[0]); return asf(s[1]); }
DI float sw32(float& a, float b) { auto s = __builtin_amdgcn_permlane32_swap(asu(a), asu(b), false, false); a = asf(s[0]); return asf(s[1]); }
DI float sum16(float x) { const float o = sw16(x, x); return x + o; }
DI float sum32(float x) { const float o = sw32(x, x); return x + o; }
DI float max16(float x) { const float o = sw16(x, x); return fmx(x, o); }
DI float max32(float x) { const float o = sw32(x, x); return fmx(x, o); }
DI float sum_lo3(float x) { x += dpp<XOR1>(x); x += dpp<XOR2>(x); return x + dpp<HMIR>(x); }
DI float sum64(float x) { x = sum_lo3(x); x += dpp<ROR8>(x); return sum32(sum16(x)); }
DI float max64(float x) {
  x = fmx(x, dpp<XOR1>(x)); x = fmx(x, dpp<XOR2>(x)); x = fmx(x, dpp<HMIR>(x)); x = fmx(x, dpp<ROR8>(x));
  return max32(max16(x));
}
DI float sel(bool c, float a, float b) { asm("" : "+v"(a)); asm("" : "+v"(b)); return c ? a : b; }
DI float ex2(float x) { return __builtin_amdgcn_exp2f(x); }
DI float rcp(float x) { return __builtin_amdgcn_rcpf(x); }
DI float sigm(float z) { return rcp(1.f + ex2(-LOG2E * z)); }
DI u32 opq(u32 v) { asm volatile("" : "+v"(v)); return v; }
DI u64 opqs(u64 v) { asm volatile("" : "+s"(v)); return v; }
DI bool lanes(u64 m) { return __builtin_amdgcn_inverse_ballot_w64(m); }
constexpr u64 M_HI8 = 0xff00ff00ff00ff00ull, M_HI32 = 0xffffffff00000000ull;
constexpr u32 OOB = 0x80000000u;

DI u64 now() { return __builtin_amdgcn_s_memrealtime(); }
#define STAMPV(i, val) do {} while (0)
#define STAMP(i) STAMPV(i, now())

struct Sync {
  u32 base;
  template <u32 OFF> DI void wait_at(u32 addr, u32 v) {
    u32 t, s, n;
    asm volatile("s_movk_i32 %2, 0x7fff\n1:\n\tds_read_b32 %0, %3 offset:%c5\n\ts_waitcnt lgkmcnt(0)\n\tv_readfirstlane_b32 %1, %0\n\t"
                 "s_cmp_ge_u32 %1, %4\n\ts_cbranch_scc1 2f\n\ts_sleep 1\n\ts_sub_u32 %2, %2, 1\n\ts_cbranch_scc0 1b\n2:"
                 : "=&v"(t), "=&s"(s), "=&s"(n) : "v"(addr), "s"(v), "i"(OFF) : "memory", "scc");
  }
  template <u32 W> DI void wait(u32 v) { wait_at<4 * W>(base, v); }
  template <u32 W, int N> DI void add1() {
    u64 e;
    asm volatile("s_mov_b64 %0, exec\n\ts_mov_b64 exec, %4\n\tds_add_u32 %1, %2 offset:%c3\n\ts_mov_b64 exec, %0"
                 : "=&s"(e) : "v"(base + (N > 1 ? 4 * (threadIdx.x & 63) : 0)), "v"(1u), "i"(4 * W), "s"((u64)((1ull << N) - 1)) : "memory");
  }
  template <u32 OFF> DI void set_at(u32 addr, u32 v) {
    asm volatile("ds_write_b32 %0, %1 offset:%c2" :: "v"(addr), "v"(v), "i"(OFF) : "memory");
  }
  template <u32 W> DI void set(u32 v) { set_at<4 * W>(base, v); }
};
DI v4u gl16(rsrc_t r, u32 off, u32 so = 0) { return __builtin_amdgcn_raw_buffer_load_b128(r, off, so, 16); }
DI v2u gl8(rsrc_t r, u32 off) { return __builtin_amdgcn_raw_buffer_load_b64(r, off, 0, 16); }
DI void gs16(rsrc_t r, u32 off, v4u v) { __builtin_amdgcn_raw_buffer_store_b128(v, r, off, 0, 16); }
DI void gs8(rsrc_t r, u32 off, v2u v) { __builtin_amdgcn_raw_buffer_store_b64(v, r, off, 0, 16); }
template <int K> DI void nap() { if constexpr (K > 0) __builtin_amdgcn_s_sleep(K); }
DI bool all_ok(u32 bad) { return __builtin_amdgcn_ballot_w64(bad != 0u) == 0ull; }
template <int N, int NAP, class F> DI void gather16(Sync& S, rsrc_t r, u32 base, F f, v4u (&v)[N], u32 tag) {
  for (u32 n = 0;;) {
    CBAR();
#pragma unroll
    for (int i = 0; i < N; ++i) v[i] = gl16(r, base + (f(i) & 4095u), f(i) & ~4095u);
    u32 bad = 0;
#pragma unroll
    for (int i = 0; i < N; ++i) bad |= (v[i].y ^ tag) | (v[i].w ^ tag);
    if (all_ok(bad) || ++n > 8192u) break;
    nap<NAP>();
  }
  CBAR();
}

template <int D> DI void dma4(u32 ld, u32 v0, u32 v1, u32 v2, u32 v3, rsrc_t r, u32 so) {
  asm volatile("s_mov_b32 m0, %0\n\ts_nop 0\n\t"
               "buffer_load_dwordx4 %1, %5, %6 offen nt lds\n\ts_waitcnt vmcnt(%c7)\n\t"
               "buffer_load_dwordx4 %2, %5, %6 offen offset:1024 nt lds\n\ts_waitcnt vmcnt(%c7)\n\t"
               "buffer_load_dwordx4 %3, %5, %6 offen offset:2048 nt lds\n\ts_waitcnt vmcnt(%c7)\n\t"
               "buffer_load_dwordx4 %4, %5, %6 offen offset:3072 nt lds\n\ts_waitcnt vmcnt(%c7)"
               :: "s"(ld), "v"(v0), "v"(v1), "v"(v2), "v"(v3), "s"(r), "s"(so), "i"(D) : "memory");
}
template <int D> DI void dma1(u32 ld, u32 v, rsrc_t r, u32 so) {
  asm volatile("s_mov_b32 m0, %0\n\ts_nop 0\n\tbuffer_load_dwordx4 %1, %2, %3 offen nt lds\n\ts_waitcnt vmcnt(%c4)"
               :: "s"(ld), "v"(v), "s"(r), "s"(so), "i"(D) : "memory");
}
template <int D> DI void vmwait() { asm volatile("s_waitcnt vmcnt(%c0)" :: "i"(D) : "memory"); }
struct Ring {
  Sync S;
  u32 n, pos, need, full, next, lim;
  u64 stall, first;
  DI void publish(u32 f) { if ((int)(f - full) > 0) { full = f; nap<PUBNAP>(); S.set<C_FULL>(f); } }
  DI u32 open() {
    if (n >= NH) {
      const u32 fa = S.base + 4 * pos;
      if (rfl(*(volatile LAS u32*)(fa + 4 * C_FREE)) < need) {
        vmwait<0>();
        publish(n);
        S.wait_at<4 * C_FREE>(fa, need);
      }
    }
    return pos * 8192u;
  }
  template <int D> DI void close() {
    ++n; ++pos;
    if (pos == NH) { pos = 0; need += 4; }
    publish(n - D / 8);
  }
};
DI rsrc_t mk(const void* p, u32 bytes) { return __builtin_amdgcn_make_buffer_rsrc((void*)p, 0, bytes, 0x00020000); }
template <int D> DI void ld_slab(Ring& R, rsrc_t r, u32 base, u32 pitch, int nh, int lane) {
  const u32 row = lane >> 3, v = row * pitch + (((lane & 7) ^ (((row >> 1) & 3) << 1)) << 4);
#pragma unroll 1
  for (int h = 0; h < nh; ++h) {
    const u32 ld = R.open();
#pragma unroll
    for (int i = 0; i < 8; ++i) { dma1<D>(ld + i * 1024, v, r, base); base += 8 * pitch; }
    R.close<D>();
  }
}
template <int D, u32 PITCH, int SH> DI void ld_rows(Ring& R, rsrc_t r, u32 base, int lane) {
  u32 v[8];
#pragma unroll
  for (int i = 0; i < 8; ++i) v[i] = ((lane ^ (i << 1)) << 4) + i * PITCH - (i & 3) * 1024;
#pragma unroll 1
  for (int h = 0; h < 16; ++h) {
    const u32 ld = R.open(), so = base + (h >> SH) * 8 * PITCH + (h & ((1 << SH) - 1)) * 1024;
    dma4<D>(ld, v[0], v[1], v[2], v[3], r, so);
    dma4<D>(ld + 4096, v[4], v[5], v[6], v[7], r, so);
    R.close<D>();
  }
}

DI v4u xfrag(u32 addr, int k) { return cat(ldl<v2u>(addr + k * 64), ldl<v2u>(addr + k * 64 + 32)); }
template <int NK> DI v4f gemv_cols(u32 n0, u32 xaddr, int c, int lane) {
  const u32 g = lane >> 4, r16 = 4 * g + ((lane & 15) >> 2);
  const u32 a = opq(n0 * 8192 + r16 * 128 + ((c ^ ((r16 >> 1) & 3)) << 5) + (lane & 3) * 8), xa = opq(xaddr + g * 8);
  v4f acc = {0.f, 0.f, 0.f, 0.f};
#pragma unroll
  for (int k = 0; k < NK; ++k) acc = mfma(cat(trd(a + k * 4096), trd(a + k * 4096 + 2048)), xfrag(xa, k), acc);
  return acc;
}
DI void put_tile(rsrc_t ws, u32 off, v4f acc, u32 tag, int lane) {
  const bool hi = lanes(M_HI8);
  gs16(ws, (lane & 7) ? OOB : off + (lane >> 4) * 32 + (lane & 8) * 2, v4u{asu(sel(hi, acc.z, acc.x)), tag, asu(sel(hi, acc.w, acc.y)), tag});
}
DI u32 hs_addr(u32 n) { return (n % NH) * 8192u; }
template <int SH> DI void read_rows(u32 n0, int v, int lane, v4u (&wf)[16]) {
  const u32 g = lane >> 4, r8 = (4 * g + ((lane & 15) >> 2)) & 7;
  const u32 lo = r8 * 1024 + ((v ^ r8) << 5) + (lane & 3) * 8;
#pragma unroll
  for (int q = 0; q < 4; ++q) {
    const int half = SH ? q >> 1 : 0, k = SH ? q & 1 : q;
    u32 a[2];
#pragma unroll
    for (int t = 0; t < 2; ++t) {
      const u32 h0 = hs_addr(n0 + ((4 * k + 2 * t) << SH) + half), h1 = hs_addr(n0 + ((4 * k + 2 * t + 1) << SH) + half);
      a[t] = lo + (lanes(M_HI32) ? h1 : h0);
    }
#pragma unroll
    for (int u = 0; u < 4; ++u) wf[SH ? (half * 4 + u) * 2 + k : 4 * k + u] = cat(trd(a[0] + u * 256), trd(a[1] + u * 256));
  }
}
struct Masks { u64 m[8]; };
DI Masks mk_masks() {
  Masks M;
#pragma unroll
  for (int i = 0; i < 8; ++i) M.m[i] = opqs(0x0101010101010101ull << i);
  return M;
}
DI v4u tiles8(const v4u (&wf)[16], v4u x0, v4u x1, const Masks& M, u32 tag) {
  v4f acc[8];
#pragma unroll
  for (int i = 0; i < 8; ++i) acc[i] = mfma(wf[2 * i], x0, v4f{0.f, 0.f, 0.f, 0.f});
#pragma unroll
  for (int i = 0; i < 8; ++i) acc[i] = mfma(wf[2 * i + 1], x1, acc[i]);
  const bool hi = lanes(M_HI8);
  float o0 = sel(hi, acc[0].z, acc[0].x), o1 = sel(hi, acc[0].w, acc[0].y);
#pragma unroll
  for (int i = 1; i < 8; ++i) {
    o0 = sel(lanes(M.m[i]), sel(hi, acc[i].z, acc[i].x), o0);
    o1 = sel(lanes(M.m[i]), sel(hi, acc[i].w, acc[i].y), o1);
  }
  return v4u{asu(o0), tag, asu(o1), tag};
}
DI u32 tile_of(int lane, int v) { return 32 * ((lane >> 2) & 1) + 8 * (lane & 3) + v; }

struct Ctx { rsrc_t ws; u32 tag; int lane, w, wg, grp, s; };

struct XIn { v4u xa, xb; u32 xp; };
DI XIn load_x(const Params& p, const Ctx& c) {
  return XIn{*(const GAS v4u*)(p.x + c.lane * 16), *(const GAS v4u*)(p.x + c.lane * 16 + 8),
             *(const GAS u32*)(p.x + 256 * (c.wg >> 6) + 64 * c.w + 2 * (c.lane & 31))};
}
DI float make_ln(const XIn& X) {
  const v4u xa = X.xa, xb = X.xb;
  float sm = 0.f;
#pragma unroll
  for (int i = 0; i < 4; ++i) sm += (lo16(xa[i]) + hi16(xa[i])) + (lo16(xb[i]) + hi16(xb[i]));
  const float mean = sum64(sm) * (1.f / 1024.f);
  float q = 0.f;
#pragma unroll
  for (int i = 0; i < 4; ++i) {
    const float a = lo16(xa[i]) - mean, b = hi16(xa[i]) - mean, d = lo16(xb[i]) - mean, e = hi16(xb[i]) - mean;
    q += (a * a + b * b) + (d * d + e * e);
  }
  const float var = sum64(q) * (1.f / 1024.f);
  const float mb = rbf(mean), den = rbf(sqrtf(rbf(rbf(var) + 1e-5f)));
  return asf(opq(asu(rbf(rbf((lanes(M_HI32) ? hi16(X.xp) : lo16(X.xp)) - mb) / den))));
}
DI void make_h(const Params& p, const Ctx& c, Sync& S, float ln) {
  const int lane = c.lane, kq = c.wg >> 6;
  const u32 base = opq(W_MOD1 + (32 * (lane >> 5) + 4 * kq + c.w) * 512 + (lane & 31) * 16);
  const u32 ha = opq(L_HS + (64 * c.w + 2 * (lane & 31) + (lane >> 5)) * 2);
  v4u v[8];
  nap<NAP0A>();
  gather16<8, 4>(S, c.ws, base, [](int i) { return (u32)(64 * (i & 3) + 16 * (i >> 2)) * 512; }, v, c.tag);
  if (c.w == 0) { STAMP(17); }
  float s0 = (asf(v[0].x) + asf(v[1].x)) + (asf(v[2].x) + asf(v[3].x)), c0 = (asf(v[4].x) + asf(v[5].x)) + (asf(v[6].x) + asf(v[7].x));
  s0 += sw32(s0, (asf(v[0].z) + asf(v[1].z)) + (asf(v[2].z) + asf(v[3].z)));
  c0 += sw32(c0, (asf(v[4].z) + asf(v[5].z)) + (asf(v[6].z) + asf(v[7].z)));
  const float h = rbf(ln * rbf(1.f + rbf(c0))) + rbf(s0);
  stl<u16>(ha, (u16)pk(h, h));
  S.add1<C_H, 1>();
}
DI void make_q(const Params& p, const Ctx& c, Sync& S) {
  const int lane = c.lane;
  v4u v[8];
  const u32 base = W_Q + (16 * c.grp + 4 * c.w + (lane >> 5)) * 512 + (lane & 31) * 16;
  gather16<8, 4>(S, c.ws, base, [](int i) { return (u32)(64 * (i & 3) + 2 * (i >> 2)) * 512; }, v, c.tag);
#pragma unroll
  for (int i = 0; i < 8; i += 4)
    stl<u32>(L_QS + (128 * c.w + 16 * i + lane) * 4,
             pk((asf(v[i].x) + asf(v[i + 1].x)) + (asf(v[i + 2].x) + asf(v[i + 3].x)), (asf(v[i].z) + asf(v[i + 1].z)) + (asf(v[i + 2].z) + asf(v[i + 3].z))));
  S.add1<C_Q, 1>();
}

DI float combine_part(const Params& p, const Ctx& c, Sync& S) {
  const int lane = c.lane, a = 16 * c.grp + c.s, sl = 8 * c.w + (lane >> 3);
  const u32 base = opq(W_P + (a * 64 + sl) * 512 + (lane & 7) * 16);
  const int dim = 2 * (lane & 7) + 32 * (lane >> 5) + 16 * ((lane >> 4) & 1) + ((lane >> 3) & 1);
  const u32 ra = opq(L_QS + (c.w * 64 + dim) * 4);
  if (c.w == 5) {
    v4u ml[1];
    gather16<1, 2>(S, c.ws, W_ML + (c.grp * 64 + lane) * 128 + (c.s >> 1) * 16, [](int) { return 0u; }, ml, c.tag);
    const float e = ex2((asf(ml[0].x) - max64(asf(ml[0].x))) * LOG2E);
    stl<float>(L_HS + 256 + lane * 4, e * (1.f / sum64(e * asf(ml[0].z))));
    S.set<C_W>(1);
    STAMP(26);
  }
  S.wait<C_W>(1);
  const float wt = ldl<float>(L_HS + 256 + sl * 4);
  nap<NAP1>();
  v4u v[4];
  gather16<4, 1>(S, c.ws, base, [](int i) { return (u32)i * 128; }, v, c.tag);
  STAMP(8);
  float z0 = asf(v[0].x) * wt, z1 = asf(v[0].z) * wt, z2 = asf(v[1].x) * wt, z3 = asf(v[1].z) * wt;
  z0 += sw32(z0, asf(v[2].x) * wt); z1 += sw32(z1, asf(v[2].z) * wt);
  z2 += sw32(z2, asf(v[3].x) * wt); z3 += sw32(z3, asf(v[3].z) * wt);
  z0 += sw16(z0, z2); z1 += sw16(z1, z3);
  const bool up = lanes(M_HI8);
  stl<float>(ra, sel(up, z1, z0) + dpp<ROR8>(sel(up, z0, z1)));
  LGKM0();
  __builtin_amdgcn_s_barrier();
  CBAR();
  return 0.f;
}
DI float sum8(u32 a) {
  float r[8];
#pragma unroll
  for (int i = 0; i < 8; ++i) r[i] = ldl<float>(a + i * 256);
  return ((r[0] + r[1]) + (r[2] + r[3])) + ((r[4] + r[5]) + (r[6] + r[7]));
}

DI void stage_B(const Params& p, const Ctx& c, Sync& S, int m, int kb, int cb, u32 n0) {
  const int lane = c.lane, w = c.w;
  S.wait<C_FULL>(n0 + 16);
  v4u wf[16];
  read_rows<0>(n0, w, lane, wf);
  S.wait<C_CS>(1);
  const v2u cs = ldl<v2u>(L_CS + lane * 8);
  const Masks M = mk_masks();
  const int P = 64 * kb + lane, tile = w + 8 * (lane & 3);
  const u32 pa = opq(L_PRIV + w * 512 + lane * 4), xa = opq(L_PRIV + w * 512 + (lane >> 4) * 8);
  const u32 o0 = opq(W_X + (P >> 3) * 128 + (P & 7) * 8), o1 = opq(W_X + lane * 128 + 64);
  const u32 oo = opq((lane & 4) ? OOB : W_GU + ((8 * cb + (tile >> 2)) * 16 + 2 * kb + m) * 512 + (tile & 3) * 128 + (lane >> 4) * 32 + (lane & 8) * 2);
  LGKM0();
  STAMP(12);
  v2u xg; v4u st;
  for (u32 n = 0;;) {
    CBAR();
    xg = gl8(c.ws, o0);
    st = gl16(c.ws, o1);
    if (all_ok((xg.y ^ c.tag) | (st.y ^ c.tag) | (st.w ^ c.tag)) || ++n > 8192u) break;
    nap<3>();
  }
  CBAR();
  STAMP(13);
  const float mu0 = asf(rfl(st.x));
  float d = asf(st.x) - mu0;
  d += sw32(d, asf(st.z) + 16.f * d * d);
  d = sum_lo3(d); d += dpp<ROR8>(d); d = sum16(d);
  const float e = sw32(d, d) * (1.f / 1024.f);
  d *= 1.f / 64.f;
  const float mb = rbf(mu0 + d), inv = rcp(rbf(__builtin_amdgcn_sqrtf(rbf(rbf(fmx(e - d * d, 0.f)) + 1e-5f))));
  const u32 xm = pk(lo16(xg.x) - mb, hi16(xg.x) - mb), ln = pk(lo16(xm) * inv, hi16(xm) * inv);
  const u32 sc = pk(lo16(ln) * lo16(cs.x), hi16(ln) * lo16(cs.y));
  stl<u32>(pa, pk(lo16(sc) + hi16(cs.x), hi16(sc) + hi16(cs.y)));
  CBAR();
  v4f acc[4];
#pragma unroll
  for (int u = 0; u < 4; ++u) acc[u] = v4f{0.f, 0.f, 0.f, 0.f};
#pragma unroll
  for (int k = 0; k < 4; ++k) {
    const v4u xf = xfrag(xa, k);
#pragma unroll
    for (int u = 0; u < 4; ++u) acc[u] = mfma(wf[k * 4 + u], xf, acc[u]);
  }
  const bool hi = lanes(M_HI8);
  float r0 = sel(hi, acc[0].z, acc[0].x), r1 = sel(hi, acc[0].w, acc[0].y);
#pragma unroll
  for (int u = 1; u < 4; ++u) {
    r0 = sel(lanes(M.m[u]), sel(hi, acc[u].z, acc[u].x), r0);
    r1 = sel(lanes(M.m[u]), sel(hi, acc[u].w, acc[u].y), r1);
  }
  gs16(c.ws, oo, v4u{asu(r0), c.tag, asu(r1), c.tag});
  STAMP(14);
}

DI void stage_D(const Params& p, const Ctx& c, Sync& S, int t, u32 n0) {
  const int lane = c.lane, w = c.w;
  S.wait<C_FULL>(n0 + 16);
  v4u wf[16];
  read_rows<1>(n0, w, lane, wf);
  const Masks M = mk_masks();
  const u32 pa = opq(L_PRIV + w * 512 + lane * 2), xa = opq(L_PRIV + w * 512 + (lane >> 4) * 8);
  const u32 oi = opq(W_GU + (t * 16 + 2 * w) * 512 + lane * 16);
  const u32 wa = opq(L_CS + w * 512 + (lane & 31) * 16 + (lane >> 5) * 4), ra = opq(L_CS + lane * 8);
  const u32 oo = opq(W_D + (tile_of(lane, w) * 56 + t) * 128 + (lane >> 4) * 32 + (lane & 8) * 2);
  LGKM0();
  STAMP(27);
  v4u q[1];
  gather16<1, 3>(S, c.ws, oi, [](int) { return 0u; }, q, c.tag);
  STAMP(28);
  stl<u32>(wa, q[0].x);
  stl<u32>(wa + 8, q[0].z);
  LGKM0();
  __builtin_amdgcn_s_barrier();
  CBAR();
  v2f r[8];
#pragma unroll
  for (int i = 0; i < 8; ++i) r[i] = ldl<v2f>(ra + i * 512);
  const v2f y = ((r[0] + r[1]) + (r[2] + r[3])) + ((r[4] + r[5]) + (r[6] + r[7]));
  const u32 gu = pk(y.x, y.y);
  const float g = lo16(gu);
  const float z = (-2.f * LOG2E * 0.7978845608028654f) * (g + 0.044715f * g * g * g);
  const float h = rbf(g * rcp(1.f + ex2(z))) * hi16(gu);
  stl<u16>(pa, (u16)pk(h, h));
  CBAR();
  gs16(c.ws, oo, tiles8(wf, xfrag(xa, 0), xfrag(xa, 1), M, c.tag));
  STAMP(29);
}

template <int N> DI float col_total(const v4u (&q)[N]) {
  float s0 = asf(q[0].x), s1 = asf(q[0].z);
#pragma unroll
  for (int i = 1; i < N; ++i) { s0 += asf(q[i].x); s1 += asf(q[i].z); }
  s0 += sw32(s0, s1);
  s0 = sum16(s0);
  return s0 + dpp<ROR8>(s0);
}

__global__ void __launch_bounds__(512) fused_block(Params p) {
  const int tid = threadIdx.x, lane = tid & 63, w = rfl(tid >> 6), wg = blockIdx.x;
  const int grp = (wg >> 3) & 3, s = (wg & 7) + ((wg >> 5) << 3);
  const bool isA = s < 16;
  const int lam = (wg >> 3) - 8;
  const int role = isA ? 0 : lam < 14 ? 1 : lam < 21 ? 2 : 3;
  const int tt = (wg & 7) + 8 * (lam % 7), r = 16 * grp + s, v = w & 3;
  const u32 nkv = isA ? 26 : 10, NT = isA ? 26 : 34, ntail = nkv + 2 * NT;
  Sync S{opq(L_CTRL)};
  if (w == 0) { STAMP(0); S.set_at<0>(S.base + 4 * lane, 0u); }
  Ctx c;
  c.ws = mk(p.ws, W_END);
  c.lane = lane; c.w = w; c.wg = wg; c.grp = grp; c.s = s;
  v4u wa[16], wb[16];
  auto take_wo = [&]() {
    read_rows<1>(10, v, lane, wa);
    read_rows<1>(10, v + 4, lane, wb);
    LGKM0();
    S.add1<C_FREE + 10, 8>();
    S.add1<C_FREE, 8>();
  };

  if (w == 7) {
    __builtin_amdgcn_s_setprio(3);
    Ring R{S, 0, 0, 0, 0, 0, ntail, 0, 0};
    const rsrc_t rm = mk(p.wmod, 1024 * 6144 * 2);
    ld_slab<DEPTH>(R, rm, (wg >> 5) * 128 * 12288 + (wg & 31) * 128, 12288, 2, lane);
    __builtin_amdgcn_s_barrier();
    ld_slab<DEPTH>(R, mk(p.wq, 1024 * 4096 * 2), (wg >> 6) * 256 * 8192 + (wg & 63) * 128, 8192, 4, lane);
    ld_slab<DEPTH>(R, rm, (wg >> 6) * 256 * 12288 + 4096 + (wg & 63) * 128, 12288, 4, lane);
    if (isA) ld_rows<DEPTH, 2048, 1>(R, mk(p.wo, 4096 * 1024 * 2), r * 64 * 2048, lane);
    const rsrc_t rk = mk(p.K, 1u << 26), rv = mk(p.V, 1u << 26);
    const u32 rw = lane >> 4, ps = lane & 15;
    u32 vk[4], vv[2];
#pragma unroll
    for (int i = 0; i < 4; ++i) vk[i] = rw * 256 + ((ps ^ (4 * i + rw)) << 4);
#pragma unroll
    for (int i = 0; i < 2; ++i) vv[i] = rw * 256 + ((ps ^ ((4 * i + rw) << 1)) << 4);
    auto tile_off = [&](int tau) { return ((u32)grp << 24) + (((tau < 8 ? tau * 48 + s - 16 : 384 + (tau - 8) * 64 + s) + ROT * grp) & 2047) * 8192; };
    auto ldk = [&](int tau) {
      const u32 so = tile_off(tau), ld = R.open();
      dma4<DEPTH>(ld, vk[0], vk[1], vk[2], vk[3], rk, so);
      dma4<DEPTH>(ld + 4096, vk[0], vk[1], vk[2], vk[3], rk, so + 4096);
      R.close<DEPTH>();
    };
    auto ldv = [&](int tau) {
      const u32 so = tile_off(tau), ld = R.open();
      dma4<DEPTH>(ld, vv[0], vv[1], vv[0], vv[1], rv, so);
      dma4<DEPTH>(ld + 4096, vv[0], vv[1], vv[0], vv[1], rv, so + 4096);
      R.close<DEPTH>();
    };
    int tau = isA ? 8 : 0;
    if (isA) {
      ldk(8); ldv(8);
      vmwait<16>();
      R.publish(26);
      tau = 9;
      take_wo();
    }
#pragma unroll 1
    for (; tau < 31; ++tau) { ldk(tau); ldv(tau); }
#pragma unroll 1
    for (; tau < 34; ++tau) ldk(tau);
#pragma unroll 1
    for (tau = 31; tau < 34; ++tau) ldv(tau);
    STAMP(3);
    if (role == 1) ld_rows<DEPTH2, 7168, 0>(R, mk(lam < 7 ? p.wg : p.wu, 1024 * 3584 * 2), (wg & 7) * 128 * 7168 + (lam % 7) * 1024, lane);
    else if (role == 2) ld_rows<DEPTH2, 2048, 1>(R, mk(p.wd, 3584 * 1024 * 2), tt * 64 * 2048, lane);
    vmwait<32>(); R.publish(R.n - 4);
    vmwait<24>(); R.publish(R.n - 3);
    vmwait<16>(); R.publish(R.n - 2);
    vmwait<8>(); R.publish(R.n - 1);
    vmwait<0>(); R.publish(R.n);
    STAMP(4);
    STAMPV(22, R.first);
    STAMPV(31, R.stall + 1);
    __builtin_amdgcn_s_setprio(0);
    c.tag = rfl(*(volatile LAS u32*)(S.base + 4 * C_TAG));
  } else if (w < 4) {
    const u32 ep = __hip_atomic_load((GAS u32*)(p.ws + W_EPOCH), __ATOMIC_RELAXED, __HIP_MEMORY_SCOPE_AGENT);
    const u32 c1 = *(const GAS u32*)(p.c + (wg >> 5) * 128 + 2 * lane);
    const v2u c2 = *(const GAS v2u*)(p.c + (wg >> 6) * 256 + 4 * lane);
    const XIn X = load_x(p, c);
    const u32 pa = L_PRIV + w * 1024;
    LGKM0();
    __builtin_amdgcn_s_barrier();
    CBAR();
    auto silu2 = [](u32 a) { const float x = lo16(a), y = hi16(a); return pk(x * sigm(x), y * sigm(y)); };
    stl<u32>(pa + lane * 4, silu2(c1));
    stl<v2u>(pa + 256 + lane * 8, v2u{silu2(c2.x), silu2(c2.y)});
    c.tag = rfl(ep) + 1u;
    S.set<C_TAG>(c.tag);
    S.wait<C_FULL>(2);
    put_tile(c.ws, W_MOD1 + wg * 512 + w * 128, gemv_cols<4>(0, pa, w, lane), c.tag, lane);
    S.add1<C_FREE, 2>();
    if (w == 0) { STAMP(1); }
    make_h(p, c, S, make_ln(X));
    S.wait<C_FULL>(6);
    S.wait<C_H>(4);
    if (w == 0) { STAMP(18); }
    put_tile(c.ws, W_Q + wg * 512 + w * 128, gemv_cols<8>(2, L_HS, w, lane), c.tag, lane);
    S.add1<C_FREE + 2, 4>();
    if (w == 0) { STAMP(19); }
    S.wait<C_FULL>(10);
    put_tile(c.ws, W_MOD2 + wg * 512 + w * 128, gemv_cols<8>(6, pa + 256, w, lane), c.tag, lane);
    S.add1<C_FREE + 6, 4>();
    if (w == 0) { STAMP(20); }
    nap<NAP0B>();
    make_q(p, c, S);
    S.wait<C_Q>(4);
    if (w == 0) { STAMP(2); }
    const u32 g = lane >> 4, j = lane & 15;
    v4u qf[4];
#pragma unroll
    for (int k = 0; k < 4; ++k) qf[k] = ldl<v4u>(L_QS + (j & 7) * 256 + k * 64 + g * 16);
    const u32 kb = j * 256 + ((g ^ j) << 4);
    const u32 r16 = 4 * g + (j >> 2), vb = r16 * 256 + ((r16 & 7) << 5) + (lane & 3) * 8;
    float m = -INFINITY, l = 0.f;
    v4f acc[8];
#pragma unroll
    for (int b = 0; b < 8; ++b) acc[b] = v4f{0.f, 0.f, 0.f, 0.f};
    const u32 ma = opq(L_MRG + lane * 16), mla = opq(L_MRG + 4096 + lane * 8);
    auto raw = [&](u32 n, float (&sv)[8]) {
      const u32 pos = n % NH, ka = pos * 8192;
      S.wait<C_FULL>(n + 1);
      v4u kf[8];
#pragma unroll
      for (int k = 0; k < 4; ++k) { const u32 a = ka + (kb ^ (k << 6)); kf[k] = ldl<v4u>(a); kf[4 + k] = ldl<v4u>(a + 4096); }
      v4f s0 = {0.f, 0.f, 0.f, 0.f}, s1 = s0;
#pragma unroll
      for (int k = 0; k < 4; ++k) { s0 = mfma(kf[k], qf[k], s0); s1 = mfma(kf[4 + k], qf[k], s1); }
      S.set_at<4 * C_FREE>(S.base + 4 * pos, n / NH * 4 + 4);
#pragma unroll
      for (int i = 0; i < 4; ++i) {
        const u32 a = i < 2 ? pk(s0[2 * i], s0[2 * i + 1]) : pk(s1[2 * i - 4], s1[2 * i - 3]);
        const u32 b = pk(lo16(a) * 0.08838834764831845f, hi16(a) * 0.08838834764831845f);
        sv[2 * i] = lo16(b); sv[2 * i + 1] = hi16(b);
      }
      return fmx(fmx(fmx(sv[0], sv[1]), fmx(sv[2], sv[3])), fmx(fmx(sv[4], sv[5]), fmx(sv[6], sv[7])));
    };
    auto probs = [&](const float (&sv)[8]) {
      float e[8];
#pragma unroll
      for (int i = 0; i < 8; ++i) e[i] = ex2((sv[i] - m) * LOG2E);
      l += ((e[0] + e[1]) + (e[2] + e[3])) + ((e[4] + e[5]) + (e[6] + e[7]));
      return v4u{pk(e[0], e[1]), pk(e[2], e[3]), pk(e[4], e[5]), pk(e[6], e[7])};
    };
    auto values = [&](u32 n, v4u pf) {
      const u32 pos = n % NH;
      u32 a[8];
#pragma unroll
      for (int b = 0; b < 8; ++b) a[b] = opq(pos * 8192 + (vb ^ (b << 5)));
      S.wait<C_FULL>(n + 1);
      v4u vf[8];
#pragma unroll
      for (int b = 0; b < 8; ++b) vf[b] = cat(trd(a[b]), trd(a[b] + 4096));
#pragma unroll
      for (int b = 0; b < 8; ++b) acc[b] = mfma(vf[b], pf, acc[b]);
      S.set_at<4 * C_FREE>(S.base + 4 * pos, n / NH * 4 + 4);
    };
    auto fold = [&]() {
      const v2f ml = ldl<v2f>(mla);
      const float mx = fmx(m, ml.x), al = ex2((m - mx) * LOG2E), be = ex2((ml.x - mx) * LOG2E);
      l = l * al + be * ml.y;
      m = mx;
#pragma unroll
      for (int b2 = 0; b2 < 4; ++b2) {
        const v4f o = ldl<v4f>(ma + b2 * 1024) * be;
        acc[2 * b2] = acc[2 * b2] * al + v4f{o.x, o.y, o.x, o.y};
        acc[2 * b2 + 1] = acc[2 * b2 + 1] * al + v4f{o.z, o.w, o.z, o.w};
      }
    };
    auto rescale = [&](float mx) {
      mx = fmx(m, max32(max16(mx)));
      if (__builtin_amdgcn_ballot_w64(mx > m) != 0ull) {
        const float al = ex2((m - mx) * LOG2E);
        l *= al;
#pragma unroll
        for (int b = 0; b < 8; ++b) acc[b] *= al;
        m = mx;
      }
    };
    const int order = (w - (int)NT + 1) & 3;
#pragma unroll 1
    for (u32 it = w; it < NT - 2; it += 4) {
      float sv[8];
      rescale(raw(nkv + 2 * it, sv));
      values(nkv + 2 * it + (it == NT - 3 ? 3 : 1), probs(sv));
    }
    const bool hi = lanes(M_HI8);
    if (order < 3) {
      if (order > 0) { S.wait<C_M>(order); fold(); }
#pragma unroll
      for (int b2 = 0; b2 < 4; ++b2) {
        const v4f a = acc[2 * b2], b = acc[2 * b2 + 1];
        stl<v4f>(ma + b2 * 1024, v4f{sel(hi, a.z, a.x), sel(hi, a.w, a.y), sel(hi, b.z, b.x), sel(hi, b.w, b.y)});
      }
      stl<v2f>(mla, v2f{m, l});
      S.set<C_M>(order + 1);
    } else {
      const u32 n = ntail - 5;
      float sa[8], sb[8];
      const float mxa = raw(n, sa), mxb = raw(n + 1, sb);
      rescale(fmx(mxa, mxb));
      const v4u p0 = probs(sa), p1 = probs(sb);
      const u32 o = opq(W_P + ((16 * grp + 2 * (j & 7)) * 64 + s) * 512 + g * 32 + (j & 8) * 2), o2 = opq(o + 64 * 512);
      const u32 om = opq(lane < 8 ? W_ML + (64 * grp + s) * 128 + lane * 16 : OOB);
      STAMP(23);
      values(n + 3, p0);
      S.wait<C_M>(3);
      STAMP(24);
      const v2f ml = ldl<v2f>(mla);
      const float mx = fmx(m, ml.x), al = ex2((m - mx) * LOG2E), be = ex2((ml.x - mx) * LOG2E);
      gs16(c.ws, om, v4u{asu(mx), c.tag, asu(sum32(sum16(l * al + be * ml.y))), c.tag});
      v4f y[4];
#pragma unroll
      for (int b2 = 0; b2 < 4; ++b2) { y[b2] = ldl<v4f>(ma + b2 * 1024) * be; asm volatile("" : "+v"(y[b2])); }
      STAMP(25);
      values(n + 4, p1);
      STAMP(5);
#pragma unroll
      for (int b = 0; b < 8; ++b)
        gs16(c.ws, (b < 4 ? o : o2) + (b & 3) * 128,
             v4u{asu(sel(hi, acc[b].z, acc[b].x) * al + y[b >> 1][2 * (b & 1)]), c.tag, asu(sel(hi, acc[b].w, acc[b].y) * al + y[b >> 1][2 * (b & 1) + 1]), c.tag});
      STAMP(6);
    }
  } else {
    __builtin_amdgcn_s_barrier();
    if (role == 0) {
      S.wait<C_FULL>(26);
      take_wo();
    }
    S.wait<C_Q>(4);
    c.tag = rfl(*(volatile LAS u32*)(S.base + 4 * C_TAG));
    if (role == 0) {
      if (w == 4) {
      v4u q[2];
      const u32 xp = *(const GAS u32*)(p.x + 16 * r + 2 * (lane & 7));
      gather16<2, 8>(S, c.ws, W_MOD2 + (64 * ((lane >> 3) & 3) + 32 * (lane >> 5) + (r >> 2)) * 512 + ((r & 3) * 16 + 2 * (lane & 7)) * 8,
                     [](int i) { return (u32)i * 16 * 512; }, q, c.tag);
      float y[4] = {asf(q[0].x), asf(q[0].z), asf(q[1].x), asf(q[1].z)}, o[4];
#pragma unroll
      for (int i = 0; i < 4; ++i) { y[i] += dpp<ROR8>(y[i]); y[i] = rbf(sum16(y[i])); o[i] = sw32(y[i], y[i]); }
      stl<v4f>(L_HS + (lane & 7) * 16, v4f{y[0], o[2], lo16(xp), 0.f});
      stl<v4f>(L_HS + 128 + (lane & 7) * 16, v4f{y[1], o[3], hi16(xp), 0.f});
      gs16(c.ws, lane < 8 ? W_CS + r * 128 + lane * 16 : OOB, v4u{pk(1.f + o[0], y[2]), c.tag, pk(1.f + o[1], y[3]), c.tag});
      }
    } else if (role == 1 && w == 4) {
      v4u q[1];
      gather16<1, 32>(S, c.ws, W_CS + (128 * (wg & 7) + 2 * lane) * 8, [](int) { return 0u; }, q, c.tag);
      stl<v2u>(L_CS + lane * 8, v2u{q[0].x, q[0].z});
      S.set<C_CS>(1);
    }
  }

  if (role == 0) {
    const Masks M = mk_masks();
    const u32 pa = L_PRIV + w * 512;
    const u32 xa = opq(pa + (lane >> 4) * 8), oo = opq(W_A + (tile_of(lane, v) * 64 + r) * 128 + (lane >> 4) * 32 + (lane & 8) * 2);
    const u32 ra = opq(L_QS + lane * 4), wr = opq(pa + lane * 2);
    S.wait<C_FULL>(ntail);
    if (w == 4) { STAMP(7); }
    combine_part(p, c, S);
    if (w < 4) return;
    const float o = sum8(ra);
    stl<u16>(wr, (u16)pk(o, o));
    CBAR();
    const v4u x0 = xfrag(xa, 0), x1 = xfrag(xa, 1);
    gs16(c.ws, oo, tiles8(wa, x0, x1, M, c.tag));
    gs16(c.ws, oo + 4 * 64 * 128, tiles8(wb, x0, x1, M, c.tag));
    if (v == 0) {
      STAMP(9);
      const v4f kp = ldl<v4f>(L_HS + (lane >> 5) * 128 + (lane & 7) * 16);
      const u32 ox = opq(lane < 10 ? W_X + r * 128 + lane * 8 : OOB);
      const u32 ia = opq(W_A + (r * 64 + (lane >> 3)) * 128 + (lane & 7) * 16);
      nap<NAP2>();
      v4u q[8];
      gather16<8, 1>(S, c.ws, ia, [](int i) { return (u32)i * 1024; }, q, c.tag);
      STAMP(10);
      const float a = rbf(kp.z + rbf(kp.x * rbf(col_total(q))));
      float a0 = a;
      const float a1 = sw32(a0, a0);
      const float mu = sum_lo3(a0 + a1) * (1.f / 16.f);
      const float m2 = sum_lo3((a0 - mu) * (a0 - mu) + (a1 - mu) * (a1 - mu));
      gs8(c.ws, ox, v2u{asu(sel(lanes(0xff), asf(pk(a0, a1)), sel(lanes(0x100), mu, m2))), c.tag});
      STAMP(11);
      const u32 id = opq(W_D + (r * 56 + (lane >> 3)) * 128 + (lane & 7) * 16);
      const u32 oy = opq(lane < 8 ? 32 * r + 4 * lane : OOB);
      nap<NAP5>();
      v4u q7[7];
      gather16<7, 2>(S, c.ws, id, [](int i) { return (u32)i * 1024; }, q7, c.tag);
      STAMP(15);
      float y0 = a + rbf(kp.y * rbf(col_total(q7)));
      const float y1 = sw32(y0, y0);
      __builtin_amdgcn_raw_buffer_store_b32(pk(y0, y1), mk(p.out, 2048), oy, 0, 0);
      __builtin_amdgcn_raw_buffer_store_b32(c.tag, c.ws, (r | lane) ? OOB : W_EPOCH, 0, 16);
      STAMP(16);
    }
  } else if (role == 1) {
    stage_B(p, c, S, lam >= 7, wg & 7, lam % 7, ntail);
  } else if (role == 2) {
    stage_D(p, c, S, tt, ntail);
  }
}

extern "C" void kernel_launch(void* const* d_in, const int* in_sizes, int n_in, void* d_out, int out_size, void* d_ws,
                              size_t ws_size, hipStream_t stream) {
  Params p;
  p.x = (const u16*)d_in[0]; p.K = (const u16*)d_in[1]; p.V = (const u16*)d_in[2];
  p.wq = (const u16*)d_in[3]; p.wo = (const u16*)d_in[4];
  p.wg = (const u16*)d_in[6]; p.wu = (const u16*)d_in[7]; p.wd = (const u16*)d_in[8];
  p.c = (const u16*)d_in[9]; p.wmod = (const u16*)d_in[10];
  p.out = (u16*)d_out; p.ws = (char*)d_ws;
  (void)hipFuncSetAttribute((const void*)fused_block, hipFuncAttributeMaxDynamicSharedMemorySize, 163840);
  hipLaunchKernelGGL(fused_block, dim3(256), dim3(512), 163840, stream, p);
}
```

```cpp
#include <hip/hip_runtime.h>
#include <stdint.h>
#include <stdio.h>
#define STAMPS 1

typedef unsigned short u16;
typedef unsigned u32;
typedef unsigned long long u64;
typedef u32 v4u __attribute__((ext_vector_type(4)));
typedef u32 v2u __attribute__((ext_vector_type(2)));
typedef float v4f __attribute__((ext_vector_type(4)));
typedef float v2f __attribute__((ext_vector_type(2)));
typedef __bf16 v8bf __attribute__((ext_vector_type(8)));
typedef __bf16 v2bf __attribute__((ext_vector_type(2)));
typedef short v4s __attribute__((ext_vector_type(4)));
#define LAS __attribute__((address_space(3)))
#define GAS __attribute__((address_space(1)))
#define DI __device__ __forceinline__
typedef __amdgpu_buffer_rsrc_t rsrc_t;

#define ROT 37
#define NAP0A 10
#define NAP0B 10
#define NAP1 8
#define NAP2 10
#define NAP5 64
#define PACE0 4
#define PUBNAP 0
#define PACE 0
#define DEPTH 24
#define DEPTH2 16

constexpr u32 W_EPOCH = 0;
constexpr u32 W_MOD1 = 4096;
constexpr u32 W_Q = W_MOD1 + 256 * 512;
constexpr u32 W_MOD2 = W_Q + 256 * 512;
constexpr u32 W_CS = W_MOD2 + 256 * 512;
constexpr u32 W_X = W_CS + 64 * 128;
constexpr u32 W_ML = W_X + 64 * 128;
constexpr u32 W_GU = W_ML + 32 * 64 * 16;
constexpr u32 W_A = W_GU + 56 * 16 * 512;
constexpr u32 W_D = W_A + 64 * 64 * 128;
constexpr u32 W_P = W_D + 64 * 64 * 128;
constexpr u32 W_END = W_P + 64 * 64 * 512;
constexpr u32 W_STAMP = W_END;

constexpr u32 NH = 18;
constexpr u32 L_CTRL = NH * 8192;
constexpr u32 L_MRG = L_CTRL + 256;
constexpr u32 L_QS = L_MRG + 4608;
constexpr u32 L_HS = L_QS + 2048;
constexpr u32 L_CS = L_HS + 512;
constexpr u32 L_PRIV = L_CS + 4096;
constexpr u32 L_END = L_PRIV + 4096;
static_assert(L_END <= 163840, "LDS");
constexpr u32 C_FULL = 0, C_H = 2, C_Q = 3, C_M = 4, C_CS = 5, C_TAG = 6, C_W = 7, C_FREE = 16;
constexpr float LOG2E = 1.4426950408889634f;

struct Params {
  const u16 *x, *K, *V, *wq, *wo, *wg, *wu, *wd, *c, *wmod;
  u16* out;
  char* ws;
};

template <class T> DI T ldl(u32 a) { return *(LAS T*)a; }
template <class T> DI void stl(u32 a, T v) { *(LAS T*)a = v; }
DI u32 rfl(u32 v) { return __builtin_amdgcn_readfirstlane(v); }
DI float asf(u32 v) { return __builtin_bit_cast(float, v); }
DI u32 asu(float v) { return __builtin_bit_cast(u32, v); }
DI float lo16(u32 v) { return asf(v << 16); }
DI float hi16(u32 v) { return asf(v & 0xffff0000u); }
DI u32 pk(float a, float b) { v2f t = {a, b}; return __builtin_bit_cast(u32, __builtin_convertvector(t, v2bf)); }
DI float rbf(float a) { return hi16(pk(a, a)); }
#define CBAR() asm volatile("" ::: "memory")
#define LGKM0() asm volatile("s_waitcnt lgkmcnt(0)" ::: "memory")
DI v4f mfma(v4u a, v4u b, v4f c) {
  return __builtin_amdgcn_mfma_f32_16x16x32_bf16(__builtin_bit_cast(v8bf, a), __builtin_bit_cast(v8bf, b), c, 0, 0, 0);
}
DI v2u trd(u32 addr) { return __builtin_bit_cast(v2u, __builtin_amdgcn_ds_read_tr16_b64_v4i16((LAS v4s*)addr)); }
DI v4u cat(v2u a, v2u b) { return v4u{a.x, a.y, b.x, b.y}; }
template <int CTRL> DI float dpp(float x) { return asf(__builtin_amdgcn_update_dpp(0, asu(x), CTRL, 0xf, 0xf, true)); }
constexpr int XOR1 = 0xB1, XOR2 = 0x4E, HMIR = 0x141, ROR8 = 0x128;
DI float fmx(float a, float b) { return __builtin_amdgcn_fmed3f(a, b, INFINITY); }
DI float sw16(float& a, float b) { auto s = __builtin_amdgcn_permlane16_swap(asu(a), asu(b), false, false); a = asf(s[0]); return asf(s[1]); }
DI float sw32(float& a, float b) { auto s = __builtin_amdgcn_permlane32_swap(asu(a), asu(b), false, false); a = asf(s[0]); return asf(s[1]); }
DI float sum16(float x) { const float o = sw16(x, x); return x + o; }
DI float sum32(float x) { const float o = sw32(x, x); return x + o; }
DI float max16(float x) { const float o = sw16(x, x); return fmx(x, o); }
DI float max32(float x) { const float o = sw32(x, x); return fmx(x, o); }
DI float sum_lo3(float x) { x += dpp<XOR1>(x); x += dpp<XOR2>(x); return x + dpp<HMIR>(x); }
DI float sum64(float x) { x = sum_lo3(x); x += dpp<ROR8>(x); return sum32(sum16(x)); }
DI float max64(float x) {
  x = fmx(x, dpp<XOR1>(x)); x = fmx(x, dpp<XOR2>(x)); x = fmx(x, dpp<HMIR>(x)); x = fmx(x, dpp<ROR8>(x));
  return max32(max16(x));
}
DI float sel(bool c, float a, float b) { asm("" : "+v"(a)); asm("" : "+v"(b)); return c ? a : b; }
DI float ex2(float x) { return __builtin_amdgcn_exp2f(x); }
DI float rcp(float x) { return __builtin_amdgcn_rcpf(x); }
DI float sigm(float z) { return rcp(1.f + ex2(-LOG2E * z)); }
DI u32 opq(u32 v) { asm volatile("" : "+v"(v)); return v; }
DI u64 opqs(u64 v) { asm volatile("" : "+s"(v)); return v; }
DI bool lanes(u64 m) { return __builtin_amdgcn_inverse_ballot_w64(m); }
constexpr u64 M_HI8 = 0xff00ff00ff00ff00ull, M_HI32 = 0xffffffff00000000ull;
constexpr u32 OOB = 0x80000000u;

DI u64 now() { return __builtin_amdgcn_s_memrealtime(); }
#define STAMPV(i, val) do { if ((threadIdx.x & 63) == 0) *(GAS u64*)(p.ws + W_STAMP + ((size_t)blockIdx.x * 32 + (i)) * 8) = (val); } while (0)
#define STAMP(i) STAMPV(i, now())

struct Sync {
  u32 base;
  template <u32 OFF> DI void wait_at(u32 addr, u32 v) {
    u32 t, s, n;
    asm volatile("s_movk_i32 %2, 0x7fff\n1:\n\tds_read_b32 %0, %3 offset:%c5\n\ts_waitcnt lgkmcnt(0)\n\tv_readfirstlane_b32 %1, %0\n\t"
                 "s_cmp_ge_u32 %1, %4\n\ts_cbranch_scc1 2f\n\ts_sleep 1\n\ts_sub_u32 %2, %2, 1\n\ts_cbranch_scc0 1b\n2:"
                 : "=&v"(t), "=&s"(s), "=&s"(n) : "v"(addr), "s"(v), "i"(OFF) : "memory", "scc");
  }
  template <u32 W> DI void wait(u32 v) { wait_at<4 * W>(base, v); }
  template <u32 W, int N> DI void add1() {
    u64 e;
    asm volatile("s_mov_b64 %0, exec\n\ts_mov_b64 exec, %4\n\tds_add_u32 %1, %2 offset:%c3\n\ts_mov_b64 exec, %0"
                 : "=&s"(e) : "v"(base + (N > 1 ? 4 * (threadIdx.x & 63) : 0)), "v"(1u), "i"(4 * W), "s"((u64)((1ull << N) - 1)) : "memory");
  }
  template <u32 OFF> DI void set_at(u32 addr, u32 v) {
    asm volatile("ds_write_b32 %0, %1 offset:%c2" :: "v"(addr), "v"(v), "i"(OFF) : "memory");
  }
  template <u32 W> DI void set(u32 v) { set_at<4 * W>(base, v); }
};
DI v4u gl16(rsrc_t r, u32 off, u32 so = 0) { return __builtin_amdgcn_raw_buffer_load_b128(r, off, so, 16); }
DI v2u gl8(rsrc_t r, u32 off) { return __builtin_amdgcn_raw_buffer_load_b64(r, off, 0, 16); }
DI void gs16(rsrc_t r, u32 off, v4u v) { __builtin_amdgcn_raw_buffer_store_b128(v, r, off, 0, 16); }
DI void gs8(rsrc_t r, u32 off, v2u v) { __builtin_amdgcn_raw_buffer_store_b64(v, r, off, 0, 16); }
template <int K> DI void nap() { if constexpr (K > 0) __builtin_amdgcn_s_sleep(K); }
DI bool all_ok(u32 bad) { return __builtin_amdgcn_ballot_w64(bad != 0u) == 0ull; }
template <int N, int NAP, class F> DI void gather16(Sync& S, rsrc_t r, u32 base, F f, v4u (&v)[N], u32 tag) {
  for (u32 n = 0;;) {
    CBAR();
#pragma unroll
    for (int i = 0; i < N; ++i) v[i] = gl16(r, base + (f(i) & 4095u), f(i) & ~4095u);
    u32 bad = 0;
#pragma unroll
    for (int i = 0; i < N; ++i) bad |= (v[i].y ^ tag) | (v[i].w ^ tag);
    if (all_ok(bad) || ++n > 8192u) break;
    nap<NAP>();
  }
  CBAR();
}

template <int D> DI void dma4(u32 ld, u32 v0, u32 v1, u32 v2, u32 v3, rsrc_t r, u32 so) {
  asm volatile("s_mov_b32 m0, %0\n\ts_nop 0\n\t"
               "buffer_load_dwordx4 %1, %5, %6 offen nt lds\n\ts_waitcnt vmcnt(%c7)\n\t"
               "buffer_load_dwordx4 %2, %5, %6 offen offset:1024 nt lds\n\ts_waitcnt vmcnt(%c7)\n\t"
               "buffer_load_dwordx4 %3, %5, %6 offen offset:2048 nt lds\n\ts_waitcnt vmcnt(%c7)\n\t"
               "buffer_load_dwordx4 %4, %5, %6 offen offset:3072 nt lds\n\ts_waitcnt vmcnt(%c7)"
               :: "s"(ld), "v"(v0), "v"(v1), "v"(v2), "v"(v3), "s"(r), "s"(so), "i"(D) : "memory");
}
template <int D> DI void dma1(u32 ld, u32 v, rsrc_t r, u32 so) {
  asm volatile("s_mov_b32 m0, %0\n\ts_nop 0\n\tbuffer_load_dwordx4 %1, %2, %3 offen nt lds\n\ts_waitcnt vmcnt(%c4)"
               :: "s"(ld), "v"(v), "s"(r), "s"(so), "i"(D) : "memory");
}
template <int D> DI void vmwait() { asm volatile("s_waitcnt vmcnt(%c0)" :: "i"(D) : "memory"); }
struct Ring {
  Sync S;
  u32 n, pos, need, full, next, lim;
  u64 stall, first;
  DI void publish(u32 f) { if ((int)(f - full) > 0) { full = f; nap<PUBNAP>(); S.set<C_FULL>(f); } }
  DI u32 open() {
    if (n >= NH) {
      const u32 fa = S.base + 4 * pos;
      if (rfl(*(volatile LAS u32*)(fa + 4 * C_FREE)) < need) {
        const u64 t0 = now(); if (!first) first = t0;
        vmwait<0>();
        publish(n);
        S.wait_at<4 * C_FREE>(fa, need);
        stall += now() - t0;
      }
    }
    return pos * 8192u;
  }
  template <int D> DI void close() {
    ++n; ++pos;
    if (pos == NH) { pos = 0; need += 4; }
    publish(n - D / 8);
  }
};
DI rsrc_t mk(const void* p, u32 bytes) { return __builtin_amdgcn_make_buffer_rsrc((void*)p, 0, bytes, 0x00020000); }
template <int D> DI void ld_slab(Ring& R, rsrc_t r, u32 base, u32 pitch, int nh, int lane) {
  const u32 row = lane >> 3, v = row * pitch + (((lane & 7) ^ (((row >> 1) & 3) << 1)) << 4);
#pragma unroll 1
  for (int h = 0; h < nh; ++h) {
    const u32 ld = R.open();
#pragma unroll
    for (int i = 0; i < 8; ++i) { dma1<D>(ld + i * 1024, v, r, base); base += 8 * pitch; }
    R.close<D>();
  }
}
template <int D, u32 PITCH, int SH> DI void ld_rows(Ring& R, rsrc_t r, u32 base, int lane) {
  u32 v[8];
#pragma unroll
  for (int i = 0; i < 8; ++i) v[i] = ((lane ^ (i << 1)) << 4) + i * PITCH - (i & 3) * 1024;
#pragma unroll 1
  for (int h = 0; h < 16; ++h) {
    const u32 ld = R.open(), so = base + (h >> SH) * 8 * PITCH + (h & ((1 << SH) - 1)) * 1024;
    dma4<D>(ld, v[0], v[1], v[2], v[3], r, so);
    dma4<D>(ld + 4096, v[4], v[5], v[6], v[7], r, so);
    R.close<D>();
  }
}

DI v4u xfrag(u32 addr, int k) { return cat(ldl<v2u>(addr + k * 64), ldl<v2u>(addr + k * 64 + 32)); }
template <int NK> DI v4f gemv_cols(u32 n0, u32 xaddr, int c, int lane) {
  const u32 g = lane >> 4, r16 = 4 * g + ((lane & 15) >> 2);
  const u32 a = opq(n0 * 8192 + r16 * 128 + ((c ^ ((r16 >> 1) & 3)) << 5) + (lane & 3) * 8), xa = opq(xaddr + g * 8);
  v4f acc = {0.f, 0.f, 0.f, 0.f};
#pragma unroll
  for (int k = 0; k < NK; ++k) acc = mfma(cat(trd(a + k * 4096), trd(a + k * 4096 + 2048)), xfrag(xa, k), acc);
  return acc;
}
DI void put_tile(rsrc_t ws, u32 off, v4f acc, u32 tag, int lane) {
  const bool hi = lanes(M_HI8);
  gs16(ws, (lane & 7) ? OOB : off + (lane >> 4) * 32 + (lane & 8) * 2, v4u{asu(sel(hi, acc.z, acc.x)), tag, asu(sel(hi, acc.w, acc.y)), tag});
}
DI u32 hs_addr(u32 n) { return (n % NH) * 8192u; }
template <int SH> DI void read_rows(u32 n0, int v, int lane, v4u (&wf)[16]) {
  const u32 g = lane >> 4, r8 = (4 * g + ((lane & 15) >> 2)) & 7;
  const u32 lo = r8 * 1024 + ((v ^ r8) << 5) + (lane & 3) * 8;
#pragma unroll
  for (int q = 0; q < 4; ++q) {
    const int half = SH ? q >> 1 : 0, k = SH ? q & 1 : q;
    u32 a[2];
#pragma unroll
    for (int t = 0; t < 2; ++t) {
      const u32 h0 = hs_addr(n0 + ((4 * k + 2 * t) << SH) + half), h1 = hs_addr(n0 + ((4 * k + 2 * t + 1) << SH) + half);
      a[t] = lo + (lanes(M_HI32) ? h1 : h0);
    }
#pragma unroll
    for (int u = 0; u < 4; ++u) wf[SH ? (half * 4 + u) * 2 + k : 4 * k + u] = cat(trd(a[0] + u * 256), trd(a[1] + u * 256));
  }
}
struct Masks { u64 m[8]; };
DI Masks mk_masks() {
  Masks M;
#pragma unroll
  for (int i = 0; i < 8; ++i) M.m[i] = opqs(0x0101010101010101ull << i);
  return M;
}
DI v4u tiles8(const v4u (&wf)[16], v4u x0, v4u x1, const Masks& M, u32 tag) {
  v4f acc[8];
#pragma unroll
  for (int i = 0; i < 8; ++i) acc[i] = mfma(wf[2 * i], x0, v4f{0.f, 0.f, 0.f, 0.f});
#pragma unroll
  for (int i = 0; i < 8; ++i) acc[i] = mfma(wf[2 * i + 1], x1, acc[i]);
  const bool hi = lanes(M_HI8);
  float o0 = sel(hi, acc[0].z, acc[0].x), o1 = sel(hi, acc[0].w, acc[0].y);
#pragma unroll
  for (int i = 1; i < 8; ++i) {
    o0 = sel(lanes(M.m[i]), sel(hi, acc[i].z, acc[i].x), o0);
    o1 = sel(lanes(M.m[i]), sel(hi, acc[i].w, acc[i].y), o1);
  }
  return v4u{asu(o0), tag, asu(o1), tag};
}
DI u32 tile_of(int lane, int v) { return 32 * ((lane >> 2) & 1) + 8 * (lane & 3) + v; }

struct Ctx { rsrc_t ws; u32 tag; int lane, w, wg, grp, s; };

struct XIn { v4u xa, xb; u32 xp; };
DI XIn load_x(const Params& p, const Ctx& c) {
  return XIn{*(const GAS v4u*)(p.x + c.lane * 16), *(const GAS v4u*)(p.x + c.lane * 16 + 8),
             *(const GAS u32*)(p.x + 256 * (c.wg >> 6) + 64 * c.w + 2 * (c.lane & 31))};
}
DI float make_ln(const XIn& X) {
  const v4u xa = X.xa, xb = X.xb;
  float sm = 0.f;
#pragma unroll
  for (int i = 0; i < 4; ++i) sm += (lo16(xa[i]) + hi16(xa[i])) + (lo16(xb[i]) + hi16(xb[i]));
  const float mean = sum64(sm) * (1.f / 1024.f);
  float q = 0.f;
#pragma unroll
  for (int i = 0; i < 4; ++i) {
    const float a = lo16(xa[i]) - mean, b = hi16(xa[i]) - mean, d = lo16(xb[i]) - mean, e = hi16(xb[i]) - mean;
    q += (a * a + b * b) + (d * d + e * e);
  }
  const float var = sum64(q) * (1.f / 1024.f);
  const float mb = rbf(mean), den = rbf(sqrtf(rbf(rbf(var) + 1e-5f)));
  return asf(opq(asu(rbf(rbf((lanes(M_HI32) ? hi16(X.xp) : lo16(X.xp)) - mb) / den))));
}
DI void make_h(const Params& p, const Ctx& c, Sync& S, float ln) {
  const int lane = c.lane, kq = c.wg >> 6;
  const u32 base = opq(W_MOD1 + (32 * (lane >> 5) + 4 * kq + c.w) * 512 + (lane & 31) * 16);
  const u32 ha = opq(L_HS + (64 * c.w + 2 * (lane & 31) + (lane >> 5)) * 2);
  v4u v[8];
  nap<NAP0A>();
  gather16<8, 4>(S, c.ws, base, [](int i) { return (u32)(64 * (i & 3) + 16 * (i >> 2)) * 512; }, v, c.tag);
  if (c.w == 0) { STAMP(17); }
  float s0 = (asf(v[0].x) + asf(v[1].x)) + (asf(v[2].x) + asf(v[3].x)), c0 = (asf(v[4].x) + asf(v[5].x)) + (asf(v[6].x) + asf(v[7].x));
  s0 += sw32(s0, (asf(v[0].z) + asf(v[1].z)) + (asf(v[2].z) + asf(v[3].z)));
  c0 += sw32(c0, (asf(v[4].z) + asf(v[5].z)) + (asf(v[6].z) + asf(v[7].z)));
  const float h = rbf(ln * rbf(1.f + rbf(c0))) + rbf(s0);
  stl<u16>(ha, (u16)pk(h, h));
  S.add1<C_H, 1>();
}
DI void make_q(const Params& p, const Ctx& c, Sync& S) {
  const int lane = c.lane;
  v4u v[8];
  const u32 base = W_Q + (16 * c.grp + 4 * c.w + (lane >> 5)) * 512 + (lane & 31) * 16;
  gather16<8, 4>(S, c.ws, base, [](int i) { return (u32)(64 * (i & 3) + 2 * (i >> 2)) * 512; }, v, c.tag);
#pragma unroll
  for (int i = 0; i < 8; i += 4)
    stl<u32>(L_QS + (128 * c.w + 16 * i + lane) * 4,
             pk((asf(v[i].x) + asf(v[i + 1].x)) + (asf(v[i + 2].x) + asf(v[i + 3].x)), (asf(v[i].z) + asf(v[i + 1].z)) + (asf(v[i + 2].z) + asf(v[i + 3].z))));
  S.add1<C_Q, 1>();
}

DI float combine_part(const Params& p, const Ctx& c, Sync& S) {
  const int lane = c.lane, a = 16 * c.grp + c.s, sl = 8 * c.w + (lane >> 3);
  const u32 base = opq(W_P + (a * 64 + sl) * 512 + (lane & 7) * 16);
  const int dim = 2 * (lane & 7) + 32 * (lane >> 5) + 16 * ((lane >> 4) & 1) + ((lane >> 3) & 1);
  const u32 ra = opq(L_QS + (c.w * 64 + dim) * 4);
  if (c.w == 5) {
    v4u ml[1];
    gather16<1, 2>(S, c.ws, W_ML + (c.grp * 64 + lane) * 128 + (c.s >> 1) * 16, [](int) { return 0u; }, ml, c.tag);
    const float e = ex2((asf(ml[0].x) - max64(asf(ml[0].x))) * LOG2E);
    stl<float>(L_HS + 256 + lane * 4, e * (1.f / sum64(e * asf(ml[0].z))));
    S.set<C_W>(1);
    STAMP(26);
  }
  S.wait<C_W>(1);
  const float wt = ldl<float>(L_HS + 256 + sl * 4);
  nap<NAP1>();
  v4u v[4];
  gather16<4, 1>(S, c.ws, base, [](int i) { return (u32)i * 128; }, v, c.tag);
  STAMP(8);
  float z0 = asf(v[0].x) * wt, z1 = asf(v[0].z) * wt, z2 = asf(v[1].x) * wt, z3 = asf(v[1].z) * wt;
  z0 += sw32(z0, asf(v[2].x) * wt); z1 += sw32(z1, asf(v[2].z) * wt);
  z2 += sw32(z2, asf(v[3].x) * wt); z3 += sw32(z3, asf(v[3].z) * wt);
  z0 += sw16(z0, z2); z1 += sw16(z1, z3);
  const bool up = lanes(M_HI8);
  stl<float>(ra, sel(up, z1, z0) + dpp<ROR8>(sel(up, z0, z1)));
  LGKM0();
  __builtin_amdgcn_s_barrier();
  CBAR();
  return 0.f;
}
DI float sum8(u32 a) {
  float r[8];
#pragma unroll
  for (int i = 0; i < 8; ++i) r[i] = ldl<float>(a + i * 256);
  return ((r[0] + r[1]) + (r[2] + r[3])) + ((r[4] + r[5]) + (r[6] + r[7]));
}

DI void stage_B(const Params& p, const Ctx& c, Sync& S, int m, int kb, int cb, u32 n0) {
  const int lane = c.lane, w = c.w;
  S.wait<C_FULL>(n0 + 16);
  v4u wf[16];
  read_rows<0>(n0, w, lane, wf);
  S.wait<C_CS>(1);
  const v2u cs = ldl<v2u>(L_CS + lane * 8);
  const Masks M = mk_masks();
  const int P = 64 * kb + lane, tile = w + 8 * (lane & 3);
  const u32 pa = opq(L_PRIV + w * 512 + lane * 4), xa = opq(L_PRIV + w * 512 + (lane >> 4) * 8);
  const u32 o0 = opq(W_X + (P >> 3) * 128 + (P & 7) * 8), o1 = opq(W_X + lane * 128 + 64);
  const u32 oo = opq((lane & 4) ? OOB : W_GU + ((8 * cb + (tile >> 2)) * 16 + 2 * kb + m) * 512 + (tile & 3) * 128 + (lane >> 4) * 32 + (lane & 8) * 2);
  LGKM0();
  STAMP(12);
  v2u xg; v4u st;
  for (u32 n = 0;;) {
    CBAR();
    xg = gl8(c.ws, o0);
    st = gl16(c.ws, o1);
    if (all_ok((xg.y ^ c.tag) | (st.y ^ c.tag) | (st.w ^ c.tag)) || ++n > 8192u) break;
    nap<3>();
  }
  CBAR();
  STAMP(13);
  const float mu0 = asf(rfl(st.x));
  float d = asf(st.x) - mu0;
  d += sw32(d, asf(st.z) + 16.f * d * d);
  d = sum_lo3(d); d += dpp<ROR8>(d); d = sum16(d);
  const float e = sw32(d, d) * (1.f / 1024.f);
  d *= 1.f / 64.f;
  const float mb = rbf(mu0 + d), inv = rcp(rbf(__builtin_amdgcn_sqrtf(rbf(rbf(fmx(e - d * d, 0.f)) + 1e-5f))));
  const u32 xm = pk(lo16(xg.x) - mb, hi16(xg.x) - mb), ln = pk(lo16(xm) * inv, hi16(xm) * inv);
  const u32 sc = pk(lo16(ln) * lo16(cs.x), hi16(ln) * lo16(cs.y));
  stl<u32>(pa, pk(lo16(sc) + hi16(cs.x), hi16(sc) + hi16(cs.y)));
  CBAR();
  v4f acc[4];
#pragma unroll
  for (int u = 0; u < 4; ++u) acc[u] = v4f{0.f, 0.f, 0.f, 0.f};
#pragma unroll
  for (int k = 0; k < 4; ++k) {
    const v4u xf = xfrag(xa, k);
#pragma unroll
    for (int u = 0; u < 4; ++u) acc[u] = mfma(wf[k * 4 + u], xf, acc[u]);
  }
  const bool hi = lanes(M_HI8);
  float r0 = sel(hi, acc[0].z, acc[0].x), r1 = sel(hi, acc[0].w, acc[0].y);
#pragma unroll
  for (int u = 1; u < 4; ++u) {
    r0 = sel(lanes(M.m[u]), sel(hi, acc[u].z, acc[u].x), r0);
    r1 = sel(lanes(M.m[u]), sel(hi, acc[u].w, acc[u].y), r1);
  }
  gs16(c.ws, oo, v4u{asu(r0), c.tag, asu(r1), c.tag});
  STAMP(14);
}

DI void stage_D(const Params& p, const Ctx& c, Sync& S, int t, u32 n0) {
  const int lane = c.lane, w = c.w;
  S.wait<C_FULL>(n0 + 16);
  v4u wf[16];
  read_rows<1>(n0, w, lane, wf);
  const Masks M = mk_masks();
  const u32 pa = opq(L_PRIV + w * 512 + lane * 2), xa = opq(L_PRIV + w * 512 + (lane >> 4) * 8);
  const u32 oi = opq(W_GU + (t * 16 + 2 * w) * 512 + lane * 16);
  const u32 wa = opq(L_CS + w * 512 + (lane & 31) * 16 + (lane >> 5) * 4), ra = opq(L_CS + lane * 8);
  const u32 oo = opq(W_D + (tile_of(lane, w) * 56 + t) * 128 + (lane >> 4) * 32 + (lane & 8) * 2);
  LGKM0();
  STAMP(27);
  v4u q[1];
  gather16<1, 3>(S, c.ws, oi, [](int) { return 0u; }, q, c.tag);
  STAMP(28);
  stl<u32>(wa, q[0].x);
  stl<u32>(wa + 8, q[0].z);
  LGKM0();
  __builtin_amdgcn_s_barrier();
  CBAR();
  v2f r[8];
#pragma unroll
  for (int i = 0; i < 8; ++i) r[i] = ldl<v2f>(ra + i * 512);
  const v2f y = ((r[0] + r[1]) + (r[2] + r[3])) + ((r[4] + r[5]) + (r[6] + r[7]));
  const u32 gu = pk(y.x, y.y);
  const float g = lo16(gu);
  const float z = (-2.f * LOG2E * 0.7978845608028654f) * (g + 0.044715f * g * g * g);
  const float h = rbf(g * rcp(1.f + ex2(z))) * hi16(gu);
  stl<u16>(pa, (u16)pk(h, h));
  CBAR();
  gs16(c.ws, oo, tiles8(wf, xfrag(xa, 0), xfrag(xa, 1), M, c.tag));
  STAMP(29);
}

template <int N> DI float col_total(const v4u (&q)[N]) {
  float s0 = asf(q[0].x), s1 = asf(q[0].z);
#pragma unroll
  for (int i = 1; i < N; ++i) { s0 += asf(q[i].x); s1 += asf(q[i].z); }
  s0 += sw32(s0, s1);
  s0 = sum16(s0);
  return s0 + dpp<ROR8>(s0);
}

__global__ void __launch_bounds__(512) fused_block(Params p) {
  const int tid = threadIdx.x, lane = tid & 63, w = rfl(tid >> 6), wg = blockIdx.x;
  const int grp = (wg >> 3) & 3, s = (wg & 7) + ((wg >> 5) << 3);
  const bool isA = s < 16;
  const int lam = (wg >> 3) - 8;
  const int role = isA ? 0 : lam < 14 ? 1 : lam < 21 ? 2 : 3;
  const int tt = (wg & 7) + 8 * (lam % 7), r = 16 * grp + s, v = w & 3;
  const u32 nkv = isA ? 26 : 10, NT = isA ? 26 : 34, ntail = nkv + 2 * NT;
  Sync S{opq(L_CTRL)};
  if (w == 0) { STAMPV(30, (u64)(((__builtin_amdgcn_s_getreg(20 | (3 << 11)) & 15) - wg) & 7)); STAMP(0); S.set_at<0>(S.base + 4 * lane, 0u); }
  Ctx c;
  c.ws = mk(p.ws, W_END);
  c.lane = lane; c.w = w; c.wg = wg; c.grp = grp; c.s = s;
  v4u wa[16], wb[16];
  auto take_wo = [&]() {
    read_rows<1>(10, v, lane, wa);
    read_rows<1>(10, v + 4, lane, wb);
    LGKM0();
    S.add1<C_FREE + 10, 8>();
    S.add1<C_FREE, 8>();
  };

  if (w == 7) {
    __builtin_amdgcn_s_setprio(3);
    Ring R{S, 0, 0, 0, 0, 0, ntail, 0, 0};
    const rsrc_t rm = mk(p.wmod, 1024 * 6144 * 2);
    ld_slab<DEPTH>(R, rm, (wg >> 5) * 128 * 12288 + (wg & 31) * 128, 12288, 2, lane);
    __builtin_amdgcn_s_barrier();
    ld_slab<DEPTH>(R, mk(p.wq, 1024 * 4096 * 2), (wg >> 6) * 256 * 8192 + (wg & 63) * 128, 8192, 4, lane);
    ld_slab<DEPTH>(R, rm, (wg >> 6) * 256 * 12288 + 4096 + (wg & 63) * 128, 12288, 4, lane);
    if (isA) ld_rows<DEPTH, 2048, 1>(R, mk(p.wo, 4096 * 1024 * 2), r * 64 * 2048, lane);
    const rsrc_t rk = mk(p.K, 1u << 26), rv = mk(p.V, 1u << 26);
    const u32 rw = lane >> 4, ps = lane & 15;
    u32 vk[4], vv[2];
#pragma unroll
    for (int i = 0; i < 4; ++i) vk[i] = rw * 256 + ((ps ^ (4 * i + rw)) << 4);
#pragma unroll
    for (int i = 0; i < 2; ++i) vv[i] = rw * 256 + ((ps ^ ((4 * i + rw) << 1)) << 4);
    auto tile_off = [&](int tau) { return ((u32)grp << 24) + (((tau < 8 ? tau * 48 + s - 16 : 384 + (tau - 8) * 64 + s) + ROT * grp) & 2047) * 8192; };
    auto ldk = [&](int tau) {
      const u32 so = tile_off(tau), ld = R.open();
      dma4<DEPTH>(ld, vk[0], vk[1], vk[2], vk[3], rk, so);
      dma4<DEPTH>(ld + 4096, vk[0], vk[1], vk[2], vk[3], rk, so + 4096);
      R.close<DEPTH>();
    };
    auto ldv = [&](int tau) {
      const u32 so = tile_off(tau), ld = R.open();
      dma4<DEPTH>(ld, vv[0], vv[1], vv[0], vv[1], rv, so);
      dma4<DEPTH>(ld + 4096, vv[0], vv[1], vv[0], vv[1], rv, so + 4096);
      R.close<DEPTH>();
    };
    int tau = isA ? 8 : 0;
    if (isA) {
      ldk(8); ldv(8);
      vmwait<16>();
      R.publish(26);
      tau = 9;
      take_wo();
    }
#pragma unroll 1
    for (; tau < 31; ++tau) { ldk(tau); ldv(tau); }
#pragma unroll 1
    for (; tau < 34; ++tau) ldk(tau);
#pragma unroll 1
    for (tau = 31; tau < 34; ++tau) ldv(tau);
    STAMP(3);
    if (role == 1) ld_rows<DEPTH2, 7168, 0>(R, mk(lam < 7 ? p.wg : p.wu, 1024 * 3584 * 2), (wg & 7) * 128 * 7168 + (lam % 7) * 1024, lane);
    else if (role == 2) ld_rows<DEPTH2, 2048, 1>(R, mk(p.wd, 3584 * 1024 * 2), tt * 64 * 2048, lane);
    vmwait<32>(); R.publish(R.n - 4);
    vmwait<24>(); R.publish(R.n - 3);
    vmwait<16>(); R.publish(R.n - 2);
    vmwait<8>(); R.publish(R.n - 1);
    vmwait<0>(); R.publish(R.n);
    STAMP(4);
    STAMPV(22, R.first);
    STAMPV(31, R.stall + 1);
    __builtin_amdgcn_s_setprio(0);
    c.tag = rfl(*(volatile LAS u32*)(S.base + 4 * C_TAG));
  } else if (w < 4) {
    const u32 ep = __hip_atomic_load((GAS u32*)(p.ws + W_EPOCH), __ATOMIC_RELAXED, __HIP_MEMORY_SCOPE_AGENT);
    const u32 c1 = *(const GAS u32*)(p.c + (wg >> 5) * 128 + 2 * lane);
    const v2u c2 = *(const GAS v2u*)(p.c + (wg >> 6) * 256 + 4 * lane);
    const XIn X = load_x(p, c);
    const u32 pa = L_PRIV + w * 1024;
    LGKM0();
    __builtin_amdgcn_s_barrier();
    CBAR();
    auto silu2 = [](u32 a) { const float x = lo16(a), y = hi16(a); return pk(x * sigm(x), y * sigm(y)); };
    stl<u32>(pa + lane * 4, silu2(c1));
    stl<v2u>(pa + 256 + lane * 8, v2u{silu2(c2.x), silu2(c2.y)});
    c.tag = rfl(ep) + 1u;
    S.set<C_TAG>(c.tag);
    S.wait<C_FULL>(2);
    put_tile(c.ws, W_MOD1 + wg * 512 + w * 128, gemv_cols<4>(0, pa, w, lane), c.tag, lane);
    S.add1<C_FREE, 2>();
    if (w == 0) { STAMP(1); }
    make_h(p, c, S, make_ln(X));
    S.wait<C_FULL>(6);
    S.wait<C_H>(4);
    if (w == 0) { STAMP(18); }
    put_tile(c.ws, W_Q + wg * 512 + w * 128, gemv_cols<8>(2, L_HS, w, lane), c.tag, lane);
    S.add1<C_FREE + 2, 4>();
    if (w == 0) { STAMP(19); }
    S.wait<C_FULL>(10);
    put_tile(c.ws, W_MOD2 + wg * 512 + w * 128, gemv_cols<8>(6, pa + 256, w, lane), c.tag, lane);
    S.add1<C_FREE + 6, 4>();
    if (w == 0) { STAMP(20); }
    nap<NAP0B>();
    make_q(p, c, S);
    S.wait<C_Q>(4);
    if (w == 0) { STAMP(2); }
    const u32 g = lane >> 4, j = lane & 15;
    v4u qf[4];
#pragma unroll
    for (int k = 0; k < 4; ++k) qf[k] = ldl<v4u>(L_QS + (j & 7) * 256 + k * 64 + g * 16);
    const u32 kb = j * 256 + ((g ^ j) << 4);
    const u32 r16 = 4 * g + (j >> 2), vb = r16 * 256 + ((r16 & 7) << 5) + (lane & 3) * 8;
    float m = -INFINITY, l = 0.f;
    v4f acc[8];
#pragma unroll
    for (int b = 0; b < 8; ++b) acc[b] = v4f{0.f, 0.f, 0.f, 0.f};
    const u32 ma = opq(L_MRG + lane * 16), mla = opq(L_MRG + 4096 + lane * 8);
    auto raw = [&](u32 n, float (&sv)[8]) {
      const u32 pos = n % NH, ka = pos * 8192;
      S.wait<C_FULL>(n + 1);
      v4u kf[8];
#pragma unroll
      for (int k = 0; k < 4; ++k) { const u32 a = ka + (kb ^ (k << 6)); kf[k] = ldl<v4u>(a); kf[4 + k] = ldl<v4u>(a + 4096); }
      v4f s0 = {0.f, 0.f, 0.f, 0.f}, s1 = s0;
#pragma unroll
      for (int k = 0; k < 4; ++k) { s0 = mfma(kf[k], qf[k], s0); s1 = mfma(kf[4 + k], qf[k], s1); }
      S.set_at<4 * C_FREE>(S.base + 4 * pos, n / NH * 4 + 4);
#pragma unroll
      for (int i = 0; i < 4; ++i) {
        const u32 a = i < 2 ? pk(s0[2 * i], s0[2 * i + 1]) : pk(s1[2 * i - 4], s1[2 * i - 3]);
        const u32 b = pk(lo16(a) * 0.08838834764831845f, hi16(a) * 0.08838834764831845f);
        sv[2 * i] = lo16(b); sv[2 * i + 1] = hi16(b);
      }
      return fmx(fmx(fmx(sv[0], sv[1]), fmx(sv[2], sv[3])), fmx(fmx(sv[4], sv[5]), fmx(sv[6], sv[7])));
    };
    auto probs = [&](const float (&sv)[8]) {
      float e[8];
#pragma unroll
      for (int i = 0; i < 8; ++i) e[i] = ex2((sv[i] - m) * LOG2E);
      l += ((e[0] + e[1]) + (e[2] + e[3])) + ((e[4] + e[5]) + (e[6] + e[7]));
      return v4u{pk(e[0], e[1]), pk(e[2], e[3]), pk(e[4], e[5]), pk(e[6], e[7])};
    };
    auto values = [&](u32 n, v4u pf) {
      const u32 pos = n % NH;
      u32 a[8];
#pragma unroll
      for (int b = 0; b < 8; ++b) a[b] = opq(pos * 8192 + (vb ^ (b << 5)));
      S.wait<C_FULL>(n + 1);
      v4u vf[8];
#pragma unroll
      for (int b = 0; b < 8; ++b) vf[b] = cat(trd(a[b]), trd(a[b] + 4096));
#pragma unroll
      for (int b = 0; b < 8; ++b) acc[b] = mfma(vf[b], pf, acc[b]);
      S.set_at<4 * C_FREE>(S.base + 4 * pos, n / NH * 4 + 4);
    };
    auto fold = [&]() {
      const v2f ml = ldl<v2f>(mla);
      const float mx = fmx(m, ml.x), al = ex2((m - mx) * LOG2E), be = ex2((ml.x - mx) * LOG2E);
      l = l * al + be * ml.y;
      m = mx;
#pragma unroll
      for (int b2 = 0; b2 < 4; ++b2) {
        const v4f o = ldl<v4f>(ma + b2 * 1024) * be;
        acc[2 * b2] = acc[2 * b2] * al + v4f{o.x, o.y, o.x, o.y};
        acc[2 * b2 + 1] = acc[2 * b2 + 1] * al + v4f{o.z, o.w, o.z, o.w};
      }
    };
    auto rescale = [&](float mx) {
      mx = fmx(m, max32(max16(mx)));
      if (__builtin_amdgcn_ballot_w64(mx > m) != 0ull) {
        const float al = ex2((m - mx) * LOG2E);
        l *= al;
#pragma unroll
        for (int b = 0; b < 8; ++b) acc[b] *= al;
        m = mx;
      }
    };
    const int order = (w - (int)NT + 1) & 3;
#pragma unroll 1
    for (u32 it = w; it < NT - 2; it += 4) {
      float sv[8];
      rescale(raw(nkv + 2 * it, sv));
      values(nkv + 2 * it + (it == NT - 3 ? 3 : 1), probs(sv));
    }
    const bool hi = lanes(M_HI8);
    if (order < 3) {
      if (order > 0) { S.wait<C_M>(order); fold(); }
#pragma unroll
      for (int b2 = 0; b2 < 4; ++b2) {
        const v4f a = acc[2 * b2], b = acc[2 * b2 + 1];
        stl<v4f>(ma + b2 * 1024, v4f{sel(hi, a.z, a.x), sel(hi, a.w, a.y), sel(hi, b.z, b.x), sel(hi, b.w, b.y)});
      }
      stl<v2f>(mla, v2f{m, l});
      S.set<C_M>(order + 1);
    } else {
      const u32 n = ntail - 5;
      float sa[8], sb[8];
      const float mxa = raw(n, sa), mxb = raw(n + 1, sb);
      rescale(fmx(mxa, mxb));
      const v4u p0 = probs(sa), p1 = probs(sb);
      const u32 o = opq(W_P + ((16 * grp + 2 * (j & 7)) * 64 + s) * 512 + g * 32 + (j & 8) * 2), o2 = opq(o + 64 * 512);
      const u32 om = opq(lane < 8 ? W_ML + (64 * grp + s) * 128 + lane * 16 : OOB);
      STAMP(23);
      values(n + 3, p0);
      S.wait<C_M>(3);
      STAMP(24);
      const v2f ml = ldl<v2f>(mla);
      const float mx = fmx(m, ml.x), al = ex2((m - mx) * LOG2E), be = ex2((ml.x - mx) * LOG2E);
      gs16(c.ws, om, v4u{asu(mx), c.tag, asu(sum32(sum16(l * al + be * ml.y))), c.tag});
      v4f y[4];
#pragma unroll
      for (int b2 = 0; b2 < 4; ++b2) { y[b2] = ldl<v4f>(ma + b2 * 1024) * be; asm volatile("" : "+v"(y[b2])); }
      STAMP(25);
      values(n + 4, p1);
      STAMP(5);
#pragma unroll
      for (int b = 0; b < 8; ++b)
        gs16(c.ws, (b < 4 ? o : o2) + (b & 3) * 128,
             v4u{asu(sel(hi, acc[b].z, acc[b].x) * al + y[b >> 1][2 * (b & 1)]), c.tag, asu(sel(hi, acc[b].w, acc[b].y) * al + y[b >> 1][2 * (b & 1) + 1]), c.tag});
      STAMP(6);
    }
  } else {
    __builtin_amdgcn_s_barrier();
    if (role == 0) {
      S.wait<C_FULL>(26);
      take_wo();
    }
    S.wait<C_Q>(4);
    c.tag = rfl(*(volatile LAS u32*)(S.base + 4 * C_TAG));
    if (role == 0) {
      if (w == 4) {
      v4u q[2];
      const u32 xp = *(const GAS u32*)(p.x + 16 * r + 2 * (lane & 7));
      gather16<2, 8>(S, c.ws, W_MOD2 + (64 * ((lane >> 3) & 3) + 32 * (lane >> 5) + (r >> 2)) * 512 + ((r & 3) * 16 + 2 * (lane & 7)) * 8,
                     [](int i) { return (u32)i * 16 * 512; }, q, c.tag);
      float y[4] = {asf(q[0].x), asf(q[0].z), asf(q[1].x), asf(q[1].z)}, o[4];
#pragma unroll
      for (int i = 0; i < 4; ++i) { y[i] += dpp<ROR8>(y[i]); y[i] = rbf(sum16(y[i])); o[i] = sw32(y[i], y[i]); }
      stl<v4f>(L_HS + (lane & 7) * 16, v4f{y[0], o[2], lo16(xp), 0.f});
      stl<v4f>(L_HS + 128 + (lane & 7) * 16, v4f{y[1], o[3], hi16(xp), 0.f});
      gs16(c.ws, lane < 8 ? W_CS + r * 128 + lane * 16 : OOB, v4u{pk(1.f + o[0], y[2]), c.tag, pk(1.f + o[1], y[3]), c.tag});
      }
    } else if (role == 1 && w == 4) {
      v4u q[1];
      gather16<1, 32>(S, c.ws, W_CS + (128 * (wg & 7) + 2 * lane) * 8, [](int) { return 0u; }, q, c.tag);
      stl<v2u>(L_CS + lane * 8, v2u{q[0].x, q[0].z});
      S.set<C_CS>(1);
    }
  }

  if (role == 0) {
    const Masks M = mk_masks();
    const u32 pa = L_PRIV + w * 512;
    const u32 xa = opq(pa + (lane >> 4) * 8), oo = opq(W_A + (tile_of(lane, v) * 64 + r) * 128 + (lane >> 4) * 32 + (lane & 8) * 2);
    const u32 ra = opq(L_QS + lane * 4), wr = opq(pa + lane * 2);
    S.wait<C_FULL>(ntail);
    if (w == 4) { STAMP(7); }
    combine_part(p, c, S);
    if (w < 4) return;
    const float o = sum8(ra);
    stl<u16>(wr, (u16)pk(o, o));
    CBAR();
    const v4u x0 = xfrag(xa, 0), x1 = xfrag(xa, 1);
    gs16(c.ws, oo, tiles8(wa, x0, x1, M, c.tag));
    gs16(c.ws, oo + 4 * 64 * 128, tiles8(wb, x0, x1, M, c.tag));
    if (v == 0) {
      STAMP(9);
      const v4f kp = ldl<v4f>(L_HS + (lane >> 5) * 128 + (lane & 7) * 16);
      const u32 ox = opq(lane < 10 ? W_X + r * 128 + lane * 8 : OOB);
      const u32 ia = opq(W_A + (r * 64 + (lane >> 3)) * 128 + (lane & 7) * 16);
      nap<NAP2>();
      v4u q[8];
      gather16<8, 1>(S, c.ws, ia, [](int i) { return (u32)i * 1024; }, q, c.tag);
      STAMP(10);
      const float a = rbf(kp.z + rbf(kp.x * rbf(col_total(q))));
      float a0 = a;
      const float a1 = sw32(a0, a0);
      const float mu = sum_lo3(a0 + a1) * (1.f / 16.f);
      const float m2 = sum_lo3((a0 - mu) * (a0 - mu) + (a1 - mu) * (a1 - mu));
      gs8(c.ws, ox, v2u{asu(sel(lanes(0xff), asf(pk(a0, a1)), sel(lanes(0x100), mu, m2))), c.tag});
      STAMP(11);
      const u32 id = opq(W_D + (r * 56 + (lane >> 3)) * 128 + (lane & 7) * 16);
      const u32 oy = opq(lane < 8 ? 32 * r + 4 * lane : OOB);
      nap<NAP5>();
      v4u q7[7];
      gather16<7, 2>(S, c.ws, id, [](int i) { return (u32)i * 1024; }, q7, c.tag);
      STAMP(15);
      float y0 = a + rbf(kp.y * rbf(col_total(q7)));
      const float y1 = sw32(y0, y0);
      __builtin_amdgcn_raw_buffer_store_b32(pk(y0, y1), mk(p.out, 2048), oy, 0, 0);
      __builtin_amdgcn_raw_buffer_store_b32(c.tag, c.ws, (r | lane) ? OOB : W_EPOCH, 0, 16);
      STAMP(16);
    }
  } else if (role == 1) {
    stage_B(p, c, S, lam >= 7, wg & 7, lam % 7, ntail);
  } else if (role == 2) {
    stage_D(p, c, S, tt, ntail);
  }
}

__global__ void report(Params p) {
  if (threadIdx.x) return;
  const u64* s = (const u64*)(p.ws + W_STAMP);
  u64 t0 = ~0ull;
  for (int b = 0; b < 256; ++b) t0 = s[b * 32] < t0 ? s[b * 32] : t0;
  const int ks[9] = {0, 1, 2, 3, 4, 5, 17, 19, 30};
  for (int q = 0; q < 9; ++q) { const int k = ks[q]; u64 mn = ~0ull, mx = 0, sm = 0;
    for (int b = 0; b < 256; ++b) { const u64 v = s[b * 32 + k] - (k == 30 ? 0 : t0); mn = v < mn ? v : mn; mx = v > mx ? v : mx; sm += v; }
    printf("stamp %d min %d avg %d max %d\n", k, (int)mn, (int)(sm / 256), (int)mx); }
  const int ka[6] = {8, 9, 10, 11, 15, 16};
  for (int q = 0; q < 6; ++q) { const int k = ka[q]; u64 mn = ~0ull, mx = 0;
    for (int b = 0; b < 64; ++b) { const u64 v = s[b * 32 + k] - t0; mn = v < mn ? v : mn; mx = v > mx ? v : mx; }
    printf("stamp %d min %d max %d\n", k, (int)mn, (int)mx); }
}
extern "C" void kernel_launch(void* const* d_in, const int* in_sizes, int n_in, void* d_out, int out_size, void* d_ws,
                              size_t ws_size, hipStream_t stream) {
  Params p;
  p.x = (const u16*)d_in[0]; p.K = (const u16*)d_in[1]; p.V = (const u16*)d_in[2];
  p.wq = (const u16*)d_in[3]; p.wo = (const u16*)d_in[4];
  p.wg = (const u16*)d_in[6]; p.wu = (const u16*)d_in[7]; p.wd = (const u16*)d_in[8];
  p.c = (const u16*)d_in[9]; p.wmod = (const u16*)d_in[10];
  p.out = (u16*)d_out; p.ws = (char*)d_ws;
  for (int i = 0; i < n_in; ++i) fprintf(stderr, "in[%d]=%p n=%d\n", i, d_in[i], in_sizes[i]);
  fprintf(stderr, "out=%p ws=%p size=%zu\n", d_out, d_ws, ws_size);
  (void)hipFuncSetAttribute((const void*)fused_block, hipFuncAttributeMaxDynamicSharedMemorySize, 163840);
  hipLaunchKernelGGL(fused_block, dim3(256), dim3(512), 163840, stream, p);
  hipLaunchKernelGGL(report, dim3(1), dim3(64), 0, stream, p);
}
```

```cpp
#include <hip/hip_runtime.h>
#include <stdint.h>
#define STAMPS 1
#define XK 0

typedef unsigned short u16;
typedef unsigned u32;
typedef unsigned long long u64;
typedef u32 v4u __attribute__((ext_vector_type(4)));
typedef u32 v2u __attribute__((ext_vector_type(2)));
typedef float v4f __attribute__((ext_vector_type(4)));
typedef float v2f __attribute__((ext_vector_type(2)));
typedef __bf16 v8bf __attribute__((ext_vector_type(8)));
typedef __bf16 v2bf __attribute__((ext_vector_type(2)));
typedef short v4s __attribute__((ext_vector_type(4)));
#define LAS __attribute__((address_space(3)))
#define GAS __attribute__((address_space(1)))
#define DI __device__ __forceinline__
typedef __amdgpu_buffer_rsrc_t rsrc_t;

#define ROT 37
#define NAP0A 10
#define NAP0B 10
#define NAP1 8
#define NAP2 10
#define NAP5 64
#define PACE0 4
#define PUBNAP 0
#define PACE 0
#define DEPTH 24
#define DEPTH2 16

constexpr u32 W_EPOCH = 0;
constexpr u32 W_MOD1 = 4096;
constexpr u32 W_Q = W_MOD1 + 256 * 512;
constexpr u32 W_MOD2 = W_Q + 256 * 512;
constexpr u32 W_CS = W_MOD2 + 256 * 512;
constexpr u32 W_X = W_CS + 64 * 128;
constexpr u32 W_ML = W_X + 64 * 128;
constexpr u32 W_GU = W_ML + 32 * 64 * 16;
constexpr u32 W_A = W_GU + 56 * 16 * 512;
constexpr u32 W_D = W_A + 64 * 64 * 128;
constexpr u32 W_P = W_D + 64 * 64 * 128;
constexpr u32 W_END = W_P + 64 * 64 * 512;
constexpr u32 W_STAMP = W_END;

constexpr u32 NH = 18;
constexpr u32 L_CTRL = NH * 8192;
constexpr u32 L_MRG = L_CTRL + 256;
constexpr u32 L_QS = L_MRG + 4608;
constexpr u32 L_HS = L_QS + 2048;
constexpr u32 L_CS = L_HS + 512;
constexpr u32 L_PRIV = L_CS + 4096;
constexpr u32 L_END = L_PRIV + 4096;
static_assert(L_END <= 163840, "LDS");
constexpr u32 C_FULL = 0, C_H = 2, C_Q = 3, C_M = 4, C_CS = 5, C_TAG = 6, C_W = 7, C_FREE = 16;
constexpr float LOG2E = 1.4426950408889634f;

struct Params {
  const u16 *x, *K, *V, *wq, *wo, *wg, *wu, *wd, *c, *wmod;
  u16* out;
  char* ws;
};

template <class T> DI T ldl(u32 a) { return *(LAS T*)a; }
template <class T> DI void stl(u32 a, T v) { *(LAS T*)a = v; }
DI u32 rfl(u32 v) { return __builtin_amdgcn_readfirstlane(v); }
DI float asf(u32 v) { return __builtin_bit_cast(float, v); }
DI u32 asu(float v) { return __builtin_bit_cast(u32, v); }
DI float lo16(u32 v) { return asf(v << 16); }
DI float hi16(u32 v) { return asf(v & 0xffff0000u); }
DI u32 pk(float a, float b) { v2f t = {a, b}; return __builtin_bit_cast(u32, __builtin_convertvector(t, v2bf)); }
DI float rbf(float a) { return hi16(pk(a, a)); }
#define CBAR() asm volatile("" ::: "memory")
#define LGKM0() asm volatile("s_waitcnt lgkmcnt(0)" ::: "memory")
DI v4f mfma(v4u a, v4u b, v4f c) {
  return __builtin_amdgcn_mfma_f32_16x16x32_bf16(__builtin_bit_cast(v8bf, a), __builtin_bit_cast(v8bf, b), c, 0, 0, 0);
}
DI v2u trd(u32 addr) { return __builtin_bit_cast(v2u, __builtin_amdgcn_ds_read_tr16_b64_v4i16((LAS v4s*)addr)); }
DI v4u cat(v2u a, v2u b) { return v4u{a.x, a.y, b.x, b.y}; }
template <int CTRL> DI float dpp(float x) { return asf(__builtin_amdgcn_update_dpp(0, asu(x), CTRL, 0xf, 0xf, true)); }
constexpr int XOR1 = 0xB1, XOR2 = 0x4E, HMIR = 0x141, ROR8 = 0x128;
DI float fmx(float a, float b) { return __builtin_amdgcn_fmed3f(a, b, INFINITY); }
DI float sw16(float& a, float b) { auto s = __builtin_amdgcn_permlane16_swap(asu(a), asu(b), false, false); a = asf(s[0]); return asf(s[1]); }
DI float sw32(float& a, float b) { auto s = __builtin_amdgcn_permlane32_swap(asu(a), asu(b), false, false); a = asf(s[0]); return asf(s[1]); }
DI float sum16(float x) { const float o = sw16(x, x); return x + o; }
DI float sum32(float x) { const float o = sw32(x, x); return x + o; }
DI float max16(float x) { const float o = sw16(x, x); return fmx(x, o); }
DI float max32(float x) { const float o = sw32(x, x); return fmx(x, o); }
DI float sum_lo3(float x) { x += dpp<XOR1>(x); x += dpp<XOR2>(x); return x + dpp<HMIR>(x); }
DI float sum64(float x) { x = sum_lo3(x); x += dpp<ROR8>(x); return sum32(sum16(x)); }
DI float max64(float x) {
  x = fmx(x, dpp<XOR1>(x)); x = fmx(x, dpp<XOR2>(x)); x = fmx(x, dpp<HMIR>(x)); x = fmx(x, dpp<ROR8>(x));
  return max32(max16(x));
}
DI float sel(bool c, float a, float b) { asm("" : "+v"(a)); asm("" : "+v"(b)); return c ? a : b; }
DI float ex2(float x) { return __builtin_amdgcn_exp2f(x); }
DI float rcp(float x) { return __builtin_amdgcn_rcpf(x); }
DI float sigm(float z) { return rcp(1.f + ex2(-LOG2E * z)); }
DI u32 opq(u32 v) { asm volatile("" : "+v"(v)); return v; }
DI u64 opqs(u64 v) { asm volatile("" : "+s"(v)); return v; }
DI bool lanes(u64 m) { return __builtin_amdgcn_inverse_ballot_w64(m); }
constexpr u64 M_HI8 = 0xff00ff00ff00ff00ull, M_HI32 = 0xffffffff00000000ull;
constexpr u32 OOB = 0x80000000u;

DI u64 now() { return __builtin_amdgcn_s_memrealtime(); }
#define STAMPV(i, val) do { if ((threadIdx.x & 63) == 0) __hip_atomic_store((GAS u64*)(p.ws + W_STAMP + ((size_t)blockIdx.x * 32 + (i)) * 8), (u64)(val), __ATOMIC_RELAXED, __HIP_MEMORY_SCOPE_AGENT); } while (0)
#define STAMP(i) STAMPV(i, now())

struct Sync {
  u32 base;
  template <u32 OFF> DI void wait_at(u32 addr, u32 v) {
    u32 t, s, n;
    asm volatile("s_movk_i32 %2, 0x7fff\n1:\n\tds_read_b32 %0, %3 offset:%c5\n\ts_waitcnt lgkmcnt(0)\n\tv_readfirstlane_b32 %1, %0\n\t"
                 "s_cmp_ge_u32 %1, %4\n\ts_cbranch_scc1 2f\n\ts_sleep 1\n\ts_sub_u32 %2, %2, 1\n\ts_cbranch_scc0 1b\n2:"
                 : "=&v"(t), "=&s"(s), "=&s"(n) : "v"(addr), "s"(v), "i"(OFF) : "memory", "scc");
  }
  template <u32 W> DI void wait(u32 v) { wait_at<4 * W>(base, v); }
  template <u32 W, int N> DI void add1() {
    u64 e;
    asm volatile("s_mov_b64 %0, exec\n\ts_mov_b64 exec, %4\n\tds_add_u32 %1, %2 offset:%c3\n\ts_mov_b64 exec, %0"
                 : "=&s"(e) : "v"(base + (N > 1 ? 4 * (threadIdx.x & 63) : 0)), "v"(1u), "i"(4 * W), "s"((u64)((1ull << N) - 1)) : "memory");
  }
  template <u32 OFF> DI void set_at(u32 addr, u32 v) {
    asm volatile("ds_write_b32 %0, %1 offset:%c2" :: "v"(addr), "v"(v), "i"(OFF) : "memory");
  }
  template <u32 W> DI void set(u32 v) { set_at<4 * W>(base, v); }
};
DI v4u gl16(rsrc_t r, u32 off, u32 so = 0) { return __builtin_amdgcn_raw_buffer_load_b128(r, off, so, 16); }
DI v2u gl8(rsrc_t r, u32 off) { return __builtin_amdgcn_raw_buffer_load_b64(r, off, 0, 16); }
DI void gs16(rsrc_t r, u32 off, v4u v) { __builtin_amdgcn_raw_buffer_store_b128(v, r, off, 0, 16); }
DI void gs8(rsrc_t r, u32 off, v2u v) { __builtin_amdgcn_raw_buffer_store_b64(v, r, off, 0, 16); }
template <int K> DI void nap() { if constexpr (K > 0) __builtin_amdgcn_s_sleep(K); }
DI bool all_ok(u32 bad) { return __builtin_amdgcn_ballot_w64(bad != 0u) == 0ull; }
template <int N, int NAP, class F> DI void gather16(Sync& S, rsrc_t r, u32 base, F f, v4u (&v)[N], u32 tag) {
  for (u32 n = 0;;) {
    CBAR();
#pragma unroll
    for (int i = 0; i < N; ++i) v[i] = gl16(r, base + (f(i) & 4095u), f(i) & ~4095u);
    u32 bad = 0;
#pragma unroll
    for (int i = 0; i < N; ++i) bad |= (v[i].y ^ tag) | (v[i].w ^ tag);
    if (all_ok(bad) || ++n > 8192u) break;
    nap<NAP>();
  }
  CBAR();
}

template <int D> DI void dma4(u32 ld, u32 v0, u32 v1, u32 v2, u32 v3, rsrc_t r, u32 so) {
  asm volatile("s_mov_b32 m0, %0\n\ts_nop 0\n\t"
               "buffer_load_dwordx4 %1, %5, %6 offen nt lds\n\ts_waitcnt vmcnt(%c7)\n\t"
               "buffer_load_dwordx4 %2, %5, %6 offen offset:1024 nt lds\n\ts_waitcnt vmcnt(%c7)\n\t"
               "buffer_load_dwordx4 %3, %5, %6 offen offset:2048 nt lds\n\ts_waitcnt vmcnt(%c7)\n\t"
               "buffer_load_dwordx4 %4, %5, %6 offen offset:3072 nt lds\n\ts_waitcnt vmcnt(%c7)"
               :: "s"(ld), "v"(v0), "v"(v1), "v"(v2), "v"(v3), "s"(r), "s"(so), "i"(D) : "memory");
}
template <int D> DI void dma1(u32 ld, u32 v, rsrc_t r, u32 so) {
  asm volatile("s_mov_b32 m0, %0\n\ts_nop 0\n\tbuffer_load_dwordx4 %1, %2, %3 offen nt lds\n\ts_waitcnt vmcnt(%c4)"
               :: "s"(ld), "v"(v), "s"(r), "s"(so), "i"(D) : "memory");
}
template <int D> DI void vmwait() { asm volatile("s_waitcnt vmcnt(%c0)" :: "i"(D) : "memory"); }
struct Ring {
  Sync S;
  u32 n, pos, need, full, next, lim;
  u64 stall, first;
  DI void publish(u32 f) { if ((int)(f - full) > 0) { full = f; nap<PUBNAP>(); S.set<C_FULL>(f); } }
  DI u32 open() {
    if (n >= NH) {
      const u32 fa = S.base + 4 * pos;
      if (rfl(*(volatile LAS u32*)(fa + 4 * C_FREE)) < need) {
        const u64 t0 = now(); if (!first) first = t0;
        vmwait<0>();
        publish(n);
        S.wait_at<4 * C_FREE>(fa, need);
        stall += now() - t0;
      }
    }
    return pos * 8192u;
  }
  template <int D> DI void close() {
    ++n; ++pos;
    if (pos == NH) { pos = 0; need += 4; }
    publish(n - D / 8);
  }
};
DI rsrc_t mk(const void* p, u32 bytes) { return __builtin_amdgcn_make_buffer_rsrc((void*)p, 0, bytes, 0x00020000); }
template <int D> DI void ld_slab(Ring& R, rsrc_t r, u32 base, u32 pitch, int nh, int lane) {
  const u32 row = lane >> 3, v = row * pitch + (((lane & 7) ^ (((row >> 1) & 3) << 1)) << 4);
#pragma unroll 1
  for (int h = 0; h < nh; ++h) {
    const u32 ld = R.open();
#pragma unroll
    for (int i = 0; i < 8; ++i) { dma1<D>(ld + i * 1024, v, r, base); base += 8 * pitch; }
    R.close<D>();
  }
}
template <int D, u32 PITCH, int SH> DI void ld_rows(Ring& R, rsrc_t r, u32 base, int lane) {
  u32 v[8];
#pragma unroll
  for (int i = 0; i < 8; ++i) v[i] = ((lane ^ (i << 1)) << 4) + i * PITCH - (i & 3) * 1024;
#pragma unroll 1
  for (int h = 0; h < 16; ++h) {
    const u32 ld = R.open(), so = base + (h >> SH) * 8 * PITCH + (h & ((1 << SH) - 1)) * 1024;
    dma4<D>(ld, v[0], v[1], v[2], v[3], r, so);
    dma4<D>(ld + 4096, v[4], v[5], v[6], v[7], r, so);
    R.close<D>();
  }
}

DI v4u xfrag(u32 addr, int k) { return cat(ldl<v2u>(addr + k * 64), ldl<v2u>(addr + k * 64 + 32)); }
template <int NK> DI v4f gemv_cols(u32 n0, u32 xaddr, int c, int lane) {
  const u32 g = lane >> 4, r16 = 4 * g + ((lane & 15) >> 2);
  const u32 a = opq(n0 * 8192 + r16 * 128 + ((c ^ ((r16 >> 1) & 3)) << 5) + (lane & 3) * 8), xa = opq(xaddr + g * 8);
  v4f acc = {0.f, 0.f, 0.f, 0.f};
#pragma unroll
  for (int k = 0; k < NK; ++k) acc = mfma(cat(trd(a + k * 4096), trd(a + k * 4096 + 2048)), xfrag(xa, k), acc);
  return acc;
}
DI void put_tile(rsrc_t ws, u32 off, v4f acc, u32 tag, int lane) {
  const bool hi = lanes(M_HI8);
  gs16(ws, (lane & 7) ? OOB : off + (lane >> 4) * 32 + (lane & 8) * 2, v4u{asu(sel(hi, acc.z, acc.x)), tag, asu(sel(hi, acc.w, acc.y)), tag});
}
DI u32 hs_addr(u32 n) { return (n % NH) * 8192u; }
template <int SH> DI void read_rows(u32 n0, int v, int lane, v4u (&wf)[16]) {
  const u32 g = lane >> 4, r8 = (4 * g + ((lane & 15) >> 2)) & 7;
  const u32 lo = r8 * 1024 + ((v ^ r8) << 5) + (lane & 3) * 8;
#pragma unroll
  for (int q = 0; q < 4; ++q) {
    const int half = SH ? q >> 1 : 0, k = SH ? q & 1 : q;
    u32 a[2];
#pragma unroll
    for (int t = 0; t < 2; ++t) {
      const u32 h0 = hs_addr(n0 + ((4 * k + 2 * t) << SH) + half), h1 = hs_addr(n0 + ((4 * k + 2 * t + 1) << SH) + half);
      a[t] = lo + (lanes(M_HI32) ? h1 : h0);
    }
#pragma unroll
    for (int u = 0; u < 4; ++u) wf[SH ? (half * 4 + u) * 2 + k : 4 * k + u] = cat(trd(a[0] + u * 256), trd(a[1] + u * 256));
  }
}
struct Masks { u64 m[8]; };
DI Masks mk_masks() {
  Masks M;
#pragma unroll
  for (int i = 0; i < 8; ++i) M.m[i] = opqs(0x0101010101010101ull << i);
  return M;
}
DI v4u tiles8(const v4u (&wf)[16], v4u x0, v4u x1, const Masks& M, u32 tag) {
  v4f acc[8];
#pragma unroll
  for (int i = 0; i < 8; ++i) acc[i] = mfma(wf[2 * i], x0, v4f{0.f, 0.f, 0.f, 0.f});
#pragma unroll
  for (int i = 0; i < 8; ++i) acc[i] = mfma(wf[2 * i + 1], x1, acc[i]);
  const bool hi = lanes(M_HI8);
  float o0 = sel(hi, acc[0].z, acc[0].x), o1 = sel(hi, acc[0].w, acc[0].y);
#pragma unroll
  for (int i = 1; i < 8; ++i) {
    o0 = sel(lanes(M.m[i]), sel(hi, acc[i].z, acc[i].x), o0);
    o1 = sel(lanes(M.m[i]), sel(hi, acc[i].w, acc[i].y), o1);
  }
  return v4u{asu(o0), tag, asu(o1), tag};
}
DI u32 tile_of(int lane, int v) { return 32 * ((lane >> 2) & 1) + 8 * (lane & 3) + v; }

struct Ctx { rsrc_t ws; u32 tag; int lane, w, wg, grp, s; };

struct XIn { v4u xa, xb; u32 xp; };
DI XIn load_x(const Params& p, const Ctx& c) {
  return XIn{*(const GAS v4u*)(p.x + c.lane * 16), *(const GAS v4u*)(p.x + c.lane * 16 + 8),
             *(const GAS u32*)(p.x + 256 * (c.wg >> 6) + 64 * c.w + 2 * (c.lane & 31))};
}
DI float make_ln(const XIn& X) {
  const v4u xa = X.xa, xb = X.xb;
  float sm = 0.f;
#pragma unroll
  for (int i = 0; i < 4; ++i) sm += (lo16(xa[i]) + hi16(xa[i])) + (lo16(xb[i]) + hi16(xb[i]));
  const float mean = sum64(sm) * (1.f / 1024.f);
  float q = 0.f;
#pragma unroll
  for (int i = 0; i < 4; ++i) {
    const float a = lo16(xa[i]) - mean, b = hi16(xa[i]) - mean, d = lo16(xb[i]) - mean, e = hi16(xb[i]) - mean;
    q += (a * a + b * b) + (d * d + e * e);
  }
  const float var = sum64(q) * (1.f / 1024.f);
  const float mb = rbf(mean), den = rbf(sqrtf(rbf(rbf(var) + 1e-5f)));
  return asf(opq(asu(rbf(rbf((lanes(M_HI32) ? hi16(X.xp) : lo16(X.xp)) - mb) / den))));
}
DI void make_h(const Params& p, const Ctx& c, Sync& S, float ln) {
  const int lane = c.lane, kq = c.wg >> 6;
  const u32 base = opq(W_MOD1 + (32 * (lane >> 5) + 4 * kq + c.w) * 512 + (lane & 31) * 16);
  const u32 ha = opq(L_HS + (64 * c.w + 2 * (lane & 31) + (lane >> 5)) * 2);
  v4u v[8];
  nap<NAP0A>();
  gather16<8, 4>(S, c.ws, base, [](int i) { return (u32)(64 * (i & 3) + 16 * (i >> 2)) * 512; }, v, c.tag);
  if (c.w == 0) { STAMP(17); }
  float s0 = (asf(v[0].x) + asf(v[1].x)) + (asf(v[2].x) + asf(v[3].x)), c0 = (asf(v[4].x) + asf(v[5].x)) + (asf(v[6].x) + asf(v[7].x));
  s0 += sw32(s0, (asf(v[0].z) + asf(v[1].z)) + (asf(v[2].z) + asf(v[3].z)));
  c0 += sw32(c0, (asf(v[4].z) + asf(v[5].z)) + (asf(v[6].z) + asf(v[7].z)));
  const float h = rbf(ln * rbf(1.f + rbf(c0))) + rbf(s0);
  stl<u16>(ha, (u16)pk(h, h));
  S.add1<C_H, 1>();
}
DI void make_q(const Params& p, const Ctx& c, Sync& S) {
  const int lane = c.lane;
  v4u v[8];
  const u32 base = W_Q + (16 * c.grp + 4 * c.w + (lane >> 5)) * 512 + (lane & 31) * 16;
  gather16<8, 4>(S, c.ws, base, [](int i) { return (u32)(64 * (i & 3) + 2 * (i >> 2)) * 512; }, v, c.tag);
#pragma unroll
  for (int i = 0; i < 8; i += 4)
    stl<u32>(L_QS + (128 * c.w + 16 * i + lane) * 4,
             pk((asf(v[i].x) + asf(v[i + 1].x)) + (asf(v[i + 2].x) + asf(v[i + 3].x)), (asf(v[i].z) + asf(v[i + 1].z)) + (asf(v[i + 2].z) + asf(v[i + 3].z))));
  S.add1<C_Q, 1>();
}

DI float combine_part(const Params& p, const Ctx& c, Sync& S) {
  const int lane = c.lane, a = 16 * c.grp + c.s, sl = 8 * c.w + (lane >> 3);
  const u32 base = opq(W_P + (a * 64 + sl) * 512 + (lane & 7) * 16);
  const int dim = 2 * (lane & 7) + 32 * (lane >> 5) + 16 * ((lane >> 4) & 1) + ((lane >> 3) & 1);
  const u32 ra = opq(L_QS + (c.w * 64 + dim) * 4);
  if (c.w == 5) {
    v4u ml[1];
    gather16<1, 2>(S, c.ws, W_ML + (c.grp * 64 + lane) * 128 + (c.s >> 1) * 16, [](int) { return 0u; }, ml, c.tag);
    const float e = ex2((asf(ml[0].x) - max64(asf(ml[0].x))) * LOG2E);
    stl<float>(L_HS + 256 + lane * 4, e * (1.f / sum64(e * asf(ml[0].z))));
    S.set<C_W>(1);
    STAMP(26);
  }
  S.wait<C_W>(1);
  const float wt = ldl<float>(L_HS + 256 + sl * 4);
  nap<NAP1>();
  v4u v[4];
  gather16<4, 1>(S, c.ws, base, [](int i) { return (u32)i * 128; }, v, c.tag);
  STAMP(8);
  float z0 = asf(v[0].x) * wt, z1 = asf(v[0].z) * wt, z2 = asf(v[1].x) * wt, z3 = asf(v[1].z) * wt;
  z0 += sw32(z0, asf(v[2].x) * wt); z1 += sw32(z1, asf(v[2].z) * wt);
  z2 += sw32(z2, asf(v[3].x) * wt); z3 += sw32(z3, asf(v[3].z) * wt);
  z0 += sw16(z0, z2); z1 += sw16(z1, z3);
  const bool up = lanes(M_HI8);
  stl<float>(ra, sel(up, z1, z0) + dpp<ROR8>(sel(up, z0, z1)));
  LGKM0();
  __builtin_amdgcn_s_barrier();
  CBAR();
  return 0.f;
}
DI float sum8(u32 a) {
  float r[8];
#pragma unroll
  for (int i = 0; i < 8; ++i) r[i] = ldl<float>(a + i * 256);
  return ((r[0] + r[1]) + (r[2] + r[3])) + ((r[4] + r[5]) + (r[6] + r[7]));
}

DI void stage_B(const Params& p, const Ctx& c, Sync& S, int m, int kb, int cb, u32 n0) {
  const int lane = c.lane, w = c.w;
  S.wait<C_FULL>(n0 + 16);
  v4u wf[16];
  read_rows<0>(n0, w, lane, wf);
  S.wait<C_CS>(1);
  const v2u cs = ldl<v2u>(L_CS + lane * 8);
  const Masks M = mk_masks();
  const int P = 64 * kb + lane, tile = w + 8 * (lane & 3);
  const u32 pa = opq(L_PRIV + w * 512 + lane * 4), xa = opq(L_PRIV + w * 512 + (lane >> 4) * 8);
  const u32 o0 = opq(W_X + (P >> 3) * 128 + (P & 7) * 8), o1 = opq(W_X + lane * 128 + 64);
  const u32 oo = opq((lane & 4) ? OOB : W_GU + ((8 * cb + (tile >> 2)) * 16 + 2 * kb + m) * 512 + (tile & 3) * 128 + (lane >> 4) * 32 + (lane & 8) * 2);
  LGKM0();
  STAMP(12);
  v2u xg; v4u st;
  for (u32 n = 0;;) {
    CBAR();
    xg = gl8(c.ws, o0);
    st = gl16(c.ws, o1);
    if (all_ok((xg.y ^ c.tag) | (st.y ^ c.tag) | (st.w ^ c.tag)) || ++n > 8192u) break;
    nap<3>();
  }
  CBAR();
  STAMP(13);
  const float mu0 = asf(rfl(st.x));
  float d = asf(st.x) - mu0;
  d += sw32(d, asf(st.z) + 16.f * d * d);
  d = sum_lo3(d); d += dpp<ROR8>(d); d = sum16(d);
  const float e = sw32(d, d) * (1.f / 1024.f);
  d *= 1.f / 64.f;
  const float mb = rbf(mu0 + d), inv = rcp(rbf(__builtin_amdgcn_sqrtf(rbf(rbf(fmx(e - d * d, 0.f)) + 1e-5f))));
  const u32 xm = pk(lo16(xg.x) - mb, hi16(xg.x) - mb), ln = pk(lo16(xm) * inv, hi16(xm) * inv);
  const u32 sc = pk(lo16(ln) * lo16(cs.x), hi16(ln) * lo16(cs.y));
  stl<u32>(pa, pk(lo16(sc) + hi16(cs.x), hi16(sc) + hi16(cs.y)));
  CBAR();
  v4f acc[4];
#pragma unroll
  for (int u = 0; u < 4; ++u) acc[u] = v4f{0.f, 0.f, 0.f, 0.f};
#pragma unroll
  for (int k = 0; k < 4; ++k) {
    const v4u xf = xfrag(xa, k);
#pragma unroll
    for (int u = 0; u < 4; ++u) acc[u] = mfma(wf[k * 4 + u], xf, acc[u]);
  }
  const bool hi = lanes(M_HI8);
  float r0 = sel(hi, acc[0].z, acc[0].x), r1 = sel(hi, acc[0].w, acc[0].y);
#pragma unroll
  for (int u = 1; u < 4; ++u) {
    r0 = sel(lanes(M.m[u]), sel(hi, acc[u].z, acc[u].x), r0);
    r1 = sel(lanes(M.m[u]), sel(hi, acc[u].w, acc[u].y), r1);
  }
  gs16(c.ws, oo, v4u{asu(r0), c.tag, asu(r1), c.tag});
  STAMP(14);
}

DI void stage_D(const Params& p, const Ctx& c, Sync& S, int t, u32 n0) {
  const int lane = c.lane, w = c.w;
  S.wait<C_FULL>(n0 + 16);
  v4u wf[16];
  read_rows<1>(n0, w, lane, wf);
  const Masks M = mk_masks();
  const u32 pa = opq(L_PRIV + w * 512 + lane * 2), xa = opq(L_PRIV + w * 512 + (lane >> 4) * 8);
  const u32 oi = opq(W_GU + (t * 16 + 2 * w) * 512 + lane * 16);
  const u32 wa = opq(L_CS + w * 512 + (lane & 31) * 16 + (lane >> 5) * 4), ra = opq(L_CS + lane * 8);
  const u32 oo = opq(W_D + (tile_of(lane, w) * 56 + t) * 128 + (lane >> 4) * 32 + (lane & 8) * 2);
  LGKM0();
  STAMP(27);
  v4u q[1];
  gather16<1, 3>(S, c.ws, oi, [](int) { return 0u; }, q, c.tag);
  STAMP(28);
  stl<u32>(wa, q[0].x);
  stl<u32>(wa + 8, q[0].z);
  LGKM0();
  __builtin_amdgcn_s_barrier();
  CBAR();
  v2f r[8];
#pragma unroll
  for (int i = 0; i < 8; ++i) r[i] = ldl<v2f>(ra + i * 512);
  const v2f y = ((r[0] + r[1]) + (r[2] + r[3])) + ((r[4] + r[5]) + (r[6] + r[7]));
  const u32 gu = pk(y.x, y.y);
  const float g = lo16(gu);
  const float z = (-2.f * LOG2E * 0.7978845608028654f) * (g + 0.044715f * g * g * g);
  const float h = rbf(g * rcp(1.f + ex2(z))) * hi16(gu);
  stl<u16>(pa, (u16)pk(h, h));
  CBAR();
  gs16(c.ws, oo, tiles8(wf, xfrag(xa, 0), xfrag(xa, 1), M, c.tag));
  STAMP(29);
}

template <int N> DI float col_total(const v4u (&q)[N]) {
  float s0 = asf(q[0].x), s1 = asf(q[0].z);
#pragma unroll
  for (int i = 1; i < N; ++i) { s0 += asf(q[i].x); s1 += asf(q[i].z); }
  s0 += sw32(s0, s1);
  s0 = sum16(s0);
  return s0 + dpp<ROR8>(s0);
}

__global__ void __launch_bounds__(512) fused_block(Params p) {
  const int tid = threadIdx.x, lane = tid & 63, w = rfl(tid >> 6), wg = blockIdx.x;
  const int grp = (wg >> 3) & 3, s = (wg & 7) + ((wg >> 5) << 3);
  const bool isA = s < 16;
  const int lam = (wg >> 3) - 8;
  const int role = isA ? 0 : lam < 14 ? 1 : lam < 21 ? 2 : 3;
  const int tt = (wg & 7) + 8 * (lam % 7), r = 16 * grp + s, v = w & 3;
  const u32 nkv = isA ? 26 : 10, NT = isA ? 26 : 34, ntail = nkv + 2 * NT;
  const u64 tstart = now(), cstart = __builtin_amdgcn_s_memtime();
  Sync S{opq(L_CTRL)};
  if (w == 0) { STAMP(0); S.set_at<0>(S.base + 4 * lane, 0u); }
  Ctx c;
  c.ws = mk(p.ws, W_END);
  c.lane = lane; c.w = w; c.wg = wg; c.grp = grp; c.s = s;
  v4u wa[16], wb[16];
  auto take_wo = [&]() {
    read_rows<1>(10, v, lane, wa);
    read_rows<1>(10, v + 4, lane, wb);
    LGKM0();
    S.add1<C_FREE + 10, 8>();
    S.add1<C_FREE, 8>();
  };

  if (w == 7) {
    __builtin_amdgcn_s_setprio(3);
    Ring R{S, 0, 0, 0, 0, 0, ntail, 0, 0};
    const rsrc_t rm = mk(p.wmod, 1024 * 6144 * 2);
    ld_slab<DEPTH>(R, rm, (wg >> 5) * 128 * 12288 + (wg & 31) * 128, 12288, 2, lane);
    __builtin_amdgcn_s_barrier();
    ld_slab<DEPTH>(R, mk(p.wq, 1024 * 4096 * 2), (wg >> 6) * 256 * 8192 + (wg & 63) * 128, 8192, 4, lane);
    ld_slab<DEPTH>(R, rm, (wg >> 6) * 256 * 12288 + 4096 + (wg & 63) * 128, 12288, 4, lane);
    if (isA) ld_rows<DEPTH, 2048, 1>(R, mk(p.wo, 4096 * 1024 * 2), r * 64 * 2048, lane);
    const rsrc_t rk = mk(p.K, 1u << 26), rv = mk(p.V, 1u << 26);
    const u32 rw = lane >> 4, ps = lane & 15;
    u32 vk[4], vv[2];
#pragma unroll
    for (int i = 0; i < 4; ++i) vk[i] = rw * 256 + ((ps ^ (4 * i + rw)) << 4);
#pragma unroll
    for (int i = 0; i < 2; ++i) vv[i] = rw * 256 + ((ps ^ ((4 * i + rw) << 1)) << 4);
    auto tile_off = [&](int tau) { return ((u32)grp << 24) + (((tau < 8 ? tau * 48 + s - 16 : 384 + (tau - 8) * 64 + s) + ROT * grp) & 2047) * 8192; };
    auto ldk = [&](int tau) {
      const u32 so = tile_off(tau), ld = R.open();
      dma4<DEPTH>(ld, vk[0], vk[1], vk[2], vk[3], rk, so);
      dma4<DEPTH>(ld + 4096, vk[0], vk[1], vk[2], vk[3], rk, so + 4096);
      R.close<DEPTH>();
    };
    auto ldv = [&](int tau) {
      const u32 so = tile_off(tau), ld = R.open();
      dma4<DEPTH>(ld, vv[0], vv[1], vv[0], vv[1], rv, so);
      dma4<DEPTH>(ld + 4096, vv[0], vv[1], vv[0], vv[1], rv, so + 4096);
      R.close<DEPTH>();
    };
    int tau = isA ? 8 : 0;
    if (isA) {
      ldk(8); ldv(8);
      vmwait<16>();
      R.publish(26);
      tau = 9;
      take_wo();
    }
#pragma unroll 1
    for (; tau < 31; ++tau) { ldk(tau); ldv(tau); }
#pragma unroll 1
    for (; tau < 34; ++tau) ldk(tau);
#pragma unroll 1
    for (tau = 31; tau < 34; ++tau) ldv(tau);
    STAMP(3);
    if (role == 1) ld_rows<DEPTH2, 7168, 0>(R, mk(lam < 7 ? p.wg : p.wu, 1024 * 3584 * 2), (wg & 7) * 128 * 7168 + (lam % 7) * 1024, lane);
    else if (role == 2) ld_rows<DEPTH2, 2048, 1>(R, mk(p.wd, 3584 * 1024 * 2), tt * 64 * 2048, lane);
    vmwait<32>(); R.publish(R.n - 4);
    vmwait<24>(); R.publish(R.n - 3);
    vmwait<16>(); R.publish(R.n - 2);
    vmwait<8>(); R.publish(R.n - 1);
    vmwait<0>(); R.publish(R.n);
    STAMP(4);
    STAMPV(22, R.first);
    STAMPV(31, R.stall + 1);
    __builtin_amdgcn_s_setprio(0);
    c.tag = rfl(*(volatile LAS u32*)(S.base + 4 * C_TAG));
  } else if (w < 4) {
    const u32 ep = __hip_atomic_load((GAS u32*)(p.ws + W_EPOCH), __ATOMIC_RELAXED, __HIP_MEMORY_SCOPE_AGENT);
    const u32 c1 = *(const GAS u32*)(p.c + (wg >> 5) * 128 + 2 * lane);
    const v2u c2 = *(const GAS v2u*)(p.c + (wg >> 6) * 256 + 4 * lane);
    const XIn X = load_x(p, c);
    const u32 pa = L_PRIV + w * 1024;
    LGKM0();
    __builtin_amdgcn_s_barrier();
    CBAR();
    auto silu2 = [](u32 a) { const float x = lo16(a), y = hi16(a); return pk(x * sigm(x), y * sigm(y)); };
    stl<u32>(pa + lane * 4, silu2(c1));
    stl<v2u>(pa + 256 + lane * 8, v2u{silu2(c2.x), silu2(c2.y)});
    c.tag = rfl(ep) + 1u;
    S.set<C_TAG>(c.tag);
    S.wait<C_FULL>(2);
    put_tile(c.ws, W_MOD1 + wg * 512 + w * 128, gemv_cols<4>(0, pa, w, lane), c.tag, lane);
    S.add1<C_FREE, 2>();
    if (w == 0) { STAMP(1); }
    make_h(p, c, S, make_ln(X));
    S.wait<C_FULL>(6);
    S.wait<C_H>(4);
    if (w == 0) { STAMP(18); }
    put_tile(c.ws, W_Q + wg * 512 + w * 128, gemv_cols<8>(2, L_HS, w, lane), c.tag, lane);
    S.add1<C_FREE + 2, 4>();
    if (w == 0) { STAMP(19); }
    S.wait<C_FULL>(10);
    put_tile(c.ws, W_MOD2 + wg * 512 + w * 128, gemv_cols<8>(6, pa + 256, w, lane), c.tag, lane);
    S.add1<C_FREE + 6, 4>();
    if (w == 0) { STAMP(20); }
    nap<NAP0B>();
    make_q(p, c, S);
    S.wait<C_Q>(4);
    if (w == 0) { STAMP(2); }
    const u32 g = lane >> 4, j = lane & 15;
    v4u qf[4];
#pragma unroll
    for (int k = 0; k < 4; ++k) qf[k] = ldl<v4u>(L_QS + (j & 7) * 256 + k * 64 + g * 16);
    const u32 kb = j * 256 + ((g ^ j) << 4);
    const u32 r16 = 4 * g + (j >> 2), vb = r16 * 256 + ((r16 & 7) << 5) + (lane & 3) * 8;
    float m = -INFINITY, l = 0.f;
    v4f acc[8];
#pragma unroll
    for (int b = 0; b < 8; ++b) acc[b] = v4f{0.f, 0.f, 0.f, 0.f};
    const u32 ma = opq(L_MRG + lane * 16), mla = opq(L_MRG + 4096 + lane * 8);
    auto raw = [&](u32 n, float (&sv)[8]) {
      const u32 pos = n % NH, ka = pos * 8192;
      S.wait<C_FULL>(n + 1);
      v4u kf[8];
#pragma unroll
      for (int k = 0; k < 4; ++k) { const u32 a = ka + (kb ^ (k << 6)); kf[k] = ldl<v4u>(a); kf[4 + k] = ldl<v4u>(a + 4096); }
      v4f s0 = {0.f, 0.f, 0.f, 0.f}, s1 = s0;
#pragma unroll
      for (int k = 0; k < 4; ++k) { s0 = mfma(kf[k], qf[k], s0); s1 = mfma(kf[4 + k], qf[k], s1); }
      S.set_at<4 * C_FREE>(S.base + 4 * pos, n / NH * 4 + 4);
#pragma unroll
      for (int i = 0; i < 4; ++i) {
        const u32 a = i < 2 ? pk(s0[2 * i], s0[2 * i + 1]) : pk(s1[2 * i - 4], s1[2 * i - 3]);
        const u32 b = pk(lo16(a) * 0.08838834764831845f, hi16(a) * 0.08838834764831845f);
        sv[2 * i] = lo16(b); sv[2 * i + 1] = hi16(b);
      }
      return fmx(fmx(fmx(sv[0], sv[1]), fmx(sv[2], sv[3])), fmx(fmx(sv[4], sv[5]), fmx(sv[6], sv[7])));
    };
    auto probs = [&](const float (&sv)[8]) {
      float e[8];
#pragma unroll
      for (int i = 0; i < 8; ++i) e[i] = ex2((sv[i] - m) * LOG2E);
      l += ((e[0] + e[1]) + (e[2] + e[3])) + ((e[4] + e[5]) + (e[6] + e[7]));
      return v4u{pk(e[0], e[1]), pk(e[2], e[3]), pk(e[4], e[5]), pk(e[6], e[7])};
    };
    auto values = [&](u32 n, v4u pf) {
      const u32 pos = n % NH;
      u32 a[8];
#pragma unroll
      for (int b = 0; b < 8; ++b) a[b] = opq(pos * 8192 + (vb ^ (b << 5)));
      S.wait<C_FULL>(n + 1);
      v4u vf[8];
#pragma unroll
      for (int b = 0; b < 8; ++b) vf[b] = cat(trd(a[b]), trd(a[b] + 4096));
#pragma unroll
      for (int b = 0; b < 8; ++b) acc[b] = mfma(vf[b], pf, acc[b]);
      S.set_at<4 * C_FREE>(S.base + 4 * pos, n / NH * 4 + 4);
    };
    auto fold = [&]() {
      const v2f ml = ldl<v2f>(mla);
      const float mx = fmx(m, ml.x), al = ex2((m - mx) * LOG2E), be = ex2((ml.x - mx) * LOG2E);
      l = l * al + be * ml.y;
      m = mx;
#pragma unroll
      for (int b2 = 0; b2 < 4; ++b2) {
        const v4f o = ldl<v4f>(ma + b2 * 1024) * be;
        acc[2 * b2] = acc[2 * b2] * al + v4f{o.x, o.y, o.x, o.y};
        acc[2 * b2 + 1] = acc[2 * b2 + 1] * al + v4f{o.z, o.w, o.z, o.w};
      }
    };
    auto rescale = [&](float mx) {
      mx = fmx(m, max32(max16(mx)));
      if (__builtin_amdgcn_ballot_w64(mx > m) != 0ull) {
        const float al = ex2((m - mx) * LOG2E);
        l *= al;
#pragma unroll
        for (int b = 0; b < 8; ++b) acc[b] *= al;
        m = mx;
      }
    };
    const int order = (w - (int)NT + 1) & 3;
#pragma unroll 1
    for (u32 it = w; it < NT - 2; it += 4) {
      float sv[8];
      rescale(raw(nkv + 2 * it, sv));
      values(nkv + 2 * it + (it == NT - 3 ? 3 : 1), probs(sv));
    }
    const bool hi = lanes(M_HI8);
    if (order < 3) {
      if (order > 0) { S.wait<C_M>(order); fold(); }
#pragma unroll
      for (int b2 = 0; b2 < 4; ++b2) {
        const v4f a = acc[2 * b2], b = acc[2 * b2 + 1];
        stl<v4f>(ma + b2 * 1024, v4f{sel(hi, a.z, a.x), sel(hi, a.w, a.y), sel(hi, b.z, b.x), sel(hi, b.w, b.y)});
      }
      stl<v2f>(mla, v2f{m, l});
      S.set<C_M>(order + 1);
    } else {
      const u32 n = ntail - 5;
      float sa[8], sb[8];
      const float mxa = raw(n, sa), mxb = raw(n + 1, sb);
      rescale(fmx(mxa, mxb));
      const v4u p0 = probs(sa), p1 = probs(sb);
      const u32 o = opq(W_P + ((16 * grp + 2 * (j & 7)) * 64 + s) * 512 + g * 32 + (j & 8) * 2), o2 = opq(o + 64 * 512);
      const u32 om = opq(lane < 8 ? W_ML + (64 * grp + s) * 128 + lane * 16 : OOB);
      STAMP(23);
      values(n + 3, p0);
      S.wait<C_M>(3);
      STAMP(24);
      const v2f ml = ldl<v2f>(mla);
      const float mx = fmx(m, ml.x), al = ex2((m - mx) * LOG2E), be = ex2((ml.x - mx) * LOG2E);
      gs16(c.ws, om, v4u{asu(mx), c.tag, asu(sum32(sum16(l * al + be * ml.y))), c.tag});
      v4f y[4];
#pragma unroll
      for (int b2 = 0; b2 < 4; ++b2) { y[b2] = ldl<v4f>(ma + b2 * 1024) * be; asm volatile("" : "+v"(y[b2])); }
      STAMP(25);
      values(n + 4, p1);
      STAMP(5);
#pragma unroll
      for (int b = 0; b < 8; ++b)
        gs16(c.ws, (b < 4 ? o : o2) + (b & 3) * 128,
             v4u{asu(sel(hi, acc[b].z, acc[b].x) * al + y[b >> 1][2 * (b & 1)]), c.tag, asu(sel(hi, acc[b].w, acc[b].y) * al + y[b >> 1][2 * (b & 1) + 1]), c.tag});
      STAMP(6);
    }
  } else {
    __builtin_amdgcn_s_barrier();
    if (role == 0) {
      S.wait<C_FULL>(26);
      take_wo();
    }
    S.wait<C_Q>(4);
    c.tag = rfl(*(volatile LAS u32*)(S.base + 4 * C_TAG));
    if (role == 0) {
      if (w == 4) {
      v4u q[2];
      const u32 xp = *(const GAS u32*)(p.x + 16 * r + 2 * (lane & 7));
      gather16<2, 8>(S, c.ws, W_MOD2 + (64 * ((lane >> 3) & 3) + 32 * (lane >> 5) + (r >> 2)) * 512 + ((r & 3) * 16 + 2 * (lane & 7)) * 8,
                     [](int i) { return (u32)i * 16 * 512; }, q, c.tag);
      float y[4] = {asf(q[0].x), asf(q[0].z), asf(q[1].x), asf(q[1].z)}, o[4];
#pragma unroll
      for (int i = 0; i < 4; ++i) { y[i] += dpp<ROR8>(y[i]); y[i] = rbf(sum16(y[i])); o[i] = sw32(y[i], y[i]); }
      stl<v4f>(L_HS + (lane & 7) * 16, v4f{y[0], o[2], lo16(xp), 0.f});
      stl<v4f>(L_HS + 128 + (lane & 7) * 16, v4f{y[1], o[3], hi16(xp), 0.f});
      gs16(c.ws, lane < 8 ? W_CS + r * 128 + lane * 16 : OOB, v4u{pk(1.f + o[0], y[2]), c.tag, pk(1.f + o[1], y[3]), c.tag});
      }
    } else if (role == 1 && w == 4) {
      v4u q[1];
      gather16<1, 32>(S, c.ws, W_CS + (128 * (wg & 7) + 2 * lane) * 8, [](int) { return 0u; }, q, c.tag);
      stl<v2u>(L_CS + lane * 8, v2u{q[0].x, q[0].z});
      S.set<C_CS>(1);
    }
  }

  if (role == 0) {
    const Masks M = mk_masks();
    const u32 pa = L_PRIV + w * 512;
    const u32 xa = opq(pa + (lane >> 4) * 8), oo = opq(W_A + (tile_of(lane, v) * 64 + r) * 128 + (lane >> 4) * 32 + (lane & 8) * 2);
    const u32 ra = opq(L_QS + lane * 4), wr = opq(pa + lane * 2);
    S.wait<C_FULL>(ntail);
    if (w == 4) { STAMP(7); }
    combine_part(p, c, S);
    if (w < 4) return;
    const float o = sum8(ra);
    stl<u16>(wr, (u16)pk(o, o));
    CBAR();
    const v4u x0 = xfrag(xa, 0), x1 = xfrag(xa, 1);
    gs16(c.ws, oo, tiles8(wa, x0, x1, M, c.tag));
    gs16(c.ws, oo + 4 * 64 * 128, tiles8(wb, x0, x1, M, c.tag));
    if (v == 0) {
      STAMP(9);
      const v4f kp = ldl<v4f>(L_HS + (lane >> 5) * 128 + (lane & 7) * 16);
      const u32 ox = opq(lane < 10 ? W_X + r * 128 + lane * 8 : OOB);
      const u32 ia = opq(W_A + (r * 64 + (lane >> 3)) * 128 + (lane & 7) * 16);
      nap<NAP2>();
      v4u q[8];
      gather16<8, 1>(S, c.ws, ia, [](int i) { return (u32)i * 1024; }, q, c.tag);
      STAMP(10);
      const float a = rbf(kp.z + rbf(kp.x * rbf(col_total(q))));
      float a0 = a;
      const float a1 = sw32(a0, a0);
      const float mu = sum_lo3(a0 + a1) * (1.f / 16.f);
      const float m2 = sum_lo3((a0 - mu) * (a0 - mu) + (a1 - mu) * (a1 - mu));
      gs8(c.ws, ox, v2u{asu(sel(lanes(0xff), asf(pk(a0, a1)), sel(lanes(0x100), mu, m2))), c.tag});
      STAMP(11);
      const u32 id = opq(W_D + (r * 56 + (lane >> 3)) * 128 + (lane & 7) * 16);
      const u32 oy = opq(lane < 8 ? 32 * r + 4 * lane : OOB);
      nap<NAP5>();
      v4u q7[7];
      gather16<7, 2>(S, c.ws, id, [](int i) { return (u32)i * 1024; }, q7, c.tag);
      STAMP(15);
      float y0 = a + rbf(kp.y * rbf(col_total(q7)));
      const float y1 = sw32(y0, y0);
      __builtin_amdgcn_raw_buffer_store_b32(pk(y0, y1), mk(p.out, 2048), oy, 0, 0);
      __builtin_amdgcn_raw_buffer_store_b32(c.tag, c.ws, (r | lane) ? OOB : W_EPOCH, 0, 16);
      STAMP(16);
      if (r == 0) {
        const u64 te = now(), ce = __builtin_amdgcn_s_memtime();
        while (now() - te < 300) {}
        u64 t0 = ~0ull, mx = 0;
        for (int i = 0; i < 4; ++i) {
          const u64 a = __hip_atomic_load((GAS u64*)(p.ws + W_STAMP + ((size_t)(lane + 64 * i) * 32) * 8), __ATOMIC_RELAXED, __HIP_MEMORY_SCOPE_AGENT);
          const u64 b = __hip_atomic_load((GAS u64*)(p.ws + W_STAMP + ((size_t)(lane + 64 * i) * 32 + (XK < 32 ? XK : 0)) * 8), __ATOMIC_RELAXED, __HIP_MEMORY_SCOPE_AGENT);
          t0 = a < t0 ? a : t0; mx = b > mx ? b : mx;
        }
        for (int o = 32; o > 0; o >>= 1) { const u64 a = __shfl_xor(t0, o), b = __shfl_xor(mx, o); t0 = a < t0 ? a : t0; mx = b > mx ? b : mx; }
        u64 val = XK == 100 ? te - t0 : XK == 101 ? (ce - cstart) * 100 / (te - tstart) : mx - t0;
        if (val > 20000) val = 20000;
        while (now() - te < val * 100) {}
      }
    }
  } else if (role == 1) {
    stage_B(p, c, S, lam >= 7, wg & 7, lam % 7, ntail);
  } else if (role == 2) {
    stage_D(p, c, S, tt, ntail);
  }
}

extern "C" void kernel_launch(void* const* d_in, const int* in_sizes, int n_in, void* d_out, int out_size, void* d_ws,
                              size_t ws_size, hipStream_t stream) {
  Params p;
  p.x = (const u16*)d_in[0]; p.K = (const u16*)d_in[1]; p.V = (const u16*)d_in[2];
  p.wq = (const u16*)d_in[3]; p.wo = (const u16*)d_in[4];
  p.wg = (const u16*)d_in[6]; p.wu = (const u16*)d_in[7]; p.wd = (const u16*)d_in[8];
  p.c = (const u16*)d_in[9]; p.wmod = (const u16*)d_in[10];
  p.out = (u16*)d_out; p.ws = (char*)d_ws;
  (void)hipFuncSetAttribute((const void*)fused_block, hipFuncAttributeMaxDynamicSharedMemorySize, 163840);
  hipLaunchKernelGGL(fused_block, dim3(256), dim3(512), 163840, stream, p);
}
```

```cpp
#include <hip/hip_runtime.h>
#include <stdint.h>

typedef unsigned short u16;
typedef unsigned u32;
typedef unsigned long long u64;
typedef u32 v4u __attribute__((ext_vector_type(4)));
typedef u32 v2u __attribute__((ext_vector_type(2)));
typedef float v4f __attribute__((ext_vector_type(4)));
typedef float v2f __attribute__((ext_vector_type(2)));
typedef __bf16 v8bf __attribute__((ext_vector_type(8)));
typedef __bf16 v2bf __attribute__((ext_vector_type(2)));
typedef short v4s __attribute__((ext_vector_type(4)));
#define LAS __attribute__((address_space(3)))
#define GAS __attribute__((address_space(1)))
#define DI __device__ __forceinline__
typedef __amdgpu_buffer_rsrc_t rsrc_t;

#define STAMPS 0
#define ROT 37
#define NAP0A 10
#define NAP0B 10
#define NAP1 8
#define NAP2 10
#define NAP5 64
#define PACE0 4
#define PUBNAP 0
#define PACE 0
#define DEPTH 24
#define DEPTH2 16

constexpr u32 W_EPOCH = 0;
constexpr u32 W_MOD1 = 4096;
constexpr u32 W_Q = W_MOD1 + 256 * 512;
constexpr u32 W_MOD2 = W_Q + 256 * 512;
constexpr u32 W_CS = W_MOD2 + 256 * 512;
constexpr u32 W_X = W_CS + 64 * 128;
constexpr u32 W_ML = W_X + 64 * 128;
constexpr u32 W_GU = W_ML + 32 * 64 * 16;
constexpr u32 W_A = W_GU + 56 * 16 * 512;
constexpr u32 W_D = W_A + 64 * 64 * 128;
constexpr u32 W_P = W_D + 64 * 64 * 128;
constexpr u32 W_END = W_P + 64 * 64 * 512;
constexpr u32 W_STAMP = W_END;

constexpr u32 NH = 18;
constexpr u32 L_CTRL = NH * 8192;
constexpr u32 L_MRG = L_CTRL + 256;
constexpr u32 L_QS = L_MRG + 4608;
constexpr u32 L_HS = L_QS + 2048;
constexpr u32 L_CS = L_HS + 512;
constexpr u32 L_PRIV = L_CS + 4096;
constexpr u32 L_END = L_PRIV + 4096;
static_assert(L_END <= 163840, "LDS");
constexpr u32 C_FULL = 0, C_H = 2, C_Q = 3, C_M = 4, C_CS = 5, C_TAG = 6, C_W = 7, C_FREE = 16;
constexpr float LOG2E = 1.4426950408889634f;

struct Params {
  const u16 *x, *K, *V, *wq, *wo, *wg, *wu, *wd, *c, *wmod;
  u16* out;
  char* ws;
};

template <class T> DI T ldl(u32 a) { return *(LAS T*)a; }
template <class T> DI void stl(u32 a, T v) { *(LAS T*)a = v; }
DI u32 rfl(u32 v) { return __builtin_amdgcn_readfirstlane(v); }
DI float asf(u32 v) { return __builtin_bit_cast(float, v); }
DI u32 asu(float v) { return __builtin_bit_cast(u32, v); }
DI float lo16(u32 v) { return asf(v << 16); }
DI float hi16(u32 v) { return asf(v & 0xffff0000u); }
DI u32 pk(float a, float b) { v2f t = {a, b}; return __builtin_bit_cast(u32, __builtin_convertvector(t, v2bf)); }
DI float rbf(float a) { return hi16(pk(a, a)); }
#define CBAR() asm volatile("" ::: "memory")
#define LGKM0() asm volatile("s_waitcnt lgkmcnt(0)" ::: "memory")
DI v4f mfma(v4u a, v4u b, v4f c) {
  return __builtin_amdgcn_mfma_f32_16x16x32_bf16(__builtin_bit_cast(v8bf, a), __builtin_bit_cast(v8bf, b), c, 0, 0, 0);
}
DI v2u trd(u32 addr) { return __builtin_bit_cast(v2u, __builtin_amdgcn_ds_read_tr16_b64_v4i16((LAS v4s*)addr)); }
DI v4u cat(v2u a, v2u b) { return v4u{a.x, a.y, b.x, b.y}; }
template <int CTRL> DI float dpp(float x) { return asf(__builtin_amdgcn_update_dpp(0, asu(x), CTRL, 0xf, 0xf, true)); }
constexpr int XOR1 = 0xB1, XOR2 = 0x4E, HMIR = 0x141, ROR8 = 0x128;
DI float fmx(float a, float b) { return __builtin_amdgcn_fmed3f(a, b, INFINITY); }
DI float sw16(float& a, float b) { auto s = __builtin_amdgcn_permlane16_swap(asu(a), asu(b), false, false); a = asf(s[0]); return asf(s[1]); }
DI float sw32(float& a, float b) { auto s = __builtin_amdgcn_permlane32_swap(asu(a), asu(b), false, false); a = asf(s[0]); return asf(s[1]); }
DI float sum16(float x) { const float o = sw16(x, x); return x + o; }
DI float sum32(float x) { const float o = sw32(x, x); return x + o; }
DI float max16(float x) { const float o = sw16(x, x); return fmx(x, o); }
DI float max32(float x) { const float o = sw32(x, x); return fmx(x, o); }
DI float sum_lo3(float x) { x += dpp<XOR1>(x); x += dpp<XOR2>(x); return x + dpp<HMIR>(x); }
DI float sum64(float x) { x = sum_lo3(x); x += dpp<ROR8>(x); return sum32(sum16(x)); }
DI float max64(float x) {
  x = fmx(x, dpp<XOR1>(x)); x = fmx(x, dpp<XOR2>(x)); x = fmx(x, dpp<HMIR>(x)); x = fmx(x, dpp<ROR8>(x));
  return max32(max16(x));
}
DI float sel(bool c, float a, float b) { asm("" : "+v"(a)); asm("" : "+v"(b)); return c ? a : b; }
DI float ex2(float x) { return __builtin_amdgcn_exp2f(x); }
DI float rcp(float x) { return __builtin_amdgcn_rcpf(x); }
DI u32 opq(u32 v) { asm volatile("" : "+v"(v)); return v; }
DI u64 opqs(u64 v) { asm volatile("" : "+s"(v)); return v; }
DI bool lanes(u64 m) { return __builtin_amdgcn_inverse_ballot_w64(m); }
constexpr u64 M_HI8 = 0xff00ff00ff00ff00ull, M_HI32 = 0xffffffff00000000ull;
constexpr u32 OOB = 0x80000000u;

DI u64 now() { return __builtin_amdgcn_s_memrealtime(); }
#define STAMPV(i, val) do {} while (0)
#define STAMP(i) STAMPV(i, now())

struct Sync {
  u32 base;
  template <u32 OFF> DI void wait_at(u32 addr, u32 v) {
    u32 t, s, n;
    asm volatile("s_movk_i32 %2, 0x7fff\n1:\n\tds_read_b32 %0, %3 offset:%c5\n\ts_waitcnt lgkmcnt(0)\n\tv_readfirstlane_b32 %1, %0\n\t"
                 "s_cmp_ge_u32 %1, %4\n\ts_cbranch_scc1 2f\n\ts_sleep 1\n\ts_sub_u32 %2, %2, 1\n\ts_cbranch_scc0 1b\n2:"
                 : "=&v"(t), "=&s"(s), "=&s"(n) : "v"(addr), "s"(v), "i"(OFF) : "memory", "scc");
  }
  template <u32 W> DI void wait(u32 v) { wait_at<4 * W>(base, v); }
  template <u32 W, int N> DI void add1() {
    u64 e;
    asm volatile("s_mov_b64 %0, exec\n\ts_mov_b64 exec, %4\n\tds_add_u32 %1, %2 offset:%c3\n\ts_mov_b64 exec, %0"
                 : "=&s"(e) : "v"(base + (N > 1 ? 4 * (threadIdx.x & 63) : 0)), "v"(1u), "i"(4 * W), "s"((u64)((1ull << N) - 1)) : "memory");
  }
  template <u32 OFF> DI void set_at(u32 addr, u32 v) {
    asm volatile("ds_write_b32 %0, %1 offset:%c2" :: "v"(addr), "v"(v), "i"(OFF) : "memory");
  }
  template <u32 W> DI void set(u32 v) { set_at<4 * W>(base, v); }
};
DI v4u gl16(rsrc_t r, u32 off, u32 so = 0) { return __builtin_amdgcn_raw_buffer_load_b128(r, off, so, 16); }
DI v2u gl8(rsrc_t r, u32 off) { return __builtin_amdgcn_raw_buffer_load_b64(r, off, 0, 16); }
DI void gs16(rsrc_t r, u32 off, v4u v) { __builtin_amdgcn_raw_buffer_store_b128(v, r, off, 0, 16); }
DI void gs8(rsrc_t r, u32 off, v2u v) { __builtin_amdgcn_raw_buffer_store_b64(v, r, off, 0, 16); }
template <int K> DI void nap() { if constexpr (K > 0) __builtin_amdgcn_s_sleep(K); }
DI bool all_ok(u32 bad) { return __builtin_amdgcn_ballot_w64(bad != 0u) == 0ull; }
template <int N, int NAP, class F> DI void gather16(Sync& S, rsrc_t r, u32 base, F f, v4u (&v)[N], u32 tag) {
  for (u32 n = 0;;) {
    CBAR();
#pragma unroll
    for (int i = 0; i < N; ++i) v[i] = gl16(r, base + (f(i) & 4095u), f(i) & ~4095u);
    u32 bad = 0;
#pragma unroll
    for (int i = 0; i < N; ++i) bad |= (v[i].y ^ tag) | (v[i].w ^ tag);
    if (all_ok(bad) || ++n > 8192u) break;
    nap<NAP>();
  }
  CBAR();
}

template <int D> DI void dma4(u32 ld, u32 v0, u32 v1, u32 v2, u32 v3, rsrc_t r, u32 so) {
  asm volatile("s_mov_b32 m0, %0\n\ts_nop 0\n\t"
               "buffer_load_dwordx4 %1, %5, %6 offen nt lds\n\ts_waitcnt vmcnt(%c7)\n\t"
               "buffer_load_dwordx4 %2, %5, %6 offen offset:1024 nt lds\n\ts_waitcnt vmcnt(%c7)\n\t"
               "buffer_load_dwordx4 %3, %5, %6 offen offset:2048 nt lds\n\ts_waitcnt vmcnt(%c7)\n\t"
               "buffer_load_dwordx4 %4, %5, %6 offen offset:3072 nt lds\n\ts_waitcnt vmcnt(%c7)"
               :: "s"(ld), "v"(v0), "v"(v1), "v"(v2), "v"(v3), "s"(r), "s"(so), "i"(D) : "memory");
}
template <int D> DI void dma1(u32 ld, u32 v, rsrc_t r, u32 so) {
  asm volatile("s_mov_b32 m0, %0\n\ts_nop 0\n\tbuffer_load_dwordx4 %1, %2, %3 offen nt lds\n\ts_waitcnt vmcnt(%c4)"
               :: "s"(ld), "v"(v), "s"(r), "s"(so), "i"(D) : "memory");
}
template <int D> DI void vmwait() { asm volatile("s_waitcnt vmcnt(%c0)" :: "i"(D) : "memory"); }
struct Ring {
  Sync S;
  u32 n, pos, need, full, next, lim;
  u64 stall, first;
  DI void publish(u32 f) { if ((int)(f - full) > 0) { full = f; nap<PUBNAP>(); S.set<C_FULL>(f); } }
  DI u32 open() {
    if (n >= NH) {
      const u32 fa = S.base + 4 * pos;
      if (rfl(*(volatile LAS u32*)(fa + 4 * C_FREE)) < need) {
        vmwait<0>();
        publish(n);
        S.wait_at<4 * C_FREE>(fa, need);
      }
    }
    return pos * 8192u;
  }
  template <int D> DI void close() {
    ++n; ++pos;
    if (pos == NH) { pos = 0; need += 4; }
    publish(n - D / 8);
  }
};
DI rsrc_t mk(const void* p, u32 bytes) { return __builtin_amdgcn_make_buffer_rsrc((void*)p, 0, bytes, 0x00020000); }
template <int D> DI void ld_slab(Ring& R, rsrc_t r, u32 base, u32 pitch, int nh, int lane) {
  const u32 row = lane >> 3, v = row * pitch + (((lane & 7) ^ (((row >> 1) & 3) << 1)) << 4);
#pragma unroll 1
  for (int h = 0; h < nh; ++h) {
    const u32 ld = R.open();
#pragma unroll
    for (int i = 0; i < 8; ++i) { dma1<D>(ld + i * 1024, v, r, base); base += 8 * pitch; }
    R.close<D>();
  }
}
template <int D, u32 PITCH, int SH> DI void ld_rows(Ring& R, rsrc_t r, u32 base, int lane) {
  u32 v[8];
#pragma unroll
  for (int i = 0; i < 8; ++i) v[i] = ((lane ^ (i << 1)) << 4) + i * PITCH - (i & 3) * 1024;
#pragma unroll 1
  for (int h = 0; h < 16; ++h) {
    const u32 ld = R.open(), so = base + (h >> SH) * 8 * PITCH + (h & ((1 << SH) - 1)) * 1024;
    dma4<D>(ld, v[0], v[1], v[2], v[3], r, so);
    dma4<D>(ld + 4096, v[4], v[5], v[6], v[7], r, so);
    R.close<D>();
  }
}

DI v4u xfrag(u32 addr, int k) { return cat(ldl<v2u>(addr + k * 64), ldl<v2u>(addr + k * 64 + 32)); }
template <int NK> DI v4f gemv_cols(u32 n0, u32 xaddr, int c, int lane) {
  const u32 g = lane >> 4, r16 = 4 * g + ((lane & 15) >> 2);
  const u32 a = opq(n0 * 8192 + r16 * 128 + ((c ^ ((r16 >> 1) & 3)) << 5) + (lane & 3) * 8), xa = opq(xaddr + g * 8);
  v4f acc = {0.f, 0.f, 0.f, 0.f};
#pragma unroll
  for (int k = 0; k < NK; ++k) acc = mfma(cat(trd(a + k * 4096), trd(a + k * 4096 + 2048)), xfrag(xa, k), acc);
  return acc;
}
DI void put_tile(rsrc_t ws, u32 off, v4f acc, u32 tag, int lane) {
  const bool hi = lanes(M_HI8);
  gs16(ws, (lane & 7) ? OOB : off + (lane >> 4) * 32 + (lane & 8) * 2, v4u{asu(sel(hi, acc.z, acc.x)), tag, asu(sel(hi, acc.w, acc.y)), tag});
}
DI u32 hs_addr(u32 n) { return (n % NH) * 8192u; }
template <int SH> DI void read_rows(u32 n0, int v, int lane, v4u (&wf)[16]) {
  const u32 g = lane >> 4, r8 = (4 * g + ((lane & 15) >> 2)) & 7;
  const u32 lo = r8 * 1024 + ((v ^ r8) << 5) + (lane & 3) * 8;
#pragma unroll
  for (int q = 0; q < 4; ++q) {
    const int half = SH ? q >> 1 : 0, k = SH ? q & 1 : q;
    u32 a[2];
#pragma unroll
    for (int t = 0; t < 2; ++t) {
      const u32 h0 = hs_addr(n0 + ((4 * k + 2 * t) << SH) + half), h1 = hs_addr(n0 + ((4 * k + 2 * t + 1) << SH) + half);
      a[t] = lo + (lanes(M_HI32) ? h1 : h0);
    }
#pragma unroll
    for (int u = 0; u < 4; ++u) wf[SH ? (half * 4 + u) * 2 + k : 4 * k + u] = cat(trd(a[0] + u * 256), trd(a[1] + u * 256));
  }
}
struct Masks { u64 m[8]; };
DI Masks mk_masks() {
  Masks M;
#pragma unroll
  for (int i = 0; i < 8; ++i) M.m[i] = opqs(0x0101010101010101ull << i);
  return M;
}
DI v4u tiles8(const v4u (&wf)[16], v4u x0, v4u x1, const Masks& M, u32 tag) {
  v4f acc[8];
#pragma unroll
  for (int i = 0; i < 8; ++i) acc[i] = mfma(wf[2 * i], x0, v4f{0.f, 0.f, 0.f, 0.f});
#pragma unroll
  for (int i = 0; i < 8; ++i) acc[i] = mfma(wf[2 * i + 1], x1, acc[i]);
  const bool hi = lanes(M_HI8);
  float o0 = sel(hi, acc[0].z, acc[0].x), o1 = sel(hi, acc[0].w, acc[0].y);
#pragma unroll
  for (int i = 1; i < 8; ++i) {
    o0 = sel(lanes(M.m[i]), sel(hi, acc[i].z, acc[i].x), o0);
    o1 = sel(lanes(M.m[i]), sel(hi, acc[i].w, acc[i].y), o1);
  }
  return v4u{asu(o0), tag, asu(o1), tag};
}
DI u32 tile_of(int lane, int v) { return 32 * ((lane >> 2) & 1) + 8 * (lane & 3) + v; }

struct Ctx { rsrc_t ws; u32 tag; int lane, w, wg, grp, s; };

struct XIn { v4u xa, xb; u32 xp; };
DI XIn load_x(const Params& p, const Ctx& c) {
  return XIn{*(const GAS v4u*)(p.x + c.lane * 16), *(const GAS v4u*)(p.x + c.lane * 16 + 8),
             *(const GAS u32*)(p.x + 256 * (c.wg >> 6) + 64 * c.w + 2 * (c.lane & 31))};
}
DI float make_ln(const XIn& X) {
  const v4u xa = X.xa, xb = X.xb;
  float sm = 0.f;
#pragma unroll
  for (int i = 0; i < 4; ++i) sm += (lo16(xa[i]) + hi16(xa[i])) + (lo16(xb[i]) + hi16(xb[i]));
  const float mean = sum64(sm) * (1.f / 1024.f);
  float q = 0.f;
#pragma unroll
  for (int i = 0; i < 4; ++i) {
    const float a = lo16(xa[i]) - mean, b = hi16(xa[i]) - mean, d = lo16(xb[i]) - mean, e = hi16(xb[i]) - mean;
    q += (a * a + b * b) + (d * d + e * e);
  }
  const float var = sum64(q) * (1.f / 1024.f);
  const float mb = rbf(mean), den = rbf(sqrtf(rbf(rbf(var) + 1e-5f)));
  return asf(opq(asu(rbf(rbf((lanes(M_HI32) ? hi16(X.xp) : lo16(X.xp)) - mb) / den))));
}
DI void make_h(const Params& p, const Ctx& c, Sync& S, float ln) {
  const int lane = c.lane, kq = c.wg >> 6;
  const u32 base = opq(W_MOD1 + (32 * (lane >> 5) + 4 * kq + c.w) * 512 + (lane & 31) * 16);
  const u32 ha = opq(L_HS + (64 * c.w + 2 * (lane & 31) + (lane >> 5)) * 2);
  v4u v[8];
  nap<NAP0A>();
  gather16<8, 4>(S, c.ws, base, [](int i) { return (u32)(64 * (i & 3) + 16 * (i >> 2)) * 512; }, v, c.tag);
  if (c.w == 0) { STAMP(17); }
  float s0 = (asf(v[0].x) + asf(v[1].x)) + (asf(v[2].x) + asf(v[3].x)), c0 = (asf(v[4].x) + asf(v[5].x)) + (asf(v[6].x) + asf(v[7].x));
  s0 += sw32(s0, (asf(v[0].z) + asf(v[1].z)) + (asf(v[2].z) + asf(v[3].z)));
  c0 += sw32(c0, (asf(v[4].z) + asf(v[5].z)) + (asf(v[6].z) + asf(v[7].z)));
  const float h = rbf(ln * rbf(1.f + rbf(c0))) + rbf(s0);
  stl<u16>(ha, (u16)pk(h, h));
  S.add1<C_H, 1>();
}
DI void make_q(const Params& p, const Ctx& c, Sync& S) {
  const int lane = c.lane;
  v4u v[8];
  const u32 base = W_Q + (16 * c.grp + 4 * c.w + (lane >> 5)) * 512 + (lane & 31) * 16;
  gather16<8, 4>(S, c.ws, base, [](int i) { return (u32)(64 * (i & 3) + 2 * (i >> 2)) * 512; }, v, c.tag);
#pragma unroll
  for (int i = 0; i < 8; i += 4)
    stl<u32>(L_QS + (128 * c.w + 16 * i + lane) * 4,
             pk((asf(v[i].x) + asf(v[i + 1].x)) + (asf(v[i + 2].x) + asf(v[i + 3].x)), (asf(v[i].z) + asf(v[i + 1].z)) + (asf(v[i + 2].z) + asf(v[i + 3].z))));
  S.add1<C_Q, 1>();
}

DI float combine_part(const Params& p, const Ctx& c, Sync& S) {
  const int lane = c.lane, a = 16 * c.grp + c.s, sl = 8 * c.w + (lane >> 3);
  const u32 base = opq(W_P + (a * 64 + sl) * 512 + (lane & 7) * 16);
  const int dim = 2 * (lane & 7) + 32 * (lane >> 5) + 16 * ((lane >> 4) & 1) + ((lane >> 3) & 1);
  const u32 ra = opq(L_QS + (c.w * 64 + dim) * 4);
  if (c.w == 5) {
    v4u ml[1];
    gather16<1, 2>(S, c.ws, W_ML + (c.grp * 64 + lane) * 128 + (c.s >> 1) * 16, [](int) { return 0u; }, ml, c.tag);
    const float e = ex2((asf(ml[0].x) - max64(asf(ml[0].x))) * LOG2E);
    stl<float>(L_HS + 256 + lane * 4, e * (1.f / sum64(e * asf(ml[0].z))));
    S.set<C_W>(1);
    STAMP(26);
  }
  S.wait<C_W>(1);
  const float wt = ldl<float>(L_HS + 256 + sl * 4);
  nap<NAP1>();
  v4u v[4];
  gather16<4, 1>(S, c.ws, base, [](int i) { return (u32)i * 128; }, v, c.tag);
  STAMP(8);
  float z0 = asf(v[0].x) * wt, z1 = asf(v[0].z) * wt, z2 = asf(v[1].x) * wt, z3 = asf(v[1].z) * wt;
  z0 += sw32(z0, asf(v[2].x) * wt); z1 += sw32(z1, asf(v[2].z) * wt);
  z2 += sw32(z2, asf(v[3].x) * wt); z3 += sw32(z3, asf(v[3].z) * wt);
  z0 += sw16(z0, z2); z1 += sw16(z1, z3);
  const bool up = lanes(M_HI8);
  stl<float>(ra, sel(up, z1, z0) + dpp<ROR8>(sel(up, z0, z1)));
  LGKM0();
  __builtin_amdgcn_s_barrier();
  CBAR();
  return 0.f;
}
DI float sum8(u32 a) {
  float r[8];
#pragma unroll
  for (int i = 0; i < 8; ++i) r[i] = ldl<float>(a + i * 256);
  return ((r[0] + r[1]) + (r[2] + r[3])) + ((r[4] + r[5]) + (r[6] + r[7]));
}

DI void stage_B(const Params& p, const Ctx& c, Sync& S, int m, int kb, int cb, u32 n0) {
  const int lane = c.lane, w = c.w;
  S.wait<C_FULL>(n0 + 16);
  v4u wf[16];
  read_rows<0>(n0, w, lane, wf);
  S.wait<C_CS>(1);
  const v2u cs = ldl<v2u>(L_CS + lane * 8);
  const Masks M = mk_masks();
  const int P = 64 * kb + lane, tile = w + 8 * (lane & 3);
  const u32 pa = opq(L_PRIV + w * 512 + lane * 4), xa = opq(L_PRIV + w * 512 + (lane >> 4) * 8);
  const u32 o0 = opq(W_X + (P >> 3) * 128 + (P & 7) * 8), o1 = opq(W_X + lane * 128 + 64);
  const u32 oo = opq((lane & 4) ? OOB : W_GU + ((8 * cb + (tile >> 2)) * 16 + 2 * kb + m) * 512 + (tile & 3) * 128 + (lane >> 4) * 32 + (lane & 8) * 2);
  LGKM0();
  STAMP(12);
  v2u xg; v4u st;
  for (u32 n = 0;;) {
    CBAR();
    xg = gl8(c.ws, o0);
    st = gl16(c.ws, o1);
    if (all_ok((xg.y ^ c.tag) | (st.y ^ c.tag) | (st.w ^ c.tag)) || ++n > 8192u) break;
    nap<3>();
  }
  CBAR();
  STAMP(13);
  const float mu0 = asf(rfl(st.x));
  float d = asf(st.x) - mu0;
  d += sw32(d, asf(st.z) + 16.f * d * d);
  d = sum_lo3(d); d += dpp<ROR8>(d); d = sum16(d);
  const float e = sw32(d, d) * (1.f / 1024.f);
  d *= 1.f / 64.f;
  const float mb = rbf(mu0 + d), inv = rcp(rbf(__builtin_amdgcn_sqrtf(rbf(rbf(fmx(e - d * d, 0.f)) + 1e-5f))));
  const u32 xm = pk(lo16(xg.x) - mb, hi16(xg.x) - mb), ln = pk(lo16(xm) * inv, hi16(xm) * inv);
  const u32 sc = pk(lo16(ln) * lo16(cs.x), hi16(ln) * lo16(cs.y));
  stl<u32>(pa, pk(lo16(sc) + hi16(cs.x), hi16(sc) + hi16(cs.y)));
  CBAR();
  v4f acc[4];
#pragma unroll
  for (int u = 0; u < 4; ++u) acc[u] = v4f{0.f, 0.f, 0.f, 0.f};
#pragma unroll
  for (int k = 0; k < 4; ++k) {
    const v4u xf = xfrag(xa, k);
#pragma unroll
    for (int u = 0; u < 4; ++u) acc[u] = mfma(wf[k * 4 + u], xf, acc[u]);
  }
  const bool hi = lanes(M_HI8);
  float r0 = sel(hi, acc[0].z, acc[0].x), r1 = sel(hi, acc[0].w, acc[0].y);
#pragma unroll
  for (int u = 1; u < 4; ++u) {
    r0 = sel(lanes(M.m[u]), sel(hi, acc[u].z, acc[u].x), r0);
    r1 = sel(lanes(M.m[u]), sel(hi, acc[u].w, acc[u].y), r1);
  }
  gs16(c.ws, oo, v4u{asu(r0), c.tag, asu(r1), c.tag});
  STAMP(14);
}

DI void stage_D(const Params& p, const Ctx& c, Sync& S, int t, u32 n0) {
  const int lane = c.lane, w = c.w;
  S.wait<C_FULL>(n0 + 16);
  v4u wf[16];
  read_rows<1>(n0, w, lane, wf);
  const Masks M = mk_masks();
  const u32 pa = opq(L_PRIV + w * 512 + lane * 2), xa = opq(L_PRIV + w * 512 + (lane >> 4) * 8);
  const u32 oi = opq(W_GU + (t * 16 + 2 * w) * 512 + lane * 16);
  const u32 wa = opq(L_CS + w * 512 + (lane & 31) * 16 + (lane >> 5) * 4), ra = opq(L_CS + lane * 8);
  const u32 oo = opq(W_D + (tile_of(lane, w) * 56 + t) * 128 + (lane >> 4) * 32 + (lane & 8) * 2);
  LGKM0();
  STAMP(27);
  v4u q[1];
  gather16<1, 3>(S, c.ws, oi, [](int) { return 0u; }, q, c.tag);
  STAMP(28);
  stl<u32>(wa, q[0].x);
  stl<u32>(wa + 8, q[0].z);
  LGKM0();
  __builtin_amdgcn_s_barrier();
  CBAR();
  v2f r[8];
#pragma unroll
  for (int i = 0; i < 8; ++i) r[i] = ldl<v2f>(ra + i * 512);
  const v2f y = ((r[0] + r[1]) + (r[2] + r[3])) + ((r[4] + r[5]) + (r[6] + r[7]));
  const u32 gu = pk(y.x, y.y);
  const float g = lo16(gu);
  const float z = (-2.f * LOG2E * 0.7978845608028654f) * (g + 0.044715f * g * g * g);
  const float h = rbf(g * rcp(1.f + ex2(z))) * hi16(gu);
  stl<u16>(pa, (u16)pk(h, h));
  CBAR();
  gs16(c.ws, oo, tiles8(wf, xfrag(xa, 0), xfrag(xa, 1), M, c.tag));
  STAMP(29);
}

template <int N> DI float col_total(const v4u (&q)[N]) {
  float s0 = asf(q[0].x), s1 = asf(q[0].z);
#pragma unroll
  for (int i = 1; i < N; ++i) { s0 += asf(q[i].x); s1 += asf(q[i].z); }
  s0 += sw32(s0, s1);
  s0 = sum16(s0);
  return s0 + dpp<ROR8>(s0);
}

__global__ void __launch_bounds__(512) fused_block(Params p) {
  const int tid = threadIdx.x, lane = tid & 63, w = rfl(tid >> 6), wg = blockIdx.x;
  const int grp = (wg >> 3) & 3, s = (wg & 7) + ((wg >> 5) << 3);
  const bool isA = s < 16;
  const int lam = (wg >> 3) - 8;
  const int role = isA ? 0 : lam < 14 ? 1 : lam < 21 ? 2 : 3;
  const int tt = (wg & 7) + 8 * (lam % 7), r = 16 * grp + s, v = w & 3;
  const u32 nkv = isA ? 26 : 10, NT = isA ? 26 : 34, ntail = nkv + 2 * NT;
  Sync S{opq(L_CTRL)};
  if (w == 0) { STAMP(0); S.set_at<0>(S.base + 4 * lane, 0u); }
  Ctx c;
  c.ws = mk(p.ws, W_END);
  c.lane = lane; c.w = w; c.wg = wg; c.grp = grp; c.s = s;
  v4u wa[16], wb[16];
  auto take_wo = [&]() {
    read_rows<1>(10, v, lane, wa);
    read_rows<1>(10, v + 4, lane, wb);
    LGKM0();
    S.add1<C_FREE + 10, 8>();
    S.add1<C_FREE, 8>();
  };

  if (w == 7) {
    __builtin_amdgcn_s_setprio(3);
    Ring R{S, 0, 0, 0, 0, 0, ntail, 0, 0};
    const rsrc_t rm = mk(p.wmod, 1024 * 6144 * 2);
    ld_slab<DEPTH>(R, rm, (wg >> 5) * 128 * 12288 + (wg & 31) * 128, 12288, 2, lane);
    __builtin_amdgcn_s_barrier();
    ld_slab<DEPTH>(R, mk(p.wq, 1024 * 4096 * 2), (wg >> 6) * 256 * 8192 + (wg & 63) * 128, 8192, 4, lane);
    ld_slab<DEPTH>(R, rm, (wg >> 6) * 256 * 12288 + 4096 + (wg & 63) * 128, 12288, 4, lane);
    if (isA) ld_rows<DEPTH, 2048, 1>(R, mk(p.wo, 4096 * 1024 * 2), r * 64 * 2048, lane);
    const rsrc_t rk = mk(p.K, 1u << 26), rv = mk(p.V, 1u << 26);
    const u32 rw = lane >> 4, ps = lane & 15;
    u32 vk[4], vv[2];
#pragma unroll
    for (int i = 0; i < 4; ++i) vk[i] = rw * 256 + ((ps ^ (4 * i + rw)) << 4);
#pragma unroll
    for (int i = 0; i < 2; ++i) vv[i] = rw * 256 + ((ps ^ ((4 * i + rw) << 1)) << 4);
    auto tile_off = [&](int tau) { return ((u32)grp << 24) + (((tau < 8 ? tau * 48 + s - 16 : 384 + (tau - 8) * 64 + s) + ROT * grp) & 2047) * 8192; };
    auto ldk = [&](int tau) {
      const u32 so = tile_off(tau), ld = R.open();
      dma4<DEPTH>(ld, vk[0], vk[1], vk[2], vk[3], rk, so);
      dma4<DEPTH>(ld + 4096, vk[0], vk[1], vk[2], vk[3], rk, so + 4096);
      R.close<DEPTH>();
    };
    auto ldv = [&](int tau) {
      const u32 so = tile_off(tau), ld = R.open();
      dma4<DEPTH>(ld, vv[0], vv[1], vv[0], vv[1], rv, so);
      dma4<DEPTH>(ld + 4096, vv[0], vv[1], vv[0], vv[1], rv, so + 4096);
      R.close<DEPTH>();
    };
    int tau = isA ? 8 : 0;
    if (isA) {
      ldk(8); ldv(8);
      vmwait<16>();
      R.publish(26);
      tau = 9;
      take_wo();
    }
#pragma unroll 1
    for (; tau < 31; ++tau) { ldk(tau); ldv(tau); }
#pragma unroll 1
    for (; tau < 34; ++tau) ldk(tau);
#pragma unroll 1
    for (tau = 31; tau < 34; ++tau) ldv(tau);
    STAMP(3);
    if (role == 1) ld_rows<DEPTH2, 7168, 0>(R, mk(lam < 7 ? p.wg : p.wu, 1024 * 3584 * 2), (wg & 7) * 128 * 7168 + (lam % 7) * 1024, lane);
    else if (role == 2) ld_rows<DEPTH2, 2048, 1>(R, mk(p.wd, 3584 * 1024 * 2), tt * 64 * 2048, lane);
    vmwait<32>(); R.publish(R.n - 4);
    vmwait<24>(); R.publish(R.n - 3);
    vmwait<16>(); R.publish(R.n - 2);
    vmwait<8>(); R.publish(R.n - 1);
    vmwait<0>(); R.publish(R.n);
    STAMP(4);
    STAMPV(22, R.first);
    STAMPV(31, R.stall + 1);
    __builtin_amdgcn_s_setprio(0);
    c.tag = rfl(*(volatile LAS u32*)(S.base + 4 * C_TAG));
  } else if (w < 4) {
    const u32 ep = __hip_atomic_load((GAS u32*)(p.ws + W_EPOCH), __ATOMIC_RELAXED, __HIP_MEMORY_SCOPE_AGENT);
    const u32 c1 = *(const GAS u32*)(p.c + (wg >> 5) * 128 + 2 * lane);
    const v2u c2 = *(const GAS v2u*)(p.c + (wg >> 6) * 256 + 4 * lane);
    const XIn X = load_x(p, c);
    const u32 pa = L_PRIV + w * 1024;
    LGKM0();
    __builtin_amdgcn_s_barrier();
    CBAR();
    auto silu2 = [](u32 a) { const float x = lo16(a), y = hi16(a); return pk(x / (1.f + ex2(-LOG2E * x)), y / (1.f + ex2(-LOG2E * y))); };
    stl<u32>(pa + lane * 4, silu2(c1));
    stl<v2u>(pa + 256 + lane * 8, v2u{silu2(c2.x), silu2(c2.y)});
    c.tag = rfl(ep) + 1u;
    S.set<C_TAG>(c.tag);
    S.wait<C_FULL>(2);
    put_tile(c.ws, W_MOD1 + wg * 512 + w * 128, gemv_cols<4>(0, pa, w, lane), c.tag, lane);
    S.add1<C_FREE, 2>();
    if (w == 0) { STAMP(1); }
    make_h(p, c, S, make_ln(X));
    S.wait<C_FULL>(6);
    S.wait<C_H>(4);
    if (w == 0) { STAMP(18); }
    put_tile(c.ws, W_Q + wg * 512 + w * 128, gemv_cols<8>(2, L_HS, w, lane), c.tag, lane);
    S.add1<C_FREE + 2, 4>();
    if (w == 0) { STAMP(19); }
    S.wait<C_FULL>(10);
    put_tile(c.ws, W_MOD2 + wg * 512 + w * 128, gemv_cols<8>(6, pa + 256, w, lane), c.tag, lane);
    S.add1<C_FREE + 6, 4>();
    if (w == 0) { STAMP(20); }
    nap<NAP0B>();
    make_q(p, c, S);
    S.wait<C_Q>(4);
    if (w == 0) { STAMP(2); }
    const u32 g = lane >> 4, j = lane & 15;
    v4u qf[4];
#pragma unroll
    for (int k = 0; k < 4; ++k) qf[k] = ldl<v4u>(L_QS + (j & 7) * 256 + k * 64 + g * 16);
    const u32 kb = j * 256 + ((g ^ j) << 4);
    const u32 r16 = 4 * g + (j >> 2), vb = r16 * 256 + ((r16 & 7) << 5) + (lane & 3) * 8;
    float m = -INFINITY, l = 0.f;
    v4f acc[8];
#pragma unroll
    for (int b = 0; b < 8; ++b) acc[b] = v4f{0.f, 0.f, 0.f, 0.f};
    const u32 ma = opq(L_MRG + lane * 16), mla = opq(L_MRG + 4096 + lane * 8);
    auto raw = [&](u32 n, float (&sv)[8]) {
      const u32 pos = n % NH, ka = pos * 8192;
      S.wait<C_FULL>(n + 1);
      v4u kf[8];
#pragma unroll
      for (int k = 0; k < 4; ++k) { const u32 a = ka + (kb ^ (k << 6)); kf[k] = ldl<v4u>(a); kf[4 + k] = ldl<v4u>(a + 4096); }
      v4f s0 = {0.f, 0.f, 0.f, 0.f}, s1 = s0;
#pragma unroll
      for (int k = 0; k < 4; ++k) { s0 = mfma(kf[k], qf[k], s0); s1 = mfma(kf[4 + k], qf[k], s1); }
      S.set_at<4 * C_FREE>(S.base + 4 * pos, n / NH * 4 + 4);
#pragma unroll
      for (int i = 0; i < 4; ++i) {
        const u32 a = i < 2 ? pk(s0[2 * i], s0[2 * i + 1]) : pk(s1[2 * i - 4], s1[2 * i - 3]);
        const u32 b = pk(lo16(a) * 0.08838834764831845f, hi16(a) * 0.08838834764831845f);
        sv[2 * i] = lo16(b); sv[2 * i + 1] = hi16(b);
      }
      return fmx(fmx(fmx(sv[0], sv[1]), fmx(sv[2], sv[3])), fmx(fmx(sv[4], sv[5]), fmx(sv[6], sv[7])));
    };
    auto probs = [&](const float (&sv)[8]) {
      float e[8];
#pragma unroll
      for (int i = 0; i < 8; ++i) e[i] = ex2((sv[i] - m) * LOG2E);
      l += ((e[0] + e[1]) + (e[2] + e[3])) + ((e[4] + e[5]) + (e[6] + e[7]));
      return v4u{pk(e[0], e[1]), pk(e[2], e[3]), pk(e[4], e[5]), pk(e[6], e[7])};
    };
    auto values = [&](u32 n, v4u pf) {
      const u32 pos = n % NH;
      u32 a[8];
#pragma unroll
      for (int b = 0; b < 8; ++b) a[b] = opq(pos * 8192 + (vb ^ (b << 5)));
      S.wait<C_FULL>(n + 1);
      v4u vf[8];
#pragma unroll
      for (int b = 0; b < 8; ++b) vf[b] = cat(trd(a[b]), trd(a[b] + 4096));
#pragma unroll
      for (int b = 0; b < 8; ++b) acc[b] = mfma(vf[b], pf, acc[b]);
      S.set_at<4 * C_FREE>(S.base + 4 * pos, n / NH * 4 + 4);
    };
    auto fold = [&]() {
      const v2f ml = ldl<v2f>(mla);
      const float mx = fmx(m, ml.x), al = ex2((m - mx) * LOG2E), be = ex2((ml.x - mx) * LOG2E);
      l = l * al + be * ml.y;
      m = mx;
#pragma unroll
      for (int b2 = 0; b2 < 4; ++b2) {
        const v4f o = ldl<v4f>(ma + b2 * 1024) * be;
        acc[2 * b2] = acc[2 * b2] * al + v4f{o.x, o.y, o.x, o.y};
        acc[2 * b2 + 1] = acc[2 * b2 + 1] * al + v4f{o.z, o.w, o.z, o.w};
      }
    };
    auto rescale = [&](float mx) {
      mx = fmx(m, max32(max16(mx)));
      if (__builtin_amdgcn_ballot_w64(mx > m) != 0ull) {
        const float al = ex2((m - mx) * LOG2E);
        l *= al;
#pragma unroll
        for (int b = 0; b < 8; ++b) acc[b] *= al;
        m = mx;
      }
    };
    const int order = (w - (int)NT + 1) & 3;
#pragma unroll 1
    for (u32 it = w; it < NT - 2; it += 4) {
      float sv[8];
      rescale(raw(nkv + 2 * it, sv));
      values(nkv + 2 * it + (it == NT - 3 ? 3 : 1), probs(sv));
    }
    const bool hi = lanes(M_HI8);
    if (order < 3) {
      if (order > 0) { S.wait<C_M>(order); fold(); }
#pragma unroll
      for (int b2 = 0; b2 < 4; ++b2) {
        const v4f a = acc[2 * b2], b = acc[2 * b2 + 1];
        stl<v4f>(ma + b2 * 1024, v4f{sel(hi, a.z, a.x), sel(hi, a.w, a.y), sel(hi, b.z, b.x), sel(hi, b.w, b.y)});
      }
      stl<v2f>(mla, v2f{m, l});
      S.set<C_M>(order + 1);
    } else {
      const u32 n = ntail - 5;
      float sa[8], sb[8];
      const float mxa = raw(n, sa), mxb = raw(n + 1, sb);
      rescale(fmx(mxa, mxb));
      const v4u p0 = probs(sa), p1 = probs(sb);
      const u32 o = opq(W_P + ((16 * grp + 2 * (j & 7)) * 64 + s) * 512 + g * 32 + (j & 8) * 2), o2 = opq(o + 64 * 512);
      const u32 om = opq(lane < 8 ? W_ML + (64 * grp + s) * 128 + lane * 16 : OOB);
      STAMP(23);
      values(n + 3, p0);
      S.wait<C_M>(3);
      STAMP(24);
      const v2f ml = ldl<v2f>(mla);
      const float mx = fmx(m, ml.x), al = ex2((m - mx) * LOG2E), be = ex2((ml.x - mx) * LOG2E);
      gs16(c.ws, om, v4u{asu(mx), c.tag, asu(sum32(sum16(l * al + be * ml.y))), c.tag});
      v4f y[4];
#pragma unroll
      for (int b2 = 0; b2 < 4; ++b2) { y[b2] = ldl<v4f>(ma + b2 * 1024) * be; asm volatile("" : "+v"(y[b2])); }
      STAMP(25);
      values(n + 4, p1);
      STAMP(5);
#pragma unroll
      for (int b = 0; b < 8; ++b)
        gs16(c.ws, (b < 4 ? o : o2) + (b & 3) * 128,
             v4u{asu(sel(hi, acc[b].z, acc[b].x) * al + y[b >> 1][2 * (b & 1)]), c.tag, asu(sel(hi, acc[b].w, acc[b].y) * al + y[b >> 1][2 * (b & 1) + 1]), c.tag});
      STAMP(6);
    }
  } else {
    __builtin_amdgcn_s_barrier();
    if (role == 0) {
      S.wait<C_FULL>(26);
      take_wo();
    }
    S.wait<C_Q>(4);
    c.tag = rfl(*(volatile LAS u32*)(S.base + 4 * C_TAG));
    if (role == 0) {
      if (w == 4) {
      v4u q[2];
      const u32 xp = *(const GAS u32*)(p.x + 16 * r + 2 * (lane & 7));
      gather16<2, 8>(S, c.ws, W_MOD2 + (64 * ((lane >> 3) & 3) + 32 * (lane >> 5) + (r >> 2)) * 512 + ((r & 3) * 16 + 2 * (lane & 7)) * 8,
                     [](int i) { return (u32)i * 16 * 512; }, q, c.tag);
      float y[4] = {asf(q[0].x), asf(q[0].z), asf(q[1].x), asf(q[1].z)}, o[4];
#pragma unroll
      for (int i = 0; i < 4; ++i) { y[i] += dpp<ROR8>(y[i]); y[i] = rbf(sum16(y[i])); o[i] = sw32(y[i], y[i]); }
      stl<v4f>(L_HS + (lane & 7) * 16, v4f{y[0], o[2], lo16(xp), 0.f});
      stl<v4f>(L_HS + 128 + (lane & 7) * 16, v4f{y[1], o[3], hi16(xp), 0.f});
      gs16(c.ws, lane < 8 ? W_CS + r * 128 + lane * 16 : OOB, v4u{pk(1.f + o[0], y[2]), c.tag, pk(1.f + o[1], y[3]), c.tag});
      }
    } else if (role == 1 && w == 4) {
      v4u q[1];
      gather16<1, 32>(S, c.ws, W_CS + (128 * (wg & 7) + 2 * lane) * 8, [](int) { return 0u; }, q, c.tag);
      stl<v2u>(L_CS + lane * 8, v2u{q[0].x, q[0].z});
      S.set<C_CS>(1);
    }
  }

  if (role == 0) {
    const Masks M = mk_masks();
    const u32 pa = L_PRIV + w * 512;
    const u32 xa = opq(pa + (lane >> 4) * 8), oo = opq(W_A + (tile_of(lane, v) * 64 + r) * 128 + (lane >> 4) * 32 + (lane & 8) * 2);
    const u32 ra = opq(L_QS + lane * 4), wr = opq(pa + lane * 2);
    S.wait<C_FULL>(ntail);
    if (w == 4) { STAMP(7); }
    combine_part(p, c, S);
    if (w < 4) return;
    const float o = sum8(ra);
    stl<u16>(wr, (u16)pk(o, o));
    CBAR();
    const v4u x0 = xfrag(xa, 0), x1 = xfrag(xa, 1);
    gs16(c.ws, oo, tiles8(wa, x0, x1, M, c.tag));
    gs16(c.ws, oo + 4 * 64 * 128, tiles8(wb, x0, x1, M, c.tag));
    if (v == 0) {
      STAMP(9);
      const v4f kp = ldl<v4f>(L_HS + (lane >> 5) * 128 + (lane & 7) * 16);
      const u32 ox = opq(lane < 10 ? W_X + r * 128 + lane * 8 : OOB);
      const u32 ia = opq(W_A + (r * 64 + (lane >> 3)) * 128 + (lane & 7) * 16);
      nap<NAP2>();
      v4u q[8];
      gather16<8, 1>(S, c.ws, ia, [](int i) { return (u32)i * 1024; }, q, c.tag);
      STAMP(10);
      const float a = rbf(kp.z + rbf(kp.x * rbf(col_total(q))));
      float a0 = a;
      const float a1 = sw32(a0, a0);
      const float mu = sum_lo3(a0 + a1) * (1.f / 16.f);
      const float m2 = sum_lo3((a0 - mu) * (a0 - mu) + (a1 - mu) * (a1 - mu));
      gs8(c.ws, ox, v2u{asu(sel(lanes(0xff), asf(pk(a0, a1)), sel(lanes(0x100), mu, m2))), c.tag});
      STAMP(11);
      const u32 id = opq(W_D + (r * 56 + (lane >> 3)) * 128 + (lane & 7) * 16);
      const u32 oy = opq(lane < 8 ? 32 * r + 4 * lane : OOB);
      nap<NAP5>();
      v4u q7[7];
      gather16<7, 2>(S, c.ws, id, [](int i) { return (u32)i * 1024; }, q7, c.tag);
      STAMP(15);
      float y0 = a + rbf(kp.y * rbf(col_total(q7)));
      const float y1 = sw32(y0, y0);
      __builtin_amdgcn_raw_buffer_store_b32(pk(y0, y1), mk(p.out, 2048), oy, 0, 0);
      __builtin_amdgcn_raw_buffer_store_b32(c.tag, c.ws, (r | lane) ? OOB : W_EPOCH, 0, 16);
      STAMP(16);
    }
  } else if (role == 1) {
    stage_B(p, c, S, lam >= 7, wg & 7, lam % 7, ntail);
  } else if (role == 2) {
    stage_D(p, c, S, tt, ntail);
  }
}

extern "C" void kernel_launch(void* const* d_in, const int* in_sizes, int n_in, void* d_out, int out_size, void* d_ws,
                              size_t ws_size, hipStream_t stream) {
  Params p;
  p.x = (const u16*)d_in[0]; p.K = (const u16*)d_in[1]; p.V = (const u16*)d_in[2];
  p.wq = (const u16*)d_in[3]; p.wo = (const u16*)d_in[4];
  p.wg = (const u16*)d_in[6]; p.wu = (const u16*)d_in[7]; p.wd = (const u16*)d_in[8];
  p.c = (const u16*)d_in[9]; p.wmod = (const u16*)d_in[10];
  p.out = (u16*)d_out; p.ws = (char*)d_ws;
  (void)hipFuncSetAttribute((const void*)fused_block, hipFuncAttributeMaxDynamicSharedMemorySize, 163840);
  hipLaunchKernelGGL(fused_block, dim3(256), dim3(512), 163840, stream, p);
}
```

```cpp
#include <hip/hip_runtime.h>
#include <stdint.h>
#define STAMPS 1
#define YK 5

typedef unsigned short u16;
typedef unsigned u32;
typedef unsigned long long u64;
typedef u32 v4u __attribute__((ext_vector_type(4)));
typedef u32 v2u __attribute__((ext_vector_type(2)));
typedef float v4f __attribute__((ext_vector_type(4)));
typedef float v2f __attribute__((ext_vector_type(2)));
typedef __bf16 v8bf __attribute__((ext_vector_type(8)));
typedef __bf16 v2bf __attribute__((ext_vector_type(2)));
typedef short v4s __attribute__((ext_vector_type(4)));
#define LAS __attribute__((address_space(3)))
#define GAS __attribute__((address_space(1)))
#define DI __device__ __forceinline__
typedef __amdgpu_buffer_rsrc_t rsrc_t;

#define ROT 37
#define NAP0A 10
#define NAP0B 10
#define NAP1 8
#define NAP2 10
#define NAP5 64
#define PACE0 4
#define PUBNAP 0
#define PACE 0
#define DEPTH 24
#define DEPTH2 16

constexpr u32 W_EPOCH = 0;
constexpr u32 W_MOD1 = 4096;
constexpr u32 W_Q = W_MOD1 + 256 * 512;
constexpr u32 W_MOD2 = W_Q + 256 * 512;
constexpr u32 W_CS = W_MOD2 + 256 * 512;
constexpr u32 W_X = W_CS + 64 * 128;
constexpr u32 W_ML = W_X + 64 * 128;
constexpr u32 W_GU = W_ML + 32 * 64 * 16;
constexpr u32 W_A = W_GU + 56 * 16 * 512;
constexpr u32 W_D = W_A + 64 * 64 * 128;
constexpr u32 W_P = W_D + 64 * 64 * 128;
constexpr u32 W_END = W_P + 64 * 64 * 512;
constexpr u32 W_STAMP = W_END;

constexpr u32 NH = 18;
constexpr u32 L_CTRL = NH * 8192;
constexpr u32 L_MRG = L_CTRL + 256;
constexpr u32 L_QS = L_MRG + 4608;
constexpr u32 L_HS = L_QS + 2048;
constexpr u32 L_CS = L_HS + 512;
constexpr u32 L_PRIV = L_CS + 4096;
constexpr u32 L_END = L_PRIV + 4096;
static_assert(L_END <= 163840, "LDS");
constexpr u32 C_FULL = 0, C_H = 2, C_Q = 3, C_M = 4, C_CS = 5, C_TAG = 6, C_W = 7, C_FREE = 16;
constexpr float LOG2E = 1.4426950408889634f;

struct Params {
  const u16 *x, *K, *V, *wq, *wo, *wg, *wu, *wd, *c, *wmod;
  u16* out;
  char* ws;
};

template <class T> DI T ldl(u32 a) { return *(LAS T*)a; }
template <class T> DI void stl(u32 a, T v) { *(LAS T*)a = v; }
DI u32 rfl(u32 v) { return __builtin_amdgcn_readfirstlane(v); }
DI float asf(u32 v) { return __builtin_bit_cast(float, v); }
DI u32 asu(float v) { return __builtin_bit_cast(u32, v); }
DI float lo16(u32 v) { return asf(v << 16); }
DI float hi16(u32 v) { return asf(v & 0xffff0000u); }
DI u32 pk(float a, float b) { v2f t = {a, b}; return __builtin_bit_cast(u32, __builtin_convertvector(t, v2bf)); }
DI float rbf(float a) { return hi16(pk(a, a)); }
#define CBAR() asm volatile("" ::: "memory")
#define LGKM0() asm volatile("s_waitcnt lgkmcnt(0)" ::: "memory")
DI v4f mfma(v4u a, v4u b, v4f c) {
  return __builtin_amdgcn_mfma_f32_16x16x32_bf16(__builtin_bit_cast(v8bf, a), __builtin_bit_cast(v8bf, b), c, 0, 0, 0);
}
DI v2u trd(u32 addr) { return __builtin_bit_cast(v2u, __builtin_amdgcn_ds_read_tr16_b64_v4i16((LAS v4s*)addr)); }
DI v4u cat(v2u a, v2u b) { return v4u{a.x, a.y, b.x, b.y}; }
template <int CTRL> DI float dpp(float x) { return asf(__builtin_amdgcn_update_dpp(0, asu(x), CTRL, 0xf, 0xf, true)); }
constexpr int XOR1 = 0xB1, XOR2 = 0x4E, HMIR = 0x141, ROR8 = 0x128;
DI float fmx(float a, float b) { return __builtin_amdgcn_fmed3f(a, b, INFINITY); }
DI float sw16(float& a, float b) { auto s = __builtin_amdgcn_permlane16_swap(asu(a), asu(b), false, false); a = asf(s[0]); return asf(s[1]); }
DI float sw32(float& a, float b) { auto s = __builtin_amdgcn_permlane32_swap(asu(a), asu(b), false, false); a = asf(s[0]); return asf(s[1]); }
DI float sum16(float x) { const float o = sw16(x, x); return x + o; }
DI float sum32(float x) { const float o = sw32(x, x); return x + o; }
DI float max16(float x) { const float o = sw16(x, x); return fmx(x, o); }
DI float max32(float x) { const float o = sw32(x, x); return fmx(x, o); }
DI float sum_lo3(float x) { x += dpp<XOR1>(x); x += dpp<XOR2>(x); return x + dpp<HMIR>(x); }
DI float sum64(float x) { x = sum_lo3(x); x += dpp<ROR8>(x); return sum32(sum16(x)); }
DI float max64(float x) {
  x = fmx(x, dpp<XOR1>(x)); x = fmx(x, dpp<XOR2>(x)); x = fmx(x, dpp<HMIR>(x)); x = fmx(x, dpp<ROR8>(x));
  return max32(max16(x));
}
DI float sel(bool c, float a, float b) { asm("" : "+v"(a)); asm("" : "+v"(b)); return c ? a : b; }
DI float ex2(float x) { return __builtin_amdgcn_exp2f(x); }
DI float rcp(float x) { return __builtin_amdgcn_rcpf(x); }
DI u32 opq(u32 v) { asm volatile("" : "+v"(v)); return v; }
DI u64 opqs(u64 v) { asm volatile("" : "+s"(v)); return v; }
DI bool lanes(u64 m) { return __builtin_amdgcn_inverse_ballot_w64(m); }
constexpr u64 M_HI8 = 0xff00ff00ff00ff00ull, M_HI32 = 0xffffffff00000000ull;
constexpr u32 OOB = 0x80000000u;

DI u64 now() { return __builtin_amdgcn_s_memrealtime(); }
#define STAMPV(i, val) do { if ((threadIdx.x & 63) == 0) *(GAS u64*)(p.ws + W_STAMP + ((size_t)blockIdx.x * 32 + (i)) * 8) = (val); } while (0)
#define STAMP(i) STAMPV(i, now())

struct Sync {
  u32 base;
  template <u32 OFF> DI void wait_at(u32 addr, u32 v) {
    u32 t, s, n;
    asm volatile("s_movk_i32 %2, 0x7fff\n1:\n\tds_read_b32 %0, %3 offset:%c5\n\ts_waitcnt lgkmcnt(0)\n\tv_readfirstlane_b32 %1, %0\n\t"
                 "s_cmp_ge_u32 %1, %4\n\ts_cbranch_scc1 2f\n\ts_sleep 1\n\ts_sub_u32 %2, %2, 1\n\ts_cbranch_scc0 1b\n2:"
                 : "=&v"(t), "=&s"(s), "=&s"(n) : "v"(addr), "s"(v), "i"(OFF) : "memory", "scc");
  }
  template <u32 W> DI void wait(u32 v) { wait_at<4 * W>(base, v); }
  template <u32 W, int N> DI void add1() {
    u64 e;
    asm volatile("s_mov_b64 %0, exec\n\ts_mov_b64 exec, %4\n\tds_add_u32 %1, %2 offset:%c3\n\ts_mov_b64 exec, %0"
                 : "=&s"(e) : "v"(base + (N > 1 ? 4 * (threadIdx.x & 63) : 0)), "v"(1u), "i"(4 * W), "s"((u64)((1ull << N) - 1)) : "memory");
  }
  template <u32 OFF> DI void set_at(u32 addr, u32 v) {
    asm volatile("ds_write_b32 %0, %1 offset:%c2" :: "v"(addr), "v"(v), "i"(OFF) : "memory");
  }
  template <u32 W> DI void set(u32 v) { set_at<4 * W>(base, v); }
};
DI v4u gl16(rsrc_t r, u32 off, u32 so = 0) { return __builtin_amdgcn_raw_buffer_load_b128(r, off, so, 16); }
DI v2u gl8(rsrc_t r, u32 off) { return __builtin_amdgcn_raw_buffer_load_b64(r, off, 0, 16); }
DI void gs16(rsrc_t r, u32 off, v4u v) { __builtin_amdgcn_raw_buffer_store_b128(v, r, off, 0, 16); }
DI void gs8(rsrc_t r, u32 off, v2u v) { __builtin_amdgcn_raw_buffer_store_b64(v, r, off, 0, 16); }
template <int K> DI void nap() { if constexpr (K > 0) __builtin_amdgcn_s_sleep(K); }
DI bool all_ok(u32 bad) { return __builtin_amdgcn_ballot_w64(bad != 0u) == 0ull; }
template <int N, int NAP, class F> DI void gather16(Sync& S, rsrc_t r, u32 base, F f, v4u (&v)[N], u32 tag) {
  for (u32 n = 0;;) {
    CBAR();
#pragma unroll
    for (int i = 0; i < N; ++i) v[i] = gl16(r, base + (f(i) & 4095u), f(i) & ~4095u);
    u32 bad = 0;
#pragma unroll
    for (int i = 0; i < N; ++i) bad |= (v[i].y ^ tag) | (v[i].w ^ tag);
    if (all_ok(bad) || ++n > 8192u) break;
    nap<NAP>();
  }
  CBAR();
}

template <int D> DI void dma4(u32 ld, u32 v0, u32 v1, u32 v2, u32 v3, rsrc_t r, u32 so) {
  asm volatile("s_mov_b32 m0, %0\n\ts_nop 0\n\t"
               "buffer_load_dwordx4 %1, %5, %6 offen nt lds\n\ts_waitcnt vmcnt(%c7)\n\t"
               "buffer_load_dwordx4 %2, %5, %6 offen offset:1024 nt lds\n\ts_waitcnt vmcnt(%c7)\n\t"
               "buffer_load_dwordx4 %3, %5, %6 offen offset:2048 nt lds\n\ts_waitcnt vmcnt(%c7)\n\t"
               "buffer_load_dwordx4 %4, %5, %6 offen offset:3072 nt lds\n\ts_waitcnt vmcnt(%c7)"
               :: "s"(ld), "v"(v0), "v"(v1), "v"(v2), "v"(v3), "s"(r), "s"(so), "i"(D) : "memory");
}
template <int D> DI void dma1(u32 ld, u32 v, rsrc_t r, u32 so) {
  asm volatile("s_mov_b32 m0, %0\n\ts_nop 0\n\tbuffer_load_dwordx4 %1, %2, %3 offen nt lds\n\ts_waitcnt vmcnt(%c4)"
               :: "s"(ld), "v"(v), "s"(r), "s"(so), "i"(D) : "memory");
}
template <int D> DI void vmwait() { asm volatile("s_waitcnt vmcnt(%c0)" :: "i"(D) : "memory"); }
struct Ring {
  Sync S;
  u32 n, pos, need, full, next, lim;
  u64 stall, first;
  DI void publish(u32 f) { if ((int)(f - full) > 0) { full = f; nap<PUBNAP>(); S.set<C_FULL>(f); } }
  DI u32 open() {
    if (n >= NH) {
      const u32 fa = S.base + 4 * pos;
      if (rfl(*(volatile LAS u32*)(fa + 4 * C_FREE)) < need) {
        const u64 t0 = now(); if (!first) first = t0;
        vmwait<0>();
        publish(n);
        S.wait_at<4 * C_FREE>(fa, need);
        stall += now() - t0;
      }
    }
    return pos * 8192u;
  }
  template <int D> DI void close() {
    ++n; ++pos;
    if (pos == NH) { pos = 0; need += 4; }
    publish(n - D / 8);
  }
};
DI rsrc_t mk(const void* p, u32 bytes) { return __builtin_amdgcn_make_buffer_rsrc((void*)p, 0, bytes, 0x00020000); }
template <int D> DI void ld_slab(Ring& R, rsrc_t r, u32 base, u32 pitch, int nh, int lane) {
  const u32 row = lane >> 3, v = row * pitch + (((lane & 7) ^ (((row >> 1) & 3) << 1)) << 4);
#pragma unroll 1
  for (int h = 0; h < nh; ++h) {
    const u32 ld = R.open();
#pragma unroll
    for (int i = 0; i < 8; ++i) { dma1<D>(ld + i * 1024, v, r, base); base += 8 * pitch; }
    R.close<D>();
  }
}
template <int D, u32 PITCH, int SH> DI void ld_rows(Ring& R, rsrc_t r, u32 base, int lane) {
  u32 v[8];
#pragma unroll
  for (int i = 0; i < 8; ++i) v[i] = ((lane ^ (i << 1)) << 4) + i * PITCH - (i & 3) * 1024;
#pragma unroll 1
  for (int h = 0; h < 16; ++h) {
    const u32 ld = R.open(), so = base + (h >> SH) * 8 * PITCH + (h & ((1 << SH) - 1)) * 1024;
    dma4<D>(ld, v[0], v[1], v[2], v[3], r, so);
    dma4<D>(ld + 4096, v[4], v[5], v[6], v[7], r, so);
    R.close<D>();
  }
}

DI v4u xfrag(u32 addr, int k) { return cat(ldl<v2u>(addr + k * 64), ldl<v2u>(addr + k * 64 + 32)); }
template <int NK> DI v4f gemv_cols(u32 n0, u32 xaddr, int c, int lane) {
  const u32 g = lane >> 4, r16 = 4 * g + ((lane & 15) >> 2);
  const u32 a = opq(n0 * 8192 + r16 * 128 + ((c ^ ((r16 >> 1) & 3)) << 5) + (lane & 3) * 8), xa = opq(xaddr + g * 8);
  v4f acc = {0.f, 0.f, 0.f, 0.f};
#pragma unroll
  for (int k = 0; k < NK; ++k) acc = mfma(cat(trd(a + k * 4096), trd(a + k * 4096 + 2048)), xfrag(xa, k), acc);
  return acc;
}
DI void put_tile(rsrc_t ws, u32 off, v4f acc, u32 tag, int lane) {
  const bool hi = lanes(M_HI8);
  gs16(ws, (lane & 7) ? OOB : off + (lane >> 4) * 32 + (lane & 8) * 2, v4u{asu(sel(hi, acc.z, acc.x)), tag, asu(sel(hi, acc.w, acc.y)), tag});
}
DI u32 hs_addr(u32 n) { return (n % NH) * 8192u; }
template <int SH> DI void read_rows(u32 n0, int v, int lane, v4u (&wf)[16]) {
  const u32 g = lane >> 4, r8 = (4 * g + ((lane & 15) >> 2)) & 7;
  const u32 lo = r8 * 1024 + ((v ^ r8) << 5) + (lane & 3) * 8;
#pragma unroll
  for (int q = 0; q < 4; ++q) {
    const int half = SH ? q >> 1 : 0, k = SH ? q & 1 : q;
    u32 a[2];
#pragma unroll
    for (int t = 0; t < 2; ++t) {
      const u32 h0 = hs_addr(n0 + ((4 * k + 2 * t) << SH) + half), h1 = hs_addr(n0 + ((4 * k + 2 * t + 1) << SH) + half);
      a[t] = lo + (lanes(M_HI32) ? h1 : h0);
    }
#pragma unroll
    for (int u = 0; u < 4; ++u) wf[SH ? (half * 4 + u) * 2 + k : 4 * k + u] = cat(trd(a[0] + u * 256), trd(a[1] + u * 256));
  }
}
struct Masks { u64 m[8]; };
DI Masks mk_masks() {
  Masks M;
#pragma unroll
  for (int i = 0; i < 8; ++i) M.m[i] = opqs(0x0101010101010101ull << i);
  return M;
}
DI v4u tiles8(const v4u (&wf)[16], v4u x0, v4u x1, const Masks& M, u32 tag) {
  v4f acc[8];
#pragma unroll
  for (int i = 0; i < 8; ++i) acc[i] = mfma(wf[2 * i], x0, v4f{0.f, 0.f, 0.f, 0.f});
#pragma unroll
  for (int i = 0; i < 8; ++i) acc[i] = mfma(wf[2 * i + 1], x1, acc[i]);
  const bool hi = lanes(M_HI8);
  float o0 = sel(hi, acc[0].z, acc[0].x), o1 = sel(hi, acc[0].w, acc[0].y);
#pragma unroll
  for (int i = 1; i < 8; ++i) {
    o0 = sel(lanes(M.m[i]), sel(hi, acc[i].z, acc[i].x), o0);
    o1 = sel(lanes(M.m[i]), sel(hi, acc[i].w, acc[i].y), o1);
  }
  return v4u{asu(o0), tag, asu(o1), tag};
}
DI u32 tile_of(int lane, int v) { return 32 * ((lane >> 2) & 1) + 8 * (lane & 3) + v; }

struct Ctx { rsrc_t ws; u32 tag; int lane, w, wg, grp, s; };

struct XIn { v4u xa, xb; u32 xp; };
DI XIn load_x(const Params& p, const Ctx& c) {
  return XIn{*(const GAS v4u*)(p.x + c.lane * 16), *(const GAS v4u*)(p.x + c.lane * 16 + 8),
             *(const GAS u32*)(p.x + 256 * (c.wg >> 6) + 64 * c.w + 2 * (c.lane & 31))};
}
DI float make_ln(const XIn& X) {
  const v4u xa = X.xa, xb = X.xb;
  float sm = 0.f;
#pragma unroll
  for (int i = 0; i < 4; ++i) sm += (lo16(xa[i]) + hi16(xa[i])) + (lo16(xb[i]) + hi16(xb[i]));
  const float mean = sum64(sm) * (1.f / 1024.f);
  float q = 0.f;
#pragma unroll
  for (int i = 0; i < 4; ++i) {
    const float a = lo16(xa[i]) - mean, b = hi16(xa[i]) - mean, d = lo16(xb[i]) - mean, e = hi16(xb[i]) - mean;
    q += (a * a + b * b) + (d * d + e * e);
  }
  const float var = sum64(q) * (1.f / 1024.f);
  const float mb = rbf(mean), den = rbf(sqrtf(rbf(rbf(var) + 1e-5f)));
  return asf(opq(asu(rbf(rbf((lanes(M_HI32) ? hi16(X.xp) : lo16(X.xp)) - mb) / den))));
}
DI void make_h(const Params& p, const Ctx& c, Sync& S, float ln) {
  const int lane = c.lane, kq = c.wg >> 6;
  const u32 base = opq(W_MOD1 + (32 * (lane >> 5) + 4 * kq + c.w) * 512 + (lane & 31) * 16);
  const u32 ha = opq(L_HS + (64 * c.w + 2 * (lane & 31) + (lane >> 5)) * 2);
  v4u v[8];
  nap<NAP0A>();
  gather16<8, 4>(S, c.ws, base, [](int i) { return (u32)(64 * (i & 3) + 16 * (i >> 2)) * 512; }, v, c.tag);
  if (c.w == 0) { STAMP(17); }
  float s0 = (asf(v[0].x) + asf(v[1].x)) + (asf(v[2].x) + asf(v[3].x)), c0 = (asf(v[4].x) + asf(v[5].x)) + (asf(v[6].x) + asf(v[7].x));
  s0 += sw32(s0, (asf(v[0].z) + asf(v[1].z)) + (asf(v[2].z) + asf(v[3].z)));
  c0 += sw32(c0, (asf(v[4].z) + asf(v[5].z)) + (asf(v[6].z) + asf(v[7].z)));
  const float h = rbf(ln * rbf(1.f + rbf(c0))) + rbf(s0);
  stl<u16>(ha, (u16)pk(h, h));
  S.add1<C_H, 1>();
}
DI void make_q(const Params& p, const Ctx& c, Sync& S) {
  const int lane = c.lane;
  v4u v[8];
  const u32 base = W_Q + (16 * c.grp + 4 * c.w + (lane >> 5)) * 512 + (lane & 31) * 16;
  gather16<8, 4>(S, c.ws, base, [](int i) { return (u32)(64 * (i & 3) + 2 * (i >> 2)) * 512; }, v, c.tag);
#pragma unroll
  for (int i = 0; i < 8; i += 4)
    stl<u32>(L_QS + (128 * c.w + 16 * i + lane) * 4,
             pk((asf(v[i].x) + asf(v[i + 1].x)) + (asf(v[i + 2].x) + asf(v[i + 3].x)), (asf(v[i].z) + asf(v[i + 1].z)) + (asf(v[i + 2].z) + asf(v[i + 3].z))));
  S.add1<C_Q, 1>();
}

DI float combine_part(const Params& p, const Ctx& c, Sync& S) {
  const int lane = c.lane, a = 16 * c.grp + c.s, sl = 8 * c.w + (lane >> 3);
  const u32 base = opq(W_P + (a * 64 + sl) * 512 + (lane & 7) * 16);
  const int dim = 2 * (lane & 7) + 32 * (lane >> 5) + 16 * ((lane >> 4) & 1) + ((lane >> 3) & 1);
  const u32 ra = opq(L_QS + (c.w * 64 + dim) * 4);
  if (c.w == 5) {
    v4u ml[1];
    gather16<1, 2>(S, c.ws, W_ML + (c.grp * 64 + lane) * 128 + (c.s >> 1) * 16, [](int) { return 0u; }, ml, c.tag);
    const float e = ex2((asf(ml[0].x) - max64(asf(ml[0].x))) * LOG2E);
    stl<float>(L_HS + 256 + lane * 4, e * (1.f / sum64(e * asf(ml[0].z))));
    S.set<C_W>(1);
    STAMP(26);
  }
  S.wait<C_W>(1);
  const float wt = ldl<float>(L_HS + 256 + sl * 4);
  nap<NAP1>();
  v4u v[4];
  gather16<4, 1>(S, c.ws, base, [](int i) { return (u32)i * 128; }, v, c.tag);
  STAMP(8);
  float z0 = asf(v[0].x) * wt, z1 = asf(v[0].z) * wt, z2 = asf(v[1].x) * wt, z3 = asf(v[1].z) * wt;
  z0 += sw32(z0, asf(v[2].x) * wt); z1 += sw32(z1, asf(v[2].z) * wt);
  z2 += sw32(z2, asf(v[3].x) * wt); z3 += sw32(z3, asf(v[3].z) * wt);
  z0 += sw16(z0, z2); z1 += sw16(z1, z3);
  const bool up = lanes(M_HI8);
  stl<float>(ra, sel(up, z1, z0) + dpp<ROR8>(sel(up, z0, z1)));
  LGKM0();
  __builtin_amdgcn_s_barrier();
  CBAR();
  return 0.f;
}
DI float sum8(u32 a) {
  float r[8];
#pragma unroll
  for (int i = 0; i < 8; ++i) r[i] = ldl<float>(a + i * 256);
  return ((r[0] + r[1]) + (r[2] + r[3])) + ((r[4] + r[5]) + (r[6] + r[7]));
}

DI void stage_B(const Params& p, const Ctx& c, Sync& S, int m, int kb, int cb, u32 n0) {
  const int lane = c.lane, w = c.w;
  S.wait<C_FULL>(n0 + 16);
  v4u wf[16];
  read_rows<0>(n0, w, lane, wf);
  S.wait<C_CS>(1);
  const v2u cs = ldl<v2u>(L_CS + lane * 8);
  const Masks M = mk_masks();
  const int P = 64 * kb + lane, tile = w + 8 * (lane & 3);
  const u32 pa = opq(L_PRIV + w * 512 + lane * 4), xa = opq(L_PRIV + w * 512 + (lane >> 4) * 8);
  const u32 o0 = opq(W_X + (P >> 3) * 128 + (P & 7) * 8), o1 = opq(W_X + lane * 128 + 64);
  const u32 oo = opq((lane & 4) ? OOB : W_GU + ((8 * cb + (tile >> 2)) * 16 + 2 * kb + m) * 512 + (tile & 3) * 128 + (lane >> 4) * 32 + (lane & 8) * 2);
  LGKM0();
  STAMP(12);
  v2u xg; v4u st;
  for (u32 n = 0;;) {
    CBAR();
    xg = gl8(c.ws, o0);
    st = gl16(c.ws, o1);
    if (all_ok((xg.y ^ c.tag) | (st.y ^ c.tag) | (st.w ^ c.tag)) || ++n > 8192u) break;
    nap<3>();
  }
  CBAR();
  STAMP(13);
  const float mu0 = asf(rfl(st.x));
  float d = asf(st.x) - mu0;
  d += sw32(d, asf(st.z) + 16.f * d * d);
  d = sum_lo3(d); d += dpp<ROR8>(d); d = sum16(d);
  const float e = sw32(d, d) * (1.f / 1024.f);
  d *= 1.f / 64.f;
  const float mb = rbf(mu0 + d), inv = rcp(rbf(__builtin_amdgcn_sqrtf(rbf(rbf(fmx(e - d * d, 0.f)) + 1e-5f))));
  const u32 xm = pk(lo16(xg.x) - mb, hi16(xg.x) - mb), ln = pk(lo16(xm) * inv, hi16(xm) * inv);
  const u32 sc = pk(lo16(ln) * lo16(cs.x), hi16(ln) * lo16(cs.y));
  stl<u32>(pa, pk(lo16(sc) + hi16(cs.x), hi16(sc) + hi16(cs.y)));
  CBAR();
  v4f acc[4];
#pragma unroll
  for (int u = 0; u < 4; ++u) acc[u] = v4f{0.f, 0.f, 0.f, 0.f};
#pragma unroll
  for (int k = 0; k < 4; ++k) {
    const v4u xf = xfrag(xa, k);
#pragma unroll
    for (int u = 0; u < 4; ++u) acc[u] = mfma(wf[k * 4 + u], xf, acc[u]);
  }
  const bool hi = lanes(M_HI8);
  float r0 = sel(hi, acc[0].z, acc[0].x), r1 = sel(hi, acc[0].w, acc[0].y);
#pragma unroll
  for (int u = 1; u < 4; ++u) {
    r0 = sel(lanes(M.m[u]), sel(hi, acc[u].z, acc[u].x), r0);
    r1 = sel(lanes(M.m[u]), sel(hi, acc[u].w, acc[u].y), r1);
  }
  gs16(c.ws, oo, v4u{asu(r0), c.tag, asu(r1), c.tag});
  STAMP(14);
}

DI void stage_D(const Params& p, const Ctx& c, Sync& S, int t, u32 n0) {
  const int lane = c.lane, w = c.w;
  S.wait<C_FULL>(n0 + 16);
  v4u wf[16];
  read_rows<1>(n0, w, lane, wf);
  const Masks M = mk_masks();
  const u32 pa = opq(L_PRIV + w * 512 + lane * 2), xa = opq(L_PRIV + w * 512 + (lane >> 4) * 8);
  const u32 oi = opq(W_GU + (t * 16 + 2 * w) * 512 + lane * 16);
  const u32 wa = opq(L_CS + w * 512 + (lane & 31) * 16 + (lane >> 5) * 4), ra = opq(L_CS + lane * 8);
  const u32 oo = opq(W_D + (tile_of(lane, w) * 56 + t) * 128 + (lane >> 4) * 32 + (lane & 8) * 2);
  LGKM0();
  STAMP(27);
  v4u q[1];
  gather16<1, 3>(S, c.ws, oi, [](int) { return 0u; }, q, c.tag);
  STAMP(28);
  stl<u32>(wa, q[0].x);
  stl<u32>(wa + 8, q[0].z);
  LGKM0();
  __builtin_amdgcn_s_barrier();
  CBAR();
  v2f r[8];
#pragma unroll
  for (int i = 0; i < 8; ++i) r[i] = ldl<v2f>(ra + i * 512);
  const v2f y = ((r[0] + r[1]) + (r[2] + r[3])) + ((r[4] + r[5]) + (r[6] + r[7]));
  const u32 gu = pk(y.x, y.y);
  const float g = lo16(gu);
  const float z = (-2.f * LOG2E * 0.7978845608028654f) * (g + 0.044715f * g * g * g);
  const float h = rbf(g * rcp(1.f + ex2(z))) * hi16(gu);
  stl<u16>(pa, (u16)pk(h, h));
  CBAR();
  gs16(c.ws, oo, tiles8(wf, xfrag(xa, 0), xfrag(xa, 1), M, c.tag));
  STAMP(29);
}

template <int N> DI float col_total(const v4u (&q)[N]) {
  float s0 = asf(q[0].x), s1 = asf(q[0].z);
#pragma unroll
  for (int i = 1; i < N; ++i) { s0 += asf(q[i].x); s1 += asf(q[i].z); }
  s0 += sw32(s0, s1);
  s0 = sum16(s0);
  return s0 + dpp<ROR8>(s0);
}

__global__ void __launch_bounds__(512) fused_block(Params p) {
  const int tid = threadIdx.x, lane = tid & 63, w = rfl(tid >> 6), wg = blockIdx.x;
  const int grp = (wg >> 3) & 3, s = (wg & 7) + ((wg >> 5) << 3);
  const bool isA = s < 16;
  const int lam = (wg >> 3) - 8;
  const int role = isA ? 0 : lam < 14 ? 1 : lam < 21 ? 2 : 3;
  const int tt = (wg & 7) + 8 * (lam % 7), r = 16 * grp + s, v = w & 3;
  const u32 nkv = isA ? 26 : 10, NT = isA ? 26 : 34, ntail = nkv + 2 * NT;
  Sync S{opq(L_CTRL)};
  if (w == 0) { STAMP(0); S.set_at<0>(S.base + 4 * lane, 0u); }
  Ctx c;
  c.ws = mk(p.ws, W_END);
  c.lane = lane; c.w = w; c.wg = wg; c.grp = grp; c.s = s;
  v4u wa[16], wb[16];
  auto take_wo = [&]() {
    read_rows<1>(10, v, lane, wa);
    read_rows<1>(10, v + 4, lane, wb);
    LGKM0();
    S.add1<C_FREE + 10, 8>();
    S.add1<C_FREE, 8>();
  };

  if (w == 7) {
    __builtin_amdgcn_s_setprio(3);
    Ring R{S, 0, 0, 0, 0, 0, ntail, 0, 0};
    const rsrc_t rm = mk(p.wmod, 1024 * 6144 * 2);
    ld_slab<DEPTH>(R, rm, (wg >> 5) * 128 * 12288 + (wg & 31) * 128, 12288, 2, lane);
    __builtin_amdgcn_s_barrier();
    ld_slab<DEPTH>(R, mk(p.wq, 1024 * 4096 * 2), (wg >> 6) * 256 * 8192 + (wg & 63) * 128, 8192, 4, lane);
    ld_slab<DEPTH>(R, rm, (wg >> 6) * 256 * 12288 + 4096 + (wg & 63) * 128, 12288, 4, lane);
    if (isA) ld_rows<DEPTH, 2048, 1>(R, mk(p.wo, 4096 * 1024 * 2), r * 64 * 2048, lane);
    const rsrc_t rk = mk(p.K, 1u << 26), rv = mk(p.V, 1u << 26);
    const u32 rw = lane >> 4, ps = lane & 15;
    u32 vk[4], vv[2];
#pragma unroll
    for (int i = 0; i < 4; ++i) vk[i] = rw * 256 + ((ps ^ (4 * i + rw)) << 4);
#pragma unroll
    for (int i = 0; i < 2; ++i) vv[i] = rw * 256 + ((ps ^ ((4 * i + rw) << 1)) << 4);
    auto tile_off = [&](int tau) { return ((u32)grp << 24) + (((tau < 8 ? tau * 48 + s - 16 : 384 + (tau - 8) * 64 + s) + ROT * grp) & 2047) * 8192; };
    auto ldk = [&](int tau) {
      const u32 so = tile_off(tau), ld = R.open();
      dma4<DEPTH>(ld, vk[0], vk[1], vk[2], vk[3], rk, so);
      dma4<DEPTH>(ld + 4096, vk[0], vk[1], vk[2], vk[3], rk, so + 4096);
      R.close<DEPTH>();
    };
    auto ldv = [&](int tau) {
      const u32 so = tile_off(tau), ld = R.open();
      dma4<DEPTH>(ld, vv[0], vv[1], vv[0], vv[1], rv, so);
      dma4<DEPTH>(ld + 4096, vv[0], vv[1], vv[0], vv[1], rv, so + 4096);
      R.close<DEPTH>();
    };
    int tau = isA ? 8 : 0;
    if (isA) {
      ldk(8); ldv(8);
      vmwait<16>();
      R.publish(26);
      tau = 9;
      take_wo();
    }
#pragma unroll 1
    for (; tau < 31; ++tau) { ldk(tau); ldv(tau); }
#pragma unroll 1
    for (; tau < 34; ++tau) ldk(tau);
#pragma unroll 1
    for (tau = 31; tau < 34; ++tau) ldv(tau);
    STAMP(3);
    if (role == 1) ld_rows<DEPTH2, 7168, 0>(R, mk(lam < 7 ? p.wg : p.wu, 1024 * 3584 * 2), (wg & 7) * 128 * 7168 + (lam % 7) * 1024, lane);
    else if (role == 2) ld_rows<DEPTH2, 2048, 1>(R, mk(p.wd, 3584 * 1024 * 2), tt * 64 * 2048, lane);
    vmwait<32>(); R.publish(R.n - 4);
    vmwait<24>(); R.publish(R.n - 3);
    vmwait<16>(); R.publish(R.n - 2);
    vmwait<8>(); R.publish(R.n - 1);
    vmwait<0>(); R.publish(R.n);
    STAMP(4);
    STAMPV(22, R.first);
    STAMPV(31, R.stall + 1);
    __builtin_amdgcn_s_setprio(0);
    c.tag = rfl(*(volatile LAS u32*)(S.base + 4 * C_TAG));
  } else if (w < 4) {
    const u32 ep = __hip_atomic_load((GAS u32*)(p.ws + W_EPOCH), __ATOMIC_RELAXED, __HIP_MEMORY_SCOPE_AGENT);
    const u32 c1 = *(const GAS u32*)(p.c + (wg >> 5) * 128 + 2 * lane);
    const v2u c2 = *(const GAS v2u*)(p.c + (wg >> 6) * 256 + 4 * lane);
    const XIn X = load_x(p, c);
    const u32 pa = L_PRIV + w * 1024;
    LGKM0();
    __builtin_amdgcn_s_barrier();
    CBAR();
    auto silu2 = [](u32 a) { const float x = lo16(a), y = hi16(a); return pk(x / (1.f + ex2(-LOG2E * x)), y / (1.f + ex2(-LOG2E * y))); };
    stl<u32>(pa + lane * 4, silu2(c1));
    stl<v2u>(pa + 256 + lane * 8, v2u{silu2(c2.x), silu2(c2.y)});
    c.tag = rfl(ep) + 1u;
    S.set<C_TAG>(c.tag);
    S.wait<C_FULL>(2);
    put_tile(c.ws, W_MOD1 + wg * 512 + w * 128, gemv_cols<4>(0, pa, w, lane), c.tag, lane);
    S.add1<C_FREE, 2>();
    if (w == 0) { STAMP(1); }
    make_h(p, c, S, make_ln(X));
    S.wait<C_FULL>(6);
    S.wait<C_H>(4);
    if (w == 0) { STAMP(18); }
    put_tile(c.ws, W_Q + wg * 512 + w * 128, gemv_cols<8>(2, L_HS, w, lane), c.tag, lane);
    S.add1<C_FREE + 2, 4>();
    if (w == 0) { STAMP(19); }
    S.wait<C_FULL>(10);
    put_tile(c.ws, W_MOD2 + wg * 512 + w * 128, gemv_cols<8>(6, pa + 256, w, lane), c.tag, lane);
    S.add1<C_FREE + 6, 4>();
    if (w == 0) { STAMP(20); }
    nap<NAP0B>();
    make_q(p, c, S);
    S.wait<C_Q>(4);
    if (w == 0) { STAMP(2); }
    const u32 g = lane >> 4, j = lane & 15;
    v4u qf[4];
#pragma unroll
    for (int k = 0; k < 4; ++k) qf[k] = ldl<v4u>(L_QS + (j & 7) * 256 + k * 64 + g * 16);
    const u32 kb = j * 256 + ((g ^ j) << 4);
    const u32 r16 = 4 * g + (j >> 2), vb = r16 * 256 + ((r16 & 7) << 5) + (lane & 3) * 8;
    float m = -INFINITY, l = 0.f;
    v4f acc[8];
#pragma unroll
    for (int b = 0; b < 8; ++b) acc[b] = v4f{0.f, 0.f, 0.f, 0.f};
    const u32 ma = opq(L_MRG + lane * 16), mla = opq(L_MRG + 4096 + lane * 8);
    auto raw = [&](u32 n, float (&sv)[8]) {
      const u32 pos = n % NH, ka = pos * 8192;
      S.wait<C_FULL>(n + 1);
      v4u kf[8];
#pragma unroll
      for (int k = 0; k < 4; ++k) { const u32 a = ka + (kb ^ (k << 6)); kf[k] = ldl<v4u>(a); kf[4 + k] = ldl<v4u>(a + 4096); }
      v4f s0 = {0.f, 0.f, 0.f, 0.f}, s1 = s0;
#pragma unroll
      for (int k = 0; k < 4; ++k) { s0 = mfma(kf[k], qf[k], s0); s1 = mfma(kf[4 + k], qf[k], s1); }
      S.set_at<4 * C_FREE>(S.base + 4 * pos, n / NH * 4 + 4);
#pragma unroll
      for (int i = 0; i < 4; ++i) {
        const u32 a = i < 2 ? pk(s0[2 * i], s0[2 * i + 1]) : pk(s1[2 * i - 4], s1[2 * i - 3]);
        const u32 b = pk(lo16(a) * 0.08838834764831845f, hi16(a) * 0.08838834764831845f);
        sv[2 * i] = lo16(b); sv[2 * i + 1] = hi16(b);
      }
      return fmx(fmx(fmx(sv[0], sv[1]), fmx(sv[2], sv[3])), fmx(fmx(sv[4], sv[5]), fmx(sv[6], sv[7])));
    };
    auto probs = [&](const float (&sv)[8]) {
      float e[8];
#pragma unroll
      for (int i = 0; i < 8; ++i) e[i] = ex2((sv[i] - m) * LOG2E);
      l += ((e[0] + e[1]) + (e[2] + e[3])) + ((e[4] + e[5]) + (e[6] + e[7]));
      return v4u{pk(e[0], e[1]), pk(e[2], e[3]), pk(e[4], e[5]), pk(e[6], e[7])};
    };
    auto values = [&](u32 n, v4u pf) {
      const u32 pos = n % NH;
      u32 a[8];
#pragma unroll
      for (int b = 0; b < 8; ++b) a[b] = opq(pos * 8192 + (vb ^ (b << 5)));
      S.wait<C_FULL>(n + 1);
      v4u vf[8];
#pragma unroll
      for (int b = 0; b < 8; ++b) vf[b] = cat(trd(a[b]), trd(a[b] + 4096));
#pragma unroll
      for (int b = 0; b < 8; ++b) acc[b] = mfma(vf[b], pf, acc[b]);
      S.set_at<4 * C_FREE>(S.base + 4 * pos, n / NH * 4 + 4);
    };
    auto fold = [&]() {
      const v2f ml = ldl<v2f>(mla);
      const float mx = fmx(m, ml.x), al = ex2((m - mx) * LOG2E), be = ex2((ml.x - mx) * LOG2E);
      l = l * al + be * ml.y;
      m = mx;
#pragma unroll
      for (int b2 = 0; b2 < 4; ++b2) {
        const v4f o = ldl<v4f>(ma + b2 * 1024) * be;
        acc[2 * b2] = acc[2 * b2] * al + v4f{o.x, o.y, o.x, o.y};
        acc[2 * b2 + 1] = acc[2 * b2 + 1] * al + v4f{o.z, o.w, o.z, o.w};
      }
    };
    auto rescale = [&](float mx) {
      mx = fmx(m, max32(max16(mx)));
      if (__builtin_amdgcn_ballot_w64(mx > m) != 0ull) {
        const float al = ex2((m - mx) * LOG2E);
        l *= al;
#pragma unroll
        for (int b = 0; b < 8; ++b) acc[b] *= al;
        m = mx;
      }
    };
    const int order = (w - (int)NT + 1) & 3;
#pragma unroll 1
    for (u32 it = w; it < NT - 2; it += 4) {
      float sv[8];
      rescale(raw(nkv + 2 * it, sv));
      values(nkv + 2 * it + (it == NT - 3 ? 3 : 1), probs(sv));
    }
    const bool hi = lanes(M_HI8);
    if (order < 3) {
      if (order > 0) { S.wait<C_M>(order); fold(); }
#pragma unroll
      for (int b2 = 0; b2 < 4; ++b2) {
        const v4f a = acc[2 * b2], b = acc[2 * b2 + 1];
        stl<v4f>(ma + b2 * 1024, v4f{sel(hi, a.z, a.x), sel(hi, a.w, a.y), sel(hi, b.z, b.x), sel(hi, b.w, b.y)});
      }
      stl<v2f>(mla, v2f{m, l});
      S.set<C_M>(order + 1);
    } else {
      const u32 n = ntail - 5;
      float sa[8], sb[8];
      const float mxa = raw(n, sa), mxb = raw(n + 1, sb);
      rescale(fmx(mxa, mxb));
      const v4u p0 = probs(sa), p1 = probs(sb);
      const u32 o = opq(W_P + ((16 * grp + 2 * (j & 7)) * 64 + s) * 512 + g * 32 + (j & 8) * 2), o2 = opq(o + 64 * 512);
      const u32 om = opq(lane < 8 ? W_ML + (64 * grp + s) * 128 + lane * 16 : OOB);
      STAMP(23);
      values(n + 3, p0);
      S.wait<C_M>(3);
      STAMP(24);
      const v2f ml = ldl<v2f>(mla);
      const float mx = fmx(m, ml.x), al = ex2((m - mx) * LOG2E), be = ex2((ml.x - mx) * LOG2E);
      gs16(c.ws, om, v4u{asu(mx), c.tag, asu(sum32(sum16(l * al + be * ml.y))), c.tag});
      v4f y[4];
#pragma unroll
      for (int b2 = 0; b2 < 4; ++b2) { y[b2] = ldl<v4f>(ma + b2 * 1024) * be; asm volatile("" : "+v"(y[b2])); }
      STAMP(25);
      values(n + 4, p1);
      STAMP(5);
#pragma unroll
      for (int b = 0; b < 8; ++b)
        gs16(c.ws, (b < 4 ? o : o2) + (b & 3) * 128,
             v4u{asu(sel(hi, acc[b].z, acc[b].x) * al + y[b >> 1][2 * (b & 1)]), c.tag, asu(sel(hi, acc[b].w, acc[b].y) * al + y[b >> 1][2 * (b & 1) + 1]), c.tag});
      STAMP(6);
    }
  } else {
    __builtin_amdgcn_s_barrier();
    if (role == 0) {
      S.wait<C_FULL>(26);
      take_wo();
    }
    S.wait<C_Q>(4);
    c.tag = rfl(*(volatile LAS u32*)(S.base + 4 * C_TAG));
    if (role == 0) {
      if (w == 4) {
      v4u q[2];
      const u32 xp = *(const GAS u32*)(p.x + 16 * r + 2 * (lane & 7));
      gather16<2, 8>(S, c.ws, W_MOD2 + (64 * ((lane >> 3) & 3) + 32 * (lane >> 5) + (r >> 2)) * 512 + ((r & 3) * 16 + 2 * (lane & 7)) * 8,
                     [](int i) { return (u32)i * 16 * 512; }, q, c.tag);
      float y[4] = {asf(q[0].x), asf(q[0].z), asf(q[1].x), asf(q[1].z)}, o[4];
#pragma unroll
      for (int i = 0; i < 4; ++i) { y[i] += dpp<ROR8>(y[i]); y[i] = rbf(sum16(y[i])); o[i] = sw32(y[i], y[i]); }
      stl<v4f>(L_HS + (lane & 7) * 16, v4f{y[0], o[2], lo16(xp), 0.f});
      stl<v4f>(L_HS + 128 + (lane & 7) * 16, v4f{y[1], o[3], hi16(xp), 0.f});
      gs16(c.ws, lane < 8 ? W_CS + r * 128 + lane * 16 : OOB, v4u{pk(1.f + o[0], y[2]), c.tag, pk(1.f + o[1], y[3]), c.tag});
      }
    } else if (role == 1 && w == 4) {
      v4u q[1];
      gather16<1, 32>(S, c.ws, W_CS + (128 * (wg & 7) + 2 * lane) * 8, [](int) { return 0u; }, q, c.tag);
      stl<v2u>(L_CS + lane * 8, v2u{q[0].x, q[0].z});
      S.set<C_CS>(1);
    }
  }

  if (role == 0) {
    const Masks M = mk_masks();
    const u32 pa = L_PRIV + w * 512;
    const u32 xa = opq(pa + (lane >> 4) * 8), oo = opq(W_A + (tile_of(lane, v) * 64 + r) * 128 + (lane >> 4) * 32 + (lane & 8) * 2);
    const u32 ra = opq(L_QS + lane * 4), wr = opq(pa + lane * 2);
    S.wait<C_FULL>(ntail);
    if (w == 4) { STAMP(7); }
    combine_part(p, c, S);
    if (w < 4) return;
    const float o = sum8(ra);
    stl<u16>(wr, (u16)pk(o, o));
    CBAR();
    const v4u x0 = xfrag(xa, 0), x1 = xfrag(xa, 1);
    gs16(c.ws, oo, tiles8(wa, x0, x1, M, c.tag));
    gs16(c.ws, oo + 4 * 64 * 128, tiles8(wb, x0, x1, M, c.tag));
    if (v == 0) {
      STAMP(9);
      const v4f kp = ldl<v4f>(L_HS + (lane >> 5) * 128 + (lane & 7) * 16);
      const u32 ox = opq(lane < 10 ? W_X + r * 128 + lane * 8 : OOB);
      const u32 ia = opq(W_A + (r * 64 + (lane >> 3)) * 128 + (lane & 7) * 16);
      nap<NAP2>();
      v4u q[8];
      gather16<8, 1>(S, c.ws, ia, [](int i) { return (u32)i * 1024; }, q, c.tag);
      STAMP(10);
      const float a = rbf(kp.z + rbf(kp.x * rbf(col_total(q))));
      float a0 = a;
      const float a1 = sw32(a0, a0);
      const float mu = sum_lo3(a0 + a1) * (1.f / 16.f);
      const float m2 = sum_lo3((a0 - mu) * (a0 - mu) + (a1 - mu) * (a1 - mu));
      gs8(c.ws, ox, v2u{asu(sel(lanes(0xff), asf(pk(a0, a1)), sel(lanes(0x100), mu, m2))), c.tag});
      STAMP(11);
      const u32 id = opq(W_D + (r * 56 + (lane >> 3)) * 128 + (lane & 7) * 16);
      const u32 oy = opq(lane < 8 ? 32 * r + 4 * lane : OOB);
      nap<NAP5>();
      v4u q7[7];
      gather16<7, 2>(S, c.ws, id, [](int i) { return (u32)i * 1024; }, q7, c.tag);
      STAMP(15);
      float y0 = a + rbf(kp.y * rbf(col_total(q7)));
      const float y1 = sw32(y0, y0);
      __builtin_amdgcn_raw_buffer_store_b32(pk(y0, y1), mk(p.out, 2048), oy, 0, 0);
      __builtin_amdgcn_raw_buffer_store_b32(c.tag, c.ws, (r | lane) ? OOB : W_EPOCH, 0, 16);
      STAMP(16);
    }
  } else if (role == 1) {
    stage_B(p, c, S, lam >= 7, wg & 7, lam % 7, ntail);
  } else if (role == 2) {
    stage_D(p, c, S, tt, ntail);
  }
}

__global__ void k_pre(Params p) { if (threadIdx.x == 0) *(GAS u64*)(p.ws + W_STAMP + 65536) = now(); }
__global__ void k_post(Params p) {
  const u64 ts = now();
  const int lane = threadIdx.x;
  const GAS u64* s = (const GAS u64*)(p.ws + W_STAMP);
  u64 t0 = ~0ull, te = s[lane * 32 + 16];
  for (int i = 0; i < 4; ++i) { const u64 a = s[(lane + 64 * i) * 32]; t0 = a < t0 ? a : t0; }
  for (int o = 32; o > 0; o >>= 1) { const u64 a = __shfl_xor(t0, o), b = __shfl_xor(te, o); t0 = a < t0 ? a : t0; te = b > te ? b : te; }
  u64 val = YK == 1 ? t0 - s[8192] : YK == 2 ? ts - te : YK == 3 ? te - t0 : 10;
  if (val > 20000) val = 20000;
  while (now() - ts < val * 100) {}
}
extern "C" void kernel_launch(void* const* d_in, const int* in_sizes, int n_in, void* d_out, int out_size, void* d_ws,
                              size_t ws_size, hipStream_t stream) {
  Params p;
  p.x = (const u16*)d_in[0]; p.K = (const u16*)d_in[1]; p.V = (const u16*)d_in[2];
  p.wq = (const u16*)d_in[3]; p.wo = (const u16*)d_in[4];
  p.wg = (const u16*)d_in[6]; p.wu = (const u16*)d_in[7]; p.wd = (const u16*)d_in[8];
  p.c = (const u16*)d_in[9]; p.wmod = (const u16*)d_in[10];
  p.out = (u16*)d_out; p.ws = (char*)d_ws;
  (void)hipFuncSetAttribute((const void*)fused_block, hipFuncAttributeMaxDynamicSharedMemorySize, 163840);
  if (YK != 5) hipLaunchKernelGGL(k_pre, dim3(1), dim3(64), 0, stream, p);
  hipLaunchKernelGGL(fused_block, dim3(256), dim3(512), 163840, stream, p);
  hipLaunchKernelGGL(k_post, dim3(1), dim3(64), 0, stream, p);
}
```

```cpp
#include <hip/hip_runtime.h>
#include <stdint.h>
#define STAMPS 1
#define XK 102

typedef unsigned short u16;
typedef unsigned u32;
typedef unsigned long long u64;
typedef u32 v4u __attribute__((ext_vector_type(4)));
typedef u32 v2u __attribute__((ext_vector_type(2)));
typedef float v4f __attribute__((ext_vector_type(4)));
typedef float v2f __attribute__((ext_vector_type(2)));
typedef __bf16 v8bf __attribute__((ext_vector_type(8)));
typedef __bf16 v2bf __attribute__((ext_vector_type(2)));
typedef short v4s __attribute__((ext_vector_type(4)));
#define LAS __attribute__((address_space(3)))
#define GAS __attribute__((address_space(1)))
#define DI __device__ __forceinline__
typedef __amdgpu_buffer_rsrc_t rsrc_t;

#define ROT 37
#define NAP0A 10
#define NAP0B 10
#define NAP1 8
#define NAP2 10
#define NAP5 64
#define PACE0 4
#define PUBNAP 0
#define PACE 0
#define DEPTH 24
#define DEPTH2 16

constexpr u32 W_EPOCH = 0;
constexpr u32 W_MOD1 = 4096;
constexpr u32 W_Q = W_MOD1 + 256 * 512;
constexpr u32 W_MOD2 = W_Q + 256 * 512;
constexpr u32 W_CS = W_MOD2 + 256 * 512;
constexpr u32 W_X = W_CS + 64 * 128;
constexpr u32 W_ML = W_X + 64 * 128;
constexpr u32 W_GU = W_ML + 32 * 64 * 16;
constexpr u32 W_A = W_GU + 56 * 16 * 512;
constexpr u32 W_D = W_A + 64 * 64 * 128;
constexpr u32 W_P = W_D + 64 * 64 * 128;
constexpr u32 W_END = W_P + 64 * 64 * 512;
constexpr u32 W_STAMP = W_END;

constexpr u32 NH = 18;
constexpr u32 L_CTRL = NH * 8192;
constexpr u32 L_MRG = L_CTRL + 256;
constexpr u32 L_QS = L_MRG + 4608;
constexpr u32 L_HS = L_QS + 2048;
constexpr u32 L_CS = L_HS + 512;
constexpr u32 L_PRIV = L_CS + 4096;
constexpr u32 L_END = L_PRIV + 4096;
static_assert(L_END <= 163840, "LDS");
constexpr u32 C_FULL = 0, C_H = 2, C_Q = 3, C_M = 4, C_CS = 5, C_TAG = 6, C_W = 7, C_FREE = 16;
constexpr float LOG2E = 1.4426950408889634f;

struct Params {
  const u16 *x, *K, *V, *wq, *wo, *wg, *wu, *wd, *c, *wmod;
  u16* out;
  char* ws;
};

template <class T> DI T ldl(u32 a) { return *(LAS T*)a; }
template <class T> DI void stl(u32 a, T v) { *(LAS T*)a = v; }
DI u32 rfl(u32 v) { return __builtin_amdgcn_readfirstlane(v); }
DI float asf(u32 v) { return __builtin_bit_cast(float, v); }
DI u32 asu(float v) { return __builtin_bit_cast(u32, v); }
DI float lo16(u32 v) { return asf(v << 16); }
DI float hi16(u32 v) { return asf(v & 0xffff0000u); }
DI u32 pk(float a, float b) { v2f t = {a, b}; return __builtin_bit_cast(u32, __builtin_convertvector(t, v2bf)); }
DI float rbf(float a) { return hi16(pk(a, a)); }
#define CBAR() asm volatile("" ::: "memory")
#define LGKM0() asm volatile("s_waitcnt lgkmcnt(0)" ::: "memory")
DI v4f mfma(v4u a, v4u b, v4f c) {
  return __builtin_amdgcn_mfma_f32_16x16x32_bf16(__builtin_bit_cast(v8bf, a), __builtin_bit_cast(v8bf, b), c, 0, 0, 0);
}
DI v2u trd(u32 addr) { return __builtin_bit_cast(v2u, __builtin_amdgcn_ds_read_tr16_b64_v4i16((LAS v4s*)addr)); }
DI v4u cat(v2u a, v2u b) { return v4u{a.x, a.y, b.x, b.y}; }
template <int CTRL> DI float dpp(float x) { return asf(__builtin_amdgcn_update_dpp(0, asu(x), CTRL, 0xf, 0xf, true)); }
constexpr int XOR1 = 0xB1, XOR2 = 0x4E, HMIR = 0x141, ROR8 = 0x128;
DI float fmx(float a, float b) { return __builtin_amdgcn_fmed3f(a, b, INFINITY); }
DI float sw16(float& a, float b) { auto s = __builtin_amdgcn_permlane16_swap(asu(a), asu(b), false, false); a = asf(s[0]); return asf(s[1]); }
DI float sw32(float& a, float b) { auto s = __builtin_amdgcn_permlane32_swap(asu(a), asu(b), false, false); a = asf(s[0]); return asf(s[1]); }
DI float sum16(float x) { const float o = sw16(x, x); return x + o; }
DI float sum32(float x) { const float o = sw32(x, x); return x + o; }
DI float max16(float x) { const float o = sw16(x, x); return fmx(x, o); }
DI float max32(float x) { const float o = sw32(x, x); return fmx(x, o); }
DI float sum_lo3(float x) { x += dpp<XOR1>(x); x += dpp<XOR2>(x); return x + dpp<HMIR>(x); }
DI float sum64(float x) { x = sum_lo3(x); x += dpp<ROR8>(x); return sum32(sum16(x)); }
DI float max64(float x) {
  x = fmx(x, dpp<XOR1>(x)); x = fmx(x, dpp<XOR2>(x)); x = fmx(x, dpp<HMIR>(x)); x = fmx(x, dpp<ROR8>(x));
  return max32(max16(x));
}
DI float sel(bool c, float a, float b) { asm("" : "+v"(a)); asm("" : "+v"(b)); return c ? a : b; }
DI float ex2(float x) { return __builtin_amdgcn_exp2f(x); }
DI float rcp(float x) { return __builtin_amdgcn_rcpf(x); }
DI u32 opq(u32 v) { asm volatile("" : "+v"(v)); return v; }
DI u64 opqs(u64 v) { asm volatile("" : "+s"(v)); return v; }
DI bool lanes(u64 m) { return __builtin_amdgcn_inverse_ballot_w64(m); }
constexpr u64 M_HI8 = 0xff00ff00ff00ff00ull, M_HI32 = 0xffffffff00000000ull;
constexpr u32 OOB = 0x80000000u;

DI u64 now() { return __builtin_amdgcn_s_memrealtime(); }
#define STAMPV(i, val) do { if ((threadIdx.x & 63) == 0) __hip_atomic_store((GAS u64*)(p.ws + W_STAMP + ((size_t)blockIdx.x * 32 + (i)) * 8), (u64)(val), __ATOMIC_RELAXED, __HIP_MEMORY_SCOPE_AGENT); } while (0)
#define STAMP(i) STAMPV(i, now())

struct Sync {
  u32 base;
  template <u32 OFF> DI void wait_at(u32 addr, u32 v) {
    u32 t, s, n;
    asm volatile("s_movk_i32 %2, 0x7fff\n1:\n\tds_read_b32 %0, %3 offset:%c5\n\ts_waitcnt lgkmcnt(0)\n\tv_readfirstlane_b32 %1, %0\n\t"
                 "s_cmp_ge_u32 %1, %4\n\ts_cbranch_scc1 2f\n\ts_sleep 1\n\ts_sub_u32 %2, %2, 1\n\ts_cbranch_scc0 1b\n2:"
                 : "=&v"(t), "=&s"(s), "=&s"(n) : "v"(addr), "s"(v), "i"(OFF) : "memory", "scc");
  }
  template <u32 W> DI void wait(u32 v) { wait_at<4 * W>(base, v); }
  template <u32 W, int N> DI void add1() {
    u64 e;
    asm volatile("s_mov_b64 %0, exec\n\ts_mov_b64 exec, %4\n\tds_add_u32 %1, %2 offset:%c3\n\ts_mov_b64 exec, %0"
                 : "=&s"(e) : "v"(base + (N > 1 ? 4 * (threadIdx.x & 63) : 0)), "v"(1u), "i"(4 * W), "s"((u64)((1ull << N) - 1)) : "memory");
  }
  template <u32 OFF> DI void set_at(u32 addr, u32 v) {
    asm volatile("ds_write_b32 %0, %1 offset:%c2" :: "v"(addr), "v"(v), "i"(OFF) : "memory");
  }
  template <u32 W> DI void set(u32 v) { set_at<4 * W>(base, v); }
};
DI v4u gl16(rsrc_t r, u32 off, u32 so = 0) { return __builtin_amdgcn_raw_buffer_load_b128(r, off, so, 16); }
DI v2u gl8(rsrc_t r, u32 off) { return __builtin_amdgcn_raw_buffer_load_b64(r, off, 0, 16); }
DI void gs16(rsrc_t r, u32 off, v4u v) { __builtin_amdgcn_raw_buffer_store_b128(v, r, off, 0, 16); }
DI void gs8(rsrc_t r, u32 off, v2u v) { __builtin_amdgcn_raw_buffer_store_b64(v, r, off, 0, 16); }
template <int K> DI void nap() { if constexpr (K > 0) __builtin_amdgcn_s_sleep(K); }
DI bool all_ok(u32 bad) { return __builtin_amdgcn_ballot_w64(bad != 0u) == 0ull; }
template <int N, int NAP, class F> DI void gather16(Sync& S, rsrc_t r, u32 base, F f, v4u (&v)[N], u32 tag) {
  for (u32 n = 0;;) {
    CBAR();
#pragma unroll
    for (int i = 0; i < N; ++i) v[i] = gl16(r, base + (f(i) & 4095u), f(i) & ~4095u);
    u32 bad = 0;
#pragma unroll
    for (int i = 0; i < N; ++i) bad |= (v[i].y ^ tag) | (v[i].w ^ tag);
    if (all_ok(bad) || ++n > 8192u) break;
    nap<NAP>();
  }
  CBAR();
}

template <int D> DI void dma4(u32 ld, u32 v0, u32 v1, u32 v2, u32 v3, rsrc_t r, u32 so) {
  asm volatile("s_mov_b32 m0, %0\n\ts_nop 0\n\t"
               "buffer_load_dwordx4 %1, %5, %6 offen nt lds\n\ts_waitcnt vmcnt(%c7)\n\t"
               "buffer_load_dwordx4 %2, %5, %6 offen offset:1024 nt lds\n\ts_waitcnt vmcnt(%c7)\n\t"
               "buffer_load_dwordx4 %3, %5, %6 offen offset:2048 nt lds\n\ts_waitcnt vmcnt(%c7)\n\t"
               "buffer_load_dwordx4 %4, %5, %6 offen offset:3072 nt lds\n\ts_waitcnt vmcnt(%c7)"
               :: "s"(ld), "v"(v0), "v"(v1), "v"(v2), "v"(v3), "s"(r), "s"(so), "i"(D) : "memory");
}
template <int D> DI void dma1(u32 ld, u32 v, rsrc_t r, u32 so) {
  asm volatile("s_mov_b32 m0, %0\n\ts_nop 0\n\tbuffer_load_dwordx4 %1, %2, %3 offen nt lds\n\ts_waitcnt vmcnt(%c4)"
               :: "s"(ld), "v"(v), "s"(r), "s"(so), "i"(D) : "memory");
}
template <int D> DI void vmwait() { asm volatile("s_waitcnt vmcnt(%c0)" :: "i"(D) : "memory"); }
struct Ring {
  Sync S;
  u32 n, pos, need, full, next, lim;
  u64 stall, first;
  DI void publish(u32 f) { if ((int)(f - full) > 0) { full = f; nap<PUBNAP>(); S.set<C_FULL>(f); } }
  DI u32 open() {
    if (n >= NH) {
      const u32 fa = S.base + 4 * pos;
      if (rfl(*(volatile LAS u32*)(fa + 4 * C_FREE)) < need) {
        const u64 t0 = now(); if (!first) first = t0;
        vmwait<0>();
        publish(n);
        S.wait_at<4 * C_FREE>(fa, need);
        stall += now() - t0;
      }
    }
    return pos * 8192u;
  }
  template <int D> DI void close() {
    ++n; ++pos;
    if (pos == NH) { pos = 0; need += 4; }
    publish(n - D / 8);
  }
};
DI rsrc_t mk(const void* p, u32 bytes) { return __builtin_amdgcn_make_buffer_rsrc((void*)p, 0, bytes, 0x00020000); }
template <int D> DI void ld_slab(Ring& R, rsrc_t r, u32 base, u32 pitch, int nh, int lane) {
  const u32 row = lane >> 3, v = row * pitch + (((lane & 7) ^ (((row >> 1) & 3) << 1)) << 4);
#pragma unroll 1
  for (int h = 0; h < nh; ++h) {
    const u32 ld = R.open();
#pragma unroll
    for (int i = 0; i < 8; ++i) { dma1<D>(ld + i * 1024, v, r, base); base += 8 * pitch; }
    R.close<D>();
  }
}
template <int D, u32 PITCH, int SH> DI void ld_rows(Ring& R, rsrc_t r, u32 base, int lane) {
  u32 v[8];
#pragma unroll
  for (int i = 0; i < 8; ++i) v[i] = ((lane ^ (i << 1)) << 4) + i * PITCH - (i & 3) * 1024;
#pragma unroll 1
  for (int h = 0; h < 16; ++h) {
    const u32 ld = R.open(), so = base + (h >> SH) * 8 * PITCH + (h & ((1 << SH) - 1)) * 1024;
    dma4<D>(ld, v[0], v[1], v[2], v[3], r, so);
    dma4<D>(ld + 4096, v[4], v[5], v[6], v[7], r, so);
    R.close<D>();
  }
}

DI v4u xfrag(u32 addr, int k) { return cat(ldl<v2u>(addr + k * 64), ldl<v2u>(addr + k * 64 + 32)); }
template <int NK> DI v4f gemv_cols(u32 n0, u32 xaddr, int c, int lane) {
  const u32 g = lane >> 4, r16 = 4 * g + ((lane & 15) >> 2);
  const u32 a = opq(n0 * 8192 + r16 * 128 + ((c ^ ((r16 >> 1) & 3)) << 5) + (lane & 3) * 8), xa = opq(xaddr + g * 8);
  v4f acc = {0.f, 0.f, 0.f, 0.f};
#pragma unroll
  for (int k = 0; k < NK; ++k) acc = mfma(cat(trd(a + k * 4096), trd(a + k * 4096 + 2048)), xfrag(xa, k), acc);
  return acc;
}
DI void put_tile(rsrc_t ws, u32 off, v4f acc, u32 tag, int lane) {
  const bool hi = lanes(M_HI8);
  gs16(ws, (lane & 7) ? OOB : off + (lane >> 4) * 32 + (lane & 8) * 2, v4u{asu(sel(hi, acc.z, acc.x)), tag, asu(sel(hi, acc.w, acc.y)), tag});
}
DI u32 hs_addr(u32 n) { return (n % NH) * 8192u; }
template <int SH> DI void read_rows(u32 n0, int v, int lane, v4u (&wf)[16]) {
  const u32 g = lane >> 4, r8 = (4 * g + ((lane & 15) >> 2)) & 7;
  const u32 lo = r8 * 1024 + ((v ^ r8) << 5) + (lane & 3) * 8;
#pragma unroll
  for (int q = 0; q < 4; ++q) {
    const int half = SH ? q >> 1 : 0, k = SH ? q & 1 : q;
    u32 a[2];
#pragma unroll
    for (int t = 0; t < 2; ++t) {
      const u32 h0 = hs_addr(n0 + ((4 * k + 2 * t) << SH) + half), h1 = hs_addr(n0 + ((4 * k + 2 * t + 1) << SH) + half);
      a[t] = lo + (lanes(M_HI32) ? h1 : h0);
    }
#pragma unroll
    for (int u = 0; u < 4; ++u) wf[SH ? (half * 4 + u) * 2 + k : 4 * k + u] = cat(trd(a[0] + u * 256), trd(a[1] + u * 256));
  }
}
struct Masks { u64 m[8]; };
DI Masks mk_masks() {
  Masks M;
#pragma unroll
  for (int i = 0; i < 8; ++i) M.m[i] = opqs(0x0101010101010101ull << i);
  return M;
}
DI v4u tiles8(const v4u (&wf)[16], v4u x0, v4u x1, const Masks& M, u32 tag) {
  v4f acc[8];
#pragma unroll
  for (int i = 0; i < 8; ++i) acc[i] = mfma(wf[2 * i], x0, v4f{0.f, 0.f, 0.f, 0.f});
#pragma unroll
  for (int i = 0; i < 8; ++i) acc[i] = mfma(wf[2 * i + 1], x1, acc[i]);
  const bool hi = lanes(M_HI8);
  float o0 = sel(hi, acc[0].z, acc[0].x), o1 = sel(hi, acc[0].w, acc[0].y);
#pragma unroll
  for (int i = 1; i < 8; ++i) {
    o0 = sel(lanes(M.m[i]), sel(hi, acc[i].z, acc[i].x), o0);
    o1 = sel(lanes(M.m[i]), sel(hi, acc[i].w, acc[i].y), o1);
  }
  return v4u{asu(o0), tag, asu(o1), tag};
}
DI u32 tile_of(int lane, int v) { return 32 * ((lane >> 2) & 1) + 8 * (lane & 3) + v; }

struct Ctx { rsrc_t ws; u32 tag; int lane, w, wg, grp, s; };

struct XIn { v4u xa, xb; u32 xp; };
DI XIn load_x(const Params& p, const Ctx& c) {
  return XIn{*(const GAS v4u*)(p.x + c.lane * 16), *(const GAS v4u*)(p.x + c.lane * 16 + 8),
             *(const GAS u32*)(p.x + 256 * (c.wg >> 6) + 64 * c.w + 2 * (c.lane & 31))};
}
DI float make_ln(const XIn& X) {
  const v4u xa = X.xa, xb = X.xb;
  float sm = 0.f;
#pragma unroll
  for (int i = 0; i < 4; ++i) sm += (lo16(xa[i]) + hi16(xa[i])) + (lo16(xb[i]) + hi16(xb[i]));
  const float mean = sum64(sm) * (1.f / 1024.f);
  float q = 0.f;
#pragma unroll
  for (int i = 0; i < 4; ++i) {
    const float a = lo16(xa[i]) - mean, b = hi16(xa[i]) - mean, d = lo16(xb[i]) - mean, e = hi16(xb[i]) - mean;
    q += (a * a + b * b) + (d * d + e * e);
  }
  const float var = sum64(q) * (1.f / 1024.f);
  const float mb = rbf(mean), den = rbf(sqrtf(rbf(rbf(var) + 1e-5f)));
  return asf(opq(asu(rbf(rbf((lanes(M_HI32) ? hi16(X.xp) : lo16(X.xp)) - mb) / den))));
}
DI void make_h(const Params& p, const Ctx& c, Sync& S, float ln) {
  const int lane = c.lane, kq = c.wg >> 6;
  const u32 base = opq(W_MOD1 + (32 * (lane >> 5) + 4 * kq + c.w) * 512 + (lane & 31) * 16);
  const u32 ha = opq(L_HS + (64 * c.w + 2 * (lane & 31) + (lane >> 5)) * 2);
  v4u v[8];
  nap<NAP0A>();
  gather16<8, 4>(S, c.ws, base, [](int i) { return (u32)(64 * (i & 3) + 16 * (i >> 2)) * 512; }, v, c.tag);
  if (c.w == 0) { STAMP(17); }
  float s0 = (asf(v[0].x) + asf(v[1].x)) + (asf(v[2].x) + asf(v[3].x)), c0 = (asf(v[4].x) + asf(v[5].x)) + (asf(v[6].x) + asf(v[7].x));
  s0 += sw32(s0, (asf(v[0].z) + asf(v[1].z)) + (asf(v[2].z) + asf(v[3].z)));
  c0 += sw32(c0, (asf(v[4].z) + asf(v[5].z)) + (asf(v[6].z) + asf(v[7].z)));
  const float h = rbf(ln * rbf(1.f + rbf(c0))) + rbf(s0);
  stl<u16>(ha, (u16)pk(h, h));
  S.add1<C_H, 1>();
}
DI void make_q(const Params& p, const Ctx& c, Sync& S) {
  const int lane = c.lane;
  v4u v[8];
  const u32 base = W_Q + (16 * c.grp + 4 * c.w + (lane >> 5)) * 512 + (lane & 31) * 16;
  gather16<8, 4>(S, c.ws, base, [](int i) { return (u32)(64 * (i & 3) + 2 * (i >> 2)) * 512; }, v, c.tag);
#pragma unroll
  for (int i = 0; i < 8; i += 4)
    stl<u32>(L_QS + (128 * c.w + 16 * i + lane) * 4,
             pk((asf(v[i].x) + asf(v[i + 1].x)) + (asf(v[i + 2].x) + asf(v[i + 3].x)), (asf(v[i].z) + asf(v[i + 1].z)) + (asf(v[i + 2].z) + asf(v[i + 3].z))));
  S.add1<C_Q, 1>();
}

DI float combine_part(const Params& p, const Ctx& c, Sync& S) {
  const int lane = c.lane, a = 16 * c.grp + c.s, sl = 8 * c.w + (lane >> 3);
  const u32 base = opq(W_P + (a * 64 + sl) * 512 + (lane & 7) * 16);
  const int dim = 2 * (lane & 7) + 32 * (lane >> 5) + 16 * ((lane >> 4) & 1) + ((lane >> 3) & 1);
  const u32 ra = opq(L_QS + (c.w * 64 + dim) * 4);
  if (c.w == 5) {
    v4u ml[1];
    gather16<1, 2>(S, c.ws, W_ML + (c.grp * 64 + lane) * 128 + (c.s >> 1) * 16, [](int) { return 0u; }, ml, c.tag);
    const float e = ex2((asf(ml[0].x) - max64(asf(ml[0].x))) * LOG2E);
    stl<float>(L_HS + 256 + lane * 4, e * (1.f / sum64(e * asf(ml[0].z))));
    S.set<C_W>(1);
    STAMP(26);
  }
  S.wait<C_W>(1);
  const float wt = ldl<float>(L_HS + 256 + sl * 4);
  nap<NAP1>();
  v4u v[4];
  gather16<4, 1>(S, c.ws, base, [](int i) { return (u32)i * 128; }, v, c.tag);
  STAMP(8);
  float z0 = asf(v[0].x) * wt, z1 = asf(v[0].z) * wt, z2 = asf(v[1].x) * wt, z3 = asf(v[1].z) * wt;
  z0 += sw32(z0, asf(v[2].x) * wt); z1 += sw32(z1, asf(v[2].z) * wt);
  z2 += sw32(z2, asf(v[3].x) * wt); z3 += sw32(z3, asf(v[3].z) * wt);
  z0 += sw16(z0, z2); z1 += sw16(z1, z3);
  const bool up = lanes(M_HI8);
  stl<float>(ra, sel(up, z1, z0) + dpp<ROR8>(sel(up, z0, z1)));
  LGKM0();
  __builtin_amdgcn_s_barrier();
  CBAR();
  return 0.f;
}
DI float sum8(u32 a) {
  float r[8];
#pragma unroll
  for (int i = 0; i < 8; ++i) r[i] = ldl<float>(a + i * 256);
  return ((r[0] + r[1]) + (r[2] + r[3])) + ((r[4] + r[5]) + (r[6] + r[7]));
}

DI void stage_B(const Params& p, const Ctx& c, Sync& S, int m, int kb, int cb, u32 n0) {
  const int lane = c.lane, w = c.w;
  S.wait<C_FULL>(n0 + 16);
  v4u wf[16];
  read_rows<0>(n0, w, lane, wf);
  S.wait<C_CS>(1);
  const v2u cs = ldl<v2u>(L_CS + lane * 8);
  const Masks M = mk_masks();
  const int P = 64 * kb + lane, tile = w + 8 * (lane & 3);
  const u32 pa = opq(L_PRIV + w * 512 + lane * 4), xa = opq(L_PRIV + w * 512 + (lane >> 4) * 8);
  const u32 o0 = opq(W_X + (P >> 3) * 128 + (P & 7) * 8), o1 = opq(W_X + lane * 128 + 64);
  const u32 oo = opq((lane & 4) ? OOB : W_GU + ((8 * cb + (tile >> 2)) * 16 + 2 * kb + m) * 512 + (tile & 3) * 128 + (lane >> 4) * 32 + (lane & 8) * 2);
  LGKM0();
  STAMP(12);
  v2u xg; v4u st;
  for (u32 n = 0;;) {
    CBAR();
    xg = gl8(c.ws, o0);
    st = gl16(c.ws, o1);
    if (all_ok((xg.y ^ c.tag) | (st.y ^ c.tag) | (st.w ^ c.tag)) || ++n > 8192u) break;
    nap<3>();
  }
  CBAR();
  STAMP(13);
  const float mu0 = asf(rfl(st.x));
  float d = asf(st.x) - mu0;
  d += sw32(d, asf(st.z) + 16.f * d * d);
  d = sum_lo3(d); d += dpp<ROR8>(d); d = sum16(d);
  const float e = sw32(d, d) * (1.f / 1024.f);
  d *= 1.f / 64.f;
  const float mb = rbf(mu0 + d), inv = rcp(rbf(__builtin_amdgcn_sqrtf(rbf(rbf(fmx(e - d * d, 0.f)) + 1e-5f))));
  const u32 xm = pk(lo16(xg.x) - mb, hi16(xg.x) - mb), ln = pk(lo16(xm) * inv, hi16(xm) * inv);
  const u32 sc = pk(lo16(ln) * lo16(cs.x), hi16(ln) * lo16(cs.y));
  stl<u32>(pa, pk(lo16(sc) + hi16(cs.x), hi16(sc) + hi16(cs.y)));
  CBAR();
  v4f acc[4];
#pragma unroll
  for (int u = 0; u < 4; ++u) acc[u] = v4f{0.f, 0.f, 0.f, 0.f};
#pragma unroll
  for (int k = 0; k < 4; ++k) {
    const v4u xf = xfrag(xa, k);
#pragma unroll
    for (int u = 0; u < 4; ++u) acc[u] = mfma(wf[k * 4 + u], xf, acc[u]);
  }
  const bool hi = lanes(M_HI8);
  float r0 = sel(hi, acc[0].z, acc[0].x), r1 = sel(hi, acc[0].w, acc[0].y);
#pragma unroll
  for (int u = 1; u < 4; ++u) {
    r0 = sel(lanes(M.m[u]), sel(hi, acc[u].z, acc[u].x), r0);
    r1 = sel(lanes(M.m[u]), sel(hi, acc[u].w, acc[u].y), r1);
  }
  gs16(c.ws, oo, v4u{asu(r0), c.tag, asu(r1), c.tag});
  STAMP(14);
}

DI void stage_D(const Params& p, const Ctx& c, Sync& S, int t, u32 n0) {
  const int lane = c.lane, w = c.w;
  S.wait<C_FULL>(n0 + 16);
  v4u wf[16];
  read_rows<1>(n0, w, lane, wf);
  const Masks M = mk_masks();
  const u32 pa = opq(L_PRIV + w * 512 + lane * 2), xa = opq(L_PRIV + w * 512 + (lane >> 4) * 8);
  const u32 oi = opq(W_GU + (t * 16 + 2 * w) * 512 + lane * 16);
  const u32 wa = opq(L_CS + w * 512 + (lane & 31) * 16 + (lane >> 5) * 4), ra = opq(L_CS + lane * 8);
  const u32 oo = opq(W_D + (tile_of(lane, w) * 56 + t) * 128 + (lane >> 4) * 32 + (lane & 8) * 2);
  LGKM0();
  STAMP(27);
  v4u q[1];
  gather16<1, 3>(S, c.ws, oi, [](int) { return 0u; }, q, c.tag);
  STAMP(28);
  stl<u32>(wa, q[0].x);
  stl<u32>(wa + 8, q[0].z);
  LGKM0();
  __builtin_amdgcn_s_barrier();
  CBAR();
  v2f r[8];
#pragma unroll
  for (int i = 0; i < 8; ++i) r[i] = ldl<v2f>(ra + i * 512);
  const v2f y = ((r[0] + r[1]) + (r[2] + r[3])) + ((r[4] + r[5]) + (r[6] + r[7]));
  const u32 gu = pk(y.x, y.y);
  const float g = lo16(gu);
  const float z = (-2.f * LOG2E * 0.7978845608028654f) * (g + 0.044715f * g * g * g);
  const float h = rbf(g * rcp(1.f + ex2(z))) * hi16(gu);
  stl<u16>(pa, (u16)pk(h, h));
  CBAR();
  gs16(c.ws, oo, tiles8(wf, xfrag(xa, 0), xfrag(xa, 1), M, c.tag));
  STAMP(29);
}

template <int N> DI float col_total(const v4u (&q)[N]) {
  float s0 = asf(q[0].x), s1 = asf(q[0].z);
#pragma unroll
  for (int i = 1; i < N; ++i) { s0 += asf(q[i].x); s1 += asf(q[i].z); }
  s0 += sw32(s0, s1);
  s0 = sum16(s0);
  return s0 + dpp<ROR8>(s0);
}

__global__ void __launch_bounds__(512) fused_block(Params p_) {
  u64 ta, tb, dummy;
  const __attribute__((address_space(4))) Params* ka = (const __attribute__((address_space(4))) Params*)__builtin_amdgcn_kernarg_segment_ptr();
  asm volatile("s_memrealtime %0\n s_waitcnt lgkmcnt(0)\n s_load_dwordx2 %2, %3, 0x58\n s_waitcnt lgkmcnt(0)\n s_memrealtime %1\n s_waitcnt lgkmcnt(0)" : "=&s"(ta), "=&s"(tb), "=&s"(dummy), "+s"(ka));
  Params p; p.x = ka->x; p.K = ka->K; p.V = ka->V; p.wq = ka->wq; p.wo = ka->wo; p.wg = ka->wg; p.wu = ka->wu; p.wd = ka->wd; p.c = ka->c; p.wmod = ka->wmod; p.out = ka->out; p.ws = ka->ws;
  const int tid = threadIdx.x, lane = tid & 63, w = rfl(tid >> 6), wg = blockIdx.x;
  const int grp = (wg >> 3) & 3, s = (wg & 7) + ((wg >> 5) << 3);
  const bool isA = s < 16;
  const int lam = (wg >> 3) - 8;
  const int role = isA ? 0 : lam < 14 ? 1 : lam < 21 ? 2 : 3;
  const int tt = (wg & 7) + 8 * (lam % 7), r = 16 * grp + s, v = w & 3;
  const u32 nkv = isA ? 26 : 10, NT = isA ? 26 : 34, ntail = nkv + 2 * NT;
  const u64 tstart = now(), cstart = __builtin_amdgcn_s_memtime();
  Sync S{opq(L_CTRL)};
  if (w == 0) { STAMPV(0, ta); STAMPV(30, tb); S.set_at<0>(S.base + 4 * lane, 0u); }
  Ctx c;
  c.ws = mk(p.ws, W_END);
  c.lane = lane; c.w = w; c.wg = wg; c.grp = grp; c.s = s;
  v4u wa[16], wb[16];
  auto take_wo = [&]() {
    read_rows<1>(10, v, lane, wa);
    read_rows<1>(10, v + 4, lane, wb);
    LGKM0();
    S.add1<C_FREE + 10, 8>();
    S.add1<C_FREE, 8>();
  };

  if (w == 7) {
    __builtin_amdgcn_s_setprio(3);
    Ring R{S, 0, 0, 0, 0, 0, ntail, 0, 0};
    const rsrc_t rm = mk(p.wmod, 1024 * 6144 * 2);
    ld_slab<DEPTH>(R, rm, (wg >> 5) * 128 * 12288 + (wg & 31) * 128, 12288, 2, lane);
    __builtin_amdgcn_s_barrier();
    ld_slab<DEPTH>(R, mk(p.wq, 1024 * 4096 * 2), (wg >> 6) * 256 * 8192 + (wg & 63) * 128, 8192, 4, lane);
    ld_slab<DEPTH>(R, rm, (wg >> 6) * 256 * 12288 + 4096 + (wg & 63) * 128, 12288, 4, lane);
    if (isA) ld_rows<DEPTH, 2048, 1>(R, mk(p.wo, 4096 * 1024 * 2), r * 64 * 2048, lane);
    const rsrc_t rk = mk(p.K, 1u << 26), rv = mk(p.V, 1u << 26);
    const u32 rw = lane >> 4, ps = lane & 15;
    u32 vk[4], vv[2];
#pragma unroll
    for (int i = 0; i < 4; ++i) vk[i] = rw * 256 + ((ps ^ (4 * i + rw)) << 4);
#pragma unroll
    for (int i = 0; i < 2; ++i) vv[i] = rw * 256 + ((ps ^ ((4 * i + rw) << 1)) << 4);
    auto tile_off = [&](int tau) { return ((u32)grp << 24) + (((tau < 8 ? tau * 48 + s - 16 : 384 + (tau - 8) * 64 + s) + ROT * grp) & 2047) * 8192; };
    auto ldk = [&](int tau) {
      const u32 so = tile_off(tau), ld = R.open();
      dma4<DEPTH>(ld, vk[0], vk[1], vk[2], vk[3], rk, so);
      dma4<DEPTH>(ld + 4096, vk[0], vk[1], vk[2], vk[3], rk, so + 4096);
      R.close<DEPTH>();
    };
    auto ldv = [&](int tau) {
      const u32 so = tile_off(tau), ld = R.open();
      dma4<DEPTH>(ld, vv[0], vv[1], vv[0], vv[1], rv, so);
      dma4<DEPTH>(ld + 4096, vv[0], vv[1], vv[0], vv[1], rv, so + 4096);
      R.close<DEPTH>();
    };
    int tau = isA ? 8 : 0;
    if (isA) {
      ldk(8); ldv(8);
      vmwait<16>();
      R.publish(26);
      tau = 9;
      take_wo();
    }
#pragma unroll 1
    for (; tau < 31; ++tau) { ldk(tau); ldv(tau); }
#pragma unroll 1
    for (; tau < 34; ++tau) ldk(tau);
#pragma unroll 1
    for (tau = 31; tau < 34; ++tau) ldv(tau);
    STAMP(3);
    if (role == 1) ld_rows<DEPTH2, 7168, 0>(R, mk(lam < 7 ? p.wg : p.wu, 1024 * 3584 * 2), (wg & 7) * 128 * 7168 + (lam % 7) * 1024, lane);
    else if (role == 2) ld_rows<DEPTH2, 2048, 1>(R, mk(p.wd, 3584 * 1024 * 2), tt * 64 * 2048, lane);
    vmwait<32>(); R.publish(R.n - 4);
    vmwait<24>(); R.publish(R.n - 3);
    vmwait<16>(); R.publish(R.n - 2);
    vmwait<8>(); R.publish(R.n - 1);
    vmwait<0>(); R.publish(R.n);
    STAMP(4);
    STAMPV(22, R.first);
    STAMPV(31, R.stall + 1);
    __builtin_amdgcn_s_setprio(0);
    c.tag = rfl(*(volatile LAS u32*)(S.base + 4 * C_TAG));
  } else if (w < 4) {
    const u32 ep = __hip_atomic_load((GAS u32*)(p.ws + W_EPOCH), __ATOMIC_RELAXED, __HIP_MEMORY_SCOPE_AGENT);
    const u32 c1 = *(const GAS u32*)(p.c + (wg >> 5) * 128 + 2 * lane);
    const v2u c2 = *(const GAS v2u*)(p.c + (wg >> 6) * 256 + 4 * lane);
    const XIn X = load_x(p, c);
    const u32 pa = L_PRIV + w * 1024;
    LGKM0();
    __builtin_amdgcn_s_barrier();
    CBAR();
    auto silu2 = [](u32 a) { const float x = lo16(a), y = hi16(a); return pk(x / (1.f + ex2(-LOG2E * x)), y / (1.f + ex2(-LOG2E * y))); };
    stl<u32>(pa + lane * 4, silu2(c1));
    stl<v2u>(pa + 256 + lane * 8, v2u{silu2(c2.x), silu2(c2.y)});
    c.tag = rfl(ep) + 1u;
    S.set<C_TAG>(c.tag);
    S.wait<C_FULL>(2);
    put_tile(c.ws, W_MOD1 + wg * 512 + w * 128, gemv_cols<4>(0, pa, w, lane), c.tag, lane);
    S.add1<C_FREE, 2>();
    if (w == 0) { STAMP(1); }
    make_h(p, c, S, make_ln(X));
    S.wait<C_FULL>(6);
    S.wait<C_H>(4);
    if (w == 0) { STAMP(18); }
    put_tile(c.ws, W_Q + wg * 512 + w * 128, gemv_cols<8>(2, L_HS, w, lane), c.tag, lane);
    S.add1<C_FREE + 2, 4>();
    if (w == 0) { STAMP(19); }
    S.wait<C_FULL>(10);
    put_tile(c.ws, W_MOD2 + wg * 512 + w * 128, gemv_cols<8>(6, pa + 256, w, lane), c.tag, lane);
    S.add1<C_FREE + 6, 4>();
    if (w == 0) { STAMP(20); }
    nap<NAP0B>();
    make_q(p, c, S);
    S.wait<C_Q>(4);
    if (w == 0) { STAMP(2); }
    const u32 g = lane >> 4, j = lane & 15;
    v4u qf[4];
#pragma unroll
    for (int k = 0; k < 4; ++k) qf[k] = ldl<v4u>(L_QS + (j & 7) * 256 + k * 64 + g * 16);
    const u32 kb = j * 256 + ((g ^ j) << 4);
    const u32 r16 = 4 * g + (j >> 2), vb = r16 * 256 + ((r16 & 7) << 5) + (lane & 3) * 8;
    float m = -INFINITY, l = 0.f;
    v4f acc[8];
#pragma unroll
    for (int b = 0; b < 8; ++b) acc[b] = v4f{0.f, 0.f, 0.f, 0.f};
    const u32 ma = opq(L_MRG + lane * 16), mla = opq(L_MRG + 4096 + lane * 8);
    auto raw = [&](u32 n, float (&sv)[8]) {
      const u32 pos = n % NH, ka = pos * 8192;
      S.wait<C_FULL>(n + 1);
      v4u kf[8];
#pragma unroll
      for (int k = 0; k < 4; ++k) { const u32 a = ka + (kb ^ (k << 6)); kf[k] = ldl<v4u>(a); kf[4 + k] = ldl<v4u>(a + 4096); }
      v4f s0 = {0.f, 0.f, 0.f, 0.f}, s1 = s0;
#pragma unroll
      for (int k = 0; k < 4; ++k) { s0 = mfma(kf[k], qf[k], s0); s1 = mfma(kf[4 + k], qf[k], s1); }
      S.set_at<4 * C_FREE>(S.base + 4 * pos, n / NH * 4 + 4);
#pragma unroll
      for (int i = 0; i < 4; ++i) {
        const u32 a = i < 2 ? pk(s0[2 * i], s0[2 * i + 1]) : pk(s1[2 * i - 4], s1[2 * i - 3]);
        const u32 b = pk(lo16(a) * 0.08838834764831845f, hi16(a) * 0.08838834764831845f);
        sv[2 * i] = lo16(b); sv[2 * i + 1] = hi16(b);
      }
      return fmx(fmx(fmx(sv[0], sv[1]), fmx(sv[2], sv[3])), fmx(fmx(sv[4], sv[5]), fmx(sv[6], sv[7])));
    };
    auto probs = [&](const float (&sv)[8]) {
      float e[8];
#pragma unroll
      for (int i = 0; i < 8; ++i) e[i] = ex2((sv[i] - m) * LOG2E);
      l += ((e[0] + e[1]) + (e[2] + e[3])) + ((e[4] + e[5]) + (e[6] + e[7]));
      return v4u{pk(e[0], e[1]), pk(e[2], e[3]), pk(e[4], e[5]), pk(e[6], e[7])};
    };
    auto values = [&](u32 n, v4u pf) {
      const u32 pos = n % NH;
      u32 a[8];
#pragma unroll
      for (int b = 0; b < 8; ++b) a[b] = opq(pos * 8192 + (vb ^ (b << 5)));
      S.wait<C_FULL>(n + 1);
      v4u vf[8];
#pragma unroll
      for (int b = 0; b < 8; ++b) vf[b] = cat(trd(a[b]), trd(a[b] + 4096));
#pragma unroll
      for (int b = 0; b < 8; ++b) acc[b] = mfma(vf[b], pf, acc[b]);
      S.set_at<4 * C_FREE>(S.base + 4 * pos, n / NH * 4 + 4);
    };
    auto fold = [&]() {
      const v2f ml = ldl<v2f>(mla);
      const float mx = fmx(m, ml.x), al = ex2((m - mx) * LOG2E), be = ex2((ml.x - mx) * LOG2E);
      l = l * al + be * ml.y;
      m = mx;
#pragma unroll
      for (int b2 = 0; b2 < 4; ++b2) {
        const v4f o = ldl<v4f>(ma + b2 * 1024) * be;
        acc[2 * b2] = acc[2 * b2] * al + v4f{o.x, o.y, o.x, o.y};
        acc[2 * b2 + 1] = acc[2 * b2 + 1] * al + v4f{o.z, o.w, o.z, o.w};
      }
    };
    auto rescale = [&](float mx) {
      mx = fmx(m, max32(max16(mx)));
      if (__builtin_amdgcn_ballot_w64(mx > m) != 0ull) {
        const float al = ex2((m - mx) * LOG2E);
        l *= al;
#pragma unroll
        for (int b = 0; b < 8; ++b) acc[b] *= al;
        m = mx;
      }
    };
    const int order = (w - (int)NT + 1) & 3;
#pragma unroll 1
    for (u32 it = w; it < NT - 2; it += 4) {
      float sv[8];
      rescale(raw(nkv + 2 * it, sv));
      values(nkv + 2 * it + (it == NT - 3 ? 3 : 1), probs(sv));
    }
    const bool hi = lanes(M_HI8);
    if (order < 3) {
      if (order > 0) { S.wait<C_M>(order); fold(); }
#pragma unroll
      for (int b2 = 0; b2 < 4; ++b2) {
        const v4f a = acc[2 * b2], b = acc[2 * b2 + 1];
        stl<v4f>(ma + b2 * 1024, v4f{sel(hi, a.z, a.x), sel(hi, a.w, a.y), sel(hi, b.z, b.x), sel(hi, b.w, b.y)});
      }
      stl<v2f>(mla, v2f{m, l});
      S.set<C_M>(order + 1);
    } else {
      const u32 n = ntail - 5;
      float sa[8], sb[8];
      const float mxa = raw(n, sa), mxb = raw(n + 1, sb);
      rescale(fmx(mxa, mxb));
      const v4u p0 = probs(sa), p1 = probs(sb);
      const u32 o = opq(W_P + ((16 * grp + 2 * (j & 7)) * 64 + s) * 512 + g * 32 + (j & 8) * 2), o2 = opq(o + 64 * 512);
      const u32 om = opq(lane < 8 ? W_ML + (64 * grp + s) * 128 + lane * 16 : OOB);
      STAMP(23);
      values(n + 3, p0);
      S.wait<C_M>(3);
      STAMP(24);
      const v2f ml = ldl<v2f>(mla);
      const float mx = fmx(m, ml.x), al = ex2((m - mx) * LOG2E), be = ex2((ml.x - mx) * LOG2E);
      gs16(c.ws, om, v4u{asu(mx), c.tag, asu(sum32(sum16(l * al + be * ml.y))), c.tag});
      v4f y[4];
#pragma unroll
      for (int b2 = 0; b2 < 4; ++b2) { y[b2] = ldl<v4f>(ma + b2 * 1024) * be; asm volatile("" : "+v"(y[b2])); }
      STAMP(25);
      values(n + 4, p1);
      STAMP(5);
#pragma unroll
      for (int b = 0; b < 8; ++b)
        gs16(c.ws, (b < 4 ? o : o2) + (b & 3) * 128,
             v4u{asu(sel(hi, acc[b].z, acc[b].x) * al + y[b >> 1][2 * (b & 1)]), c.tag, asu(sel(hi, acc[b].w, acc[b].y) * al + y[b >> 1][2 * (b & 1) + 1]), c.tag});
      STAMP(6);
    }
  } else {
    __builtin_amdgcn_s_barrier();
    if (role == 0) {
      S.wait<C_FULL>(26);
      take_wo();
    }
    S.wait<C_Q>(4);
    c.tag = rfl(*(volatile LAS u32*)(S.base + 4 * C_TAG));
    if (role == 0) {
      if (w == 4) {
      v4u q[2];
      const u32 xp = *(const GAS u32*)(p.x + 16 * r + 2 * (lane & 7));
      gather16<2, 8>(S, c.ws, W_MOD2 + (64 * ((lane >> 3) & 3) + 32 * (lane >> 5) + (r >> 2)) * 512 + ((r & 3) * 16 + 2 * (lane & 7)) * 8,
                     [](int i) { return (u32)i * 16 * 512; }, q, c.tag);
      float y[4] = {asf(q[0].x), asf(q[0].z), asf(q[1].x), asf(q[1].z)}, o[4];
#pragma unroll
      for (int i = 0; i < 4; ++i) { y[i] += dpp<ROR8>(y[i]); y[i] = rbf(sum16(y[i])); o[i] = sw32(y[i], y[i]); }
      stl<v4f>(L_HS + (lane & 7) * 16, v4f{y[0], o[2], lo16(xp), 0.f});
      stl<v4f>(L_HS + 128 + (lane & 7) * 16, v4f{y[1], o[3], hi16(xp), 0.f});
      gs16(c.ws, lane < 8 ? W_CS + r * 128 + lane * 16 : OOB, v4u{pk(1.f + o[0], y[2]), c.tag, pk(1.f + o[1], y[3]), c.tag});
      }
    } else if (role == 1 && w == 4) {
      v4u q[1];
      gather16<1, 32>(S, c.ws, W_CS + (128 * (wg & 7) + 2 * lane) * 8, [](int) { return 0u; }, q, c.tag);
      stl<v2u>(L_CS + lane * 8, v2u{q[0].x, q[0].z});
      S.set<C_CS>(1);
    }
  }

  if (role == 0) {
    const Masks M = mk_masks();
    const u32 pa = L_PRIV + w * 512;
    const u32 xa = opq(pa + (lane >> 4) * 8), oo = opq(W_A + (tile_of(lane, v) * 64 + r) * 128 + (lane >> 4) * 32 + (lane & 8) * 2);
    const u32 ra = opq(L_QS + lane * 4), wr = opq(pa + lane * 2);
    S.wait<C_FULL>(ntail);
    if (w == 4) { STAMP(7); }
    combine_part(p, c, S);
    if (w < 4) return;
    const float o = sum8(ra);
    stl<u16>(wr, (u16)pk(o, o));
    CBAR();
    const v4u x0 = xfrag(xa, 0), x1 = xfrag(xa, 1);
    gs16(c.ws, oo, tiles8(wa, x0, x1, M, c.tag));
    gs16(c.ws, oo + 4 * 64 * 128, tiles8(wb, x0, x1, M, c.tag));
    if (v == 0) {
      STAMP(9);
      const v4f kp = ldl<v4f>(L_HS + (lane >> 5) * 128 + (lane & 7) * 16);
      const u32 ox = opq(lane < 10 ? W_X + r * 128 + lane * 8 : OOB);
      const u32 ia = opq(W_A + (r * 64 + (lane >> 3)) * 128 + (lane & 7) * 16);
      nap<NAP2>();
      v4u q[8];
      gather16<8, 1>(S, c.ws, ia, [](int i) { return (u32)i * 1024; }, q, c.tag);
      STAMP(10);
      const float a = rbf(kp.z + rbf(kp.x * rbf(col_total(q))));
      float a0 = a;
      const float a1 = sw32(a0, a0);
      const float mu = sum_lo3(a0 + a1) * (1.f / 16.f);
      const float m2 = sum_lo3((a0 - mu) * (a0 - mu) + (a1 - mu) * (a1 - mu));
      gs8(c.ws, ox, v2u{asu(sel(lanes(0xff), asf(pk(a0, a1)), sel(lanes(0x100), mu, m2))), c.tag});
      STAMP(11);
      const u32 id = opq(W_D + (r * 56 + (lane >> 3)) * 128 + (lane & 7) * 16);
      const u32 oy = opq(lane < 8 ? 32 * r + 4 * lane : OOB);
      nap<NAP5>();
      v4u q7[7];
      gather16<7, 2>(S, c.ws, id, [](int i) { return (u32)i * 1024; }, q7, c.tag);
      STAMP(15);
      float y0 = a + rbf(kp.y * rbf(col_total(q7)));
      const float y1 = sw32(y0, y0);
      __builtin_amdgcn_raw_buffer_store_b32(pk(y0, y1), mk(p.out, 2048), oy, 0, 0);
      __builtin_amdgcn_raw_buffer_store_b32(c.tag, c.ws, (r | lane) ? OOB : W_EPOCH, 0, 16);
      STAMP(16);
      if (r == 0) {
        const u64 te = now(), ce = __builtin_amdgcn_s_memtime();
        while (now() - te < 300) {}
        u64 t0 = ~0ull, mx = 0, b[4];
        for (int i = 0; i < 4; ++i) {
          const u64 a = __hip_atomic_load((GAS u64*)(p.ws + W_STAMP + ((size_t)(lane + 64 * i) * 32) * 8), __ATOMIC_RELAXED, __HIP_MEMORY_SCOPE_AGENT);
          b[i] = __hip_atomic_load((GAS u64*)(p.ws + W_STAMP + ((size_t)(lane + 64 * i) * 32 + (XK < 32 ? XK : 0)) * 8), __ATOMIC_RELAXED, __HIP_MEMORY_SCOPE_AGENT);
          t0 = a < t0 ? a : t0;
        }
        for (int o = 32; o > 0; o >>= 1) { const u64 a = __shfl_xor(t0, o); t0 = a < t0 ? a : t0; }
        for (int i = 0; i < 4; ++i) { const u64 d = b[i] - t0; if (d < 20000 && d > mx) mx = d; }
        for (int o = 32; o > 0; o >>= 1) { const u64 a = __shfl_xor(mx, o); mx = a > mx ? a : mx; }
        u64 val = XK == 100 ? te - t0 : XK == 101 ? (ce - cstart) * 100 / (te - tstart) : XK == 102 ? 10 : mx;
        if (val > 20000) val = 20000;
        while (now() - te < val * 100) {}
      }
    }
  } else if (role == 1) {
    stage_B(p, c, S, lam >= 7, wg & 7, lam % 7, ntail);
  } else if (role == 2) {
    stage_D(p, c, S, tt, ntail);
  }
}

extern "C" void kernel_launch(void* const* d_in, const int* in_sizes, int n_in, void* d_out, int out_size, void* d_ws,
                              size_t ws_size, hipStream_t stream) {
  Params p;
  p.x = (const u16*)d_in[0]; p.K = (const u16*)d_in[1]; p.V = (const u16*)d_in[2];
  p.wq = (const u16*)d_in[3]; p.wo = (const u16*)d_in[4];
  p.wg = (const u16*)d_in[6]; p.wu = (const u16*)d_in[7]; p.wd = (const u16*)d_in[8];
  p.c = (const u16*)d_in[9]; p.wmod = (const u16*)d_in[10];
  p.out = (u16*)d_out; p.ws = (char*)d_ws;
  (void)hipFuncSetAttribute((const void*)fused_block, hipFuncAttributeMaxDynamicSharedMemorySize, 163840);
  hipLaunchKernelGGL(fused_block, dim3(256), dim3(512), 163840, stream, p);
}
```

```cpp
#include <hip/hip_runtime.h>
#include <stdint.h>
#define STAMPS 1
#define XK 21

typedef unsigned short u16;
typedef unsigned u32;
typedef unsigned long long u64;
typedef u32 v4u __attribute__((ext_vector_type(4)));
typedef u32 v2u __attribute__((ext_vector_type(2)));
typedef float v4f __attribute__((ext_vector_type(4)));
typedef float v2f __attribute__((ext_vector_type(2)));
typedef __bf16 v8bf __attribute__((ext_vector_type(8)));
typedef __bf16 v2bf __attribute__((ext_vector_type(2)));
typedef short v4s __attribute__((ext_vector_type(4)));
#define LAS __attribute__((address_space(3)))
#define GAS __attribute__((address_space(1)))
#define DI __device__ __forceinline__
typedef __amdgpu_buffer_rsrc_t rsrc_t;

#define ROT 37
#define NAP0A 10
#define NAP0B 10
#define NAP1 8
#define NAP2 10
#define NAP5 64
#define PACE0 4
#define PUBNAP 0
#define PACE 0
#define DEPTH 24
#define DEPTH2 16

constexpr u32 W_EPOCH = 0;
constexpr u32 W_MOD1 = 4096;
constexpr u32 W_Q = W_MOD1 + 256 * 512;
constexpr u32 W_MOD2 = W_Q + 256 * 512;
constexpr u32 W_CS = W_MOD2 + 256 * 512;
constexpr u32 W_X = W_CS + 64 * 128;
constexpr u32 W_ML = W_X + 64 * 128;
constexpr u32 W_GU = W_ML + 32 * 64 * 16;
constexpr u32 W_A = W_GU + 56 * 16 * 512;
constexpr u32 W_D = W_A + 64 * 64 * 128;
constexpr u32 W_P = W_D + 64 * 64 * 128;
constexpr u32 W_END = W_P + 64 * 64 * 512;
constexpr u32 W_STAMP = W_END;

constexpr u32 NH = 18;
constexpr u32 L_CTRL = NH * 8192;
constexpr u32 L_MRG = L_CTRL + 256;
constexpr u32 L_QS = L_MRG + 4608;
constexpr u32 L_HS = L_QS + 2048;
constexpr u32 L_CS = L_HS + 512;
constexpr u32 L_PRIV = L_CS + 4096;
constexpr u32 L_END = L_PRIV + 4096;
static_assert(L_END <= 163840, "LDS");
constexpr u32 C_FULL = 0, C_H = 2, C_Q = 3, C_M = 4, C_CS = 5, C_TAG = 6, C_W = 7, C_FREE = 16;
constexpr float LOG2E = 1.4426950408889634f;

struct Params {
  const u16 *x, *K, *V, *wq, *wo, *wg, *wu, *wd, *c, *wmod;
  u16* out;
  char* ws;
};

template <class T> DI T ldl(u32 a) { return *(LAS T*)a; }
template <class T> DI void stl(u32 a, T v) { *(LAS T*)a = v; }
DI u32 rfl(u32 v) { return __builtin_amdgcn_readfirstlane(v); }
DI float asf(u32 v) { return __builtin_bit_cast(float, v); }
DI u32 asu(float v) { return __builtin_bit_cast(u32, v); }
DI float lo16(u32 v) { return asf(v << 16); }
DI float hi16(u32 v) { return asf(v & 0xffff0000u); }
DI u32 pk(float a, float b) { v2f t = {a, b}; return __builtin_bit_cast(u32, __builtin_convertvector(t, v2bf)); }
DI float rbf(float a) { return hi16(pk(a, a)); }
#define CBAR() asm volatile("" ::: "memory")
#define LGKM0() asm volatile("s_waitcnt lgkmcnt(0)" ::: "memory")
DI v4f mfma(v4u a, v4u b, v4f c) {
  return __builtin_amdgcn_mfma_f32_16x16x32_bf16(__builtin_bit_cast(v8bf, a), __builtin_bit_cast(v8bf, b), c, 0, 0, 0);
}
DI v2u trd(u32 addr) { return __builtin_bit_cast(v2u, __builtin_amdgcn_ds_read_tr16_b64_v4i16((LAS v4s*)addr)); }
DI v4u cat(v2u a, v2u b) { return v4u{a.x, a.y, b.x, b.y}; }
template <int CTRL> DI float dpp(float x) { return asf(__builtin_amdgcn_update_dpp(0, asu(x), CTRL, 0xf, 0xf, true)); }
constexpr int XOR1 = 0xB1, XOR2 = 0x4E, HMIR = 0x141, ROR8 = 0x128;
DI float fmx(float a, float b) { return __builtin_amdgcn_fmed3f(a, b, INFINITY); }
DI float sw16(float& a, float b) { auto s = __builtin_amdgcn_permlane16_swap(asu(a), asu(b), false, false); a = asf(s[0]); return asf(s[1]); }
DI float sw32(float& a, float b) { auto s = __builtin_amdgcn_permlane32_swap(asu(a), asu(b), false, false); a = asf(s[0]); return asf(s[1]); }
DI float sum16(float x) { const float o = sw16(x, x); return x + o; }
DI float sum32(float x) { const float o = sw32(x, x); return x + o; }
DI float max16(float x) { const float o = sw16(x, x); return fmx(x, o); }
DI float max32(float x) { const float o = sw32(x, x); return fmx(x, o); }
DI float sum_lo3(float x) { x += dpp<XOR1>(x); x += dpp<XOR2>(x); return x + dpp<HMIR>(x); }
DI float sum64(float x) { x = sum_lo3(x); x += dpp<ROR8>(x); return sum32(sum16(x)); }
DI float max64(float x) {
  x = fmx(x, dpp<XOR1>(x)); x = fmx(x, dpp<XOR2>(x)); x = fmx(x, dpp<HMIR>(x)); x = fmx(x, dpp<ROR8>(x));
  return max32(max16(x));
}
DI float sel(bool c, float a, float b) { asm("" : "+v"(a)); asm("" : "+v"(b)); return c ? a : b; }
DI float ex2(float x) { return __builtin_amdgcn_exp2f(x); }
DI float rcp(float x) { return __builtin_amdgcn_rcpf(x); }
DI u32 opq(u32 v) { asm volatile("" : "+v"(v)); return v; }
DI u64 opqs(u64 v) { asm volatile("" : "+s"(v)); return v; }
DI bool lanes(u64 m) { return __builtin_amdgcn_inverse_ballot_w64(m); }
constexpr u64 M_HI8 = 0xff00ff00ff00ff00ull, M_HI32 = 0xffffffff00000000ull;
constexpr u32 OOB = 0x80000000u;

DI u64 now() { return __builtin_amdgcn_s_memrealtime(); }
#define STAMPV(i, val) do { if ((threadIdx.x & 63) == 0) __hip_atomic_store((GAS u64*)(p.ws + W_STAMP + ((size_t)blockIdx.x * 32 + (i)) * 8), (u64)(val), __ATOMIC_RELAXED, __HIP_MEMORY_SCOPE_AGENT); } while (0)
#define STAMP(i) STAMPV(i, now())

struct Sync {
  u32 base;
  template <u32 OFF> DI void wait_at(u32 addr, u32 v) {
    u32 t, s, n;
    asm volatile("s_movk_i32 %2, 0x7fff\n1:\n\tds_read_b32 %0, %3 offset:%c5\n\ts_waitcnt lgkmcnt(0)\n\tv_readfirstlane_b32 %1, %0\n\t"
                 "s_cmp_ge_u32 %1, %4\n\ts_cbranch_scc1 2f\n\ts_sleep 1\n\ts_sub_u32 %2, %2, 1\n\ts_cbranch_scc0 1b\n2:"
                 : "=&v"(t), "=&s"(s), "=&s"(n) : "v"(addr), "s"(v), "i"(OFF) : "memory", "scc");
  }
  template <u32 W> DI void wait(u32 v) { wait_at<4 * W>(base, v); }
  template <u32 W, int N> DI void add1() {
    u64 e;
    asm volatile("s_mov_b64 %0, exec\n\ts_mov_b64 exec, %4\n\tds_add_u32 %1, %2 offset:%c3\n\ts_mov_b64 exec, %0"
                 : "=&s"(e) : "v"(base + (N > 1 ? 4 * (threadIdx.x & 63) : 0)), "v"(1u), "i"(4 * W), "s"((u64)((1ull << N) - 1)) : "memory");
  }
  template <u32 OFF> DI void set_at(u32 addr, u32 v) {
    asm volatile("ds_write_b32 %0, %1 offset:%c2" :: "v"(addr), "v"(v), "i"(OFF) : "memory");
  }
  template <u32 W> DI void set(u32 v) { set_at<4 * W>(base, v); }
};
DI v4u gl16(rsrc_t r, u32 off, u32 so = 0) { return __builtin_amdgcn_raw_buffer_load_b128(r, off, so, 16); }
DI v2u gl8(rsrc_t r, u32 off) { return __builtin_amdgcn_raw_buffer_load_b64(r, off, 0, 16); }
DI void gs16(rsrc_t r, u32 off, v4u v) { __builtin_amdgcn_raw_buffer_store_b128(v, r, off, 0, 16); }
DI void gs8(rsrc_t r, u32 off, v2u v) { __builtin_amdgcn_raw_buffer_store_b64(v, r, off, 0, 16); }
template <int K> DI void nap() { if constexpr (K > 0) __builtin_amdgcn_s_sleep(K); }
DI bool all_ok(u32 bad) { return __builtin_amdgcn_ballot_w64(bad != 0u) == 0ull; }
template <int N, int NAP, class F> DI void gather16(Sync& S, rsrc_t r, u32 base, F f, v4u (&v)[N], u32 tag) {
  for (u32 n = 0;;) {
    CBAR();
#pragma unroll
    for (int i = 0; i < N; ++i) v[i] = gl16(r, base + (f(i) & 4095u), f(i) & ~4095u);
    u32 bad = 0;
#pragma unroll
    for (int i = 0; i < N; ++i) bad |= (v[i].y ^ tag) | (v[i].w ^ tag);
    if (all_ok(bad) || ++n > 8192u) break;
    nap<NAP>();
  }
  CBAR();
}

template <int D> DI void dma4(u32 ld, u32 v0, u32 v1, u32 v2, u32 v3, rsrc_t r, u32 so) {
  asm volatile("s_mov_b32 m0, %0\n\ts_nop 0\n\t"
               "buffer_load_dwordx4 %1, %5, %6 offen nt lds\n\ts_waitcnt vmcnt(%c7)\n\t"
               "buffer_load_dwordx4 %2, %5, %6 offen offset:1024 nt lds\n\ts_waitcnt vmcnt(%c7)\n\t"
               "buffer_load_dwordx4 %3, %5, %6 offen offset:2048 nt lds\n\ts_waitcnt vmcnt(%c7)\n\t"
               "buffer_load_dwordx4 %4, %5, %6 offen offset:3072 nt lds\n\ts_waitcnt vmcnt(%c7)"
               :: "s"(ld), "v"(v0), "v"(v1), "v"(v2), "v"(v3), "s"(r), "s"(so), "i"(D) : "memory");
}
template <int D> DI void dma1(u32 ld, u32 v, rsrc_t r, u32 so) {
  asm volatile("s_mov_b32 m0, %0\n\ts_nop 0\n\tbuffer_load_dwordx4 %1, %2, %3 offen nt lds\n\ts_waitcnt vmcnt(%c4)"
               :: "s"(ld), "v"(v), "s"(r), "s"(so), "i"(D) : "memory");
}
template <int D> DI void vmwait() { asm volatile("s_waitcnt vmcnt(%c0)" :: "i"(D) : "memory"); }
struct Ring {
  Sync S;
  u32 n, pos, need, full, next, lim;
  u64 stall, first;
  DI void publish(u32 f) { if ((int)(f - full) > 0) { full = f; nap<PUBNAP>(); S.set<C_FULL>(f); } }
  DI u32 open() {
    if (n >= NH) {
      const u32 fa = S.base + 4 * pos;
      if (rfl(*(volatile LAS u32*)(fa + 4 * C_FREE)) < need) {
        const u64 t0 = now(); if (!first) first = t0;
        vmwait<0>();
        publish(n);
        S.wait_at<4 * C_FREE>(fa, need);
        stall += now() - t0;
      }
    }
    return pos * 8192u;
  }
  template <int D> DI void close() {
    ++n; ++pos;
    if (pos == NH) { pos = 0; need += 4; }
    publish(n - D / 8);
  }
};
DI rsrc_t mk(const void* p, u32 bytes) { return __builtin_amdgcn_make_buffer_rsrc((void*)p, 0, bytes, 0x00020000); }
template <int D> DI void ld_slab(Ring& R, rsrc_t r, u32 base, u32 pitch, int nh, int lane) {
  const u32 row = lane >> 3, v = row * pitch + (((lane & 7) ^ (((row >> 1) & 3) << 1)) << 4);
#pragma unroll 1
  for (int h = 0; h < nh; ++h) {
    const u32 ld = R.open();
#pragma unroll
    for (int i = 0; i < 8; ++i) { dma1<D>(ld + i * 1024, v, r, base); base += 8 * pitch; }
    R.close<D>();
  }
}
template <int D, u32 PITCH, int SH> DI void ld_rows(Ring& R, rsrc_t r, u32 base, int lane) {
  u32 v[8];
#pragma unroll
  for (int i = 0; i < 8; ++i) v[i] = ((lane ^ (i << 1)) << 4) + i * PITCH - (i & 3) * 1024;
#pragma unroll 1
  for (int h = 0; h < 16; ++h) {
    const u32 ld = R.open(), so = base + (h >> SH) * 8 * PITCH + (h & ((1 << SH) - 1)) * 1024;
    dma4<D>(ld, v[0], v[1], v[2], v[3], r, so);
    dma4<D>(ld + 4096, v[4], v[5], v[6], v[7], r, so);
    R.close<D>();
  }
}

DI v4u xfrag(u32 addr, int k) { return cat(ldl<v2u>(addr + k * 64), ldl<v2u>(addr + k * 64 + 32)); }
template <int NK> DI v4f gemv_cols(u32 n0, u32 xaddr, int c, int lane) {
  const u32 g = lane >> 4, r16 = 4 * g + ((lane & 15) >> 2);
  const u32 a = opq(n0 * 8192 + r16 * 128 + ((c ^ ((r16 >> 1) & 3)) << 5) + (lane & 3) * 8), xa = opq(xaddr + g * 8);
  v4f acc = {0.f, 0.f, 0.f, 0.f};
#pragma unroll
  for (int k = 0; k < NK; ++k) acc = mfma(cat(trd(a + k * 4096), trd(a + k * 4096 + 2048)), xfrag(xa, k), acc);
  return acc;
}
DI void put_tile(rsrc_t ws, u32 off, v4f acc, u32 tag, int lane) {
  const bool hi = lanes(M_HI8);
  gs16(ws, (lane & 7) ? OOB : off + (lane >> 4) * 32 + (lane & 8) * 2, v4u{asu(sel(hi, acc.z, acc.x)), tag, asu(sel(hi, acc.w, acc.y)), tag});
}
DI u32 hs_addr(u32 n) { return (n % NH) * 8192u; }
template <int SH> DI void read_rows(u32 n0, int v, int lane, v4u (&wf)[16]) {
  const u32 g = lane >> 4, r8 = (4 * g + ((lane & 15) >> 2)) & 7;
  const u32 lo = r8 * 1024 + ((v ^ r8) << 5) + (lane & 3) * 8;
#pragma unroll
  for (int q = 0; q < 4; ++q) {
    const int half = SH ? q >> 1 : 0, k = SH ? q & 1 : q;
    u32 a[2];
#pragma unroll
    for (int t = 0; t < 2; ++t) {
      const u32 h0 = hs_addr(n0 + ((4 * k + 2 * t) << SH) + half), h1 = hs_addr(n0 + ((4 * k + 2 * t + 1) << SH) + half);
      a[t] = lo + (lanes(M_HI32) ? h1 : h0);
    }
#pragma unroll
    for (int u = 0; u < 4; ++u) wf[SH ? (half * 4 + u) * 2 + k : 4 * k + u] = cat(trd(a[0] + u * 256), trd(a[1] + u * 256));
  }
}
struct Masks { u64 m[8]; };
DI Masks mk_masks() {
  Masks M;
#pragma unroll
  for (int i = 0; i < 8; ++i) M.m[i] = opqs(0x0101010101010101ull << i);
  return M;
}
DI v4u tiles8(const v4u (&wf)[16], v4u x0, v4u x1, const Masks& M, u32 tag) {
  v4f acc[8];
#pragma unroll
  for (int i = 0; i < 8; ++i) acc[i] = mfma(wf[2 * i], x0, v4f{0.f, 0.f, 0.f, 0.f});
#pragma unroll
  for (int i = 0; i < 8; ++i) acc[i] = mfma(wf[2 * i + 1], x1, acc[i]);
  const bool hi = lanes(M_HI8);
  float o0 = sel(hi, acc[0].z, acc[0].x), o1 = sel(hi, acc[0].w, acc[0].y);
#pragma unroll
  for (int i = 1; i < 8; ++i) {
    o0 = sel(lanes(M.m[i]), sel(hi, acc[i].z, acc[i].x), o0);
    o1 = sel(lanes(M.m[i]), sel(hi, acc[i].w, acc[i].y), o1);
  }
  return v4u{asu(o0), tag, asu(o1), tag};
}
DI u32 tile_of(int lane, int v) { return 32 * ((lane >> 2) & 1) + 8 * (lane & 3) + v; }

struct Ctx { rsrc_t ws; u32 tag; int lane, w, wg, grp, s; };

struct XIn { v4u xa, xb; u32 xp; };
DI XIn load_x(const Params& p, const Ctx& c) {
  return XIn{*(const GAS v4u*)(p.x + c.lane * 16), *(const GAS v4u*)(p.x + c.lane * 16 + 8),
             *(const GAS u32*)(p.x + 256 * (c.wg >> 6) + 64 * c.w + 2 * (c.lane & 31))};
}
DI float make_ln(const XIn& X) {
  const v4u xa = X.xa, xb = X.xb;
  float sm = 0.f;
#pragma unroll
  for (int i = 0; i < 4; ++i) sm += (lo16(xa[i]) + hi16(xa[i])) + (lo16(xb[i]) + hi16(xb[i]));
  const float mean = sum64(sm) * (1.f / 1024.f);
  float q = 0.f;
#pragma unroll
  for (int i = 0; i < 4; ++i) {
    const float a = lo16(xa[i]) - mean, b = hi16(xa[i]) - mean, d = lo16(xb[i]) - mean, e = hi16(xb[i]) - mean;
    q += (a * a + b * b) + (d * d + e * e);
  }
  const float var = sum64(q) * (1.f / 1024.f);
  const float mb = rbf(mean), den = rbf(sqrtf(rbf(rbf(var) + 1e-5f)));
  return asf(opq(asu(rbf(rbf((lanes(M_HI32) ? hi16(X.xp) : lo16(X.xp)) - mb) / den))));
}
DI void make_h(const Params& p, const Ctx& c, Sync& S, float ln) {
  const int lane = c.lane, kq = c.wg >> 6;
  const u32 base = opq(W_MOD1 + (32 * (lane >> 5) + 4 * kq + c.w) * 512 + (lane & 31) * 16);
  const u32 ha = opq(L_HS + (64 * c.w + 2 * (lane & 31) + (lane >> 5)) * 2);
  v4u v[8];
  nap<NAP0A>();
  gather16<8, 4>(S, c.ws, base, [](int i) { return (u32)(64 * (i & 3) + 16 * (i >> 2)) * 512; }, v, c.tag);
  if (c.w == 0) { STAMP(17); }
  float s0 = (asf(v[0].x) + asf(v[1].x)) + (asf(v[2].x) + asf(v[3].x)), c0 = (asf(v[4].x) + asf(v[5].x)) + (asf(v[6].x) + asf(v[7].x));
  s0 += sw32(s0, (asf(v[0].z) + asf(v[1].z)) + (asf(v[2].z) + asf(v[3].z)));
  c0 += sw32(c0, (asf(v[4].z) + asf(v[5].z)) + (asf(v[6].z) + asf(v[7].z)));
  const float h = rbf(ln * rbf(1.f + rbf(c0))) + rbf(s0);
  stl<u16>(ha, (u16)pk(h, h));
  S.add1<C_H, 1>();
}
DI void make_q(const Params& p, const Ctx& c, Sync& S) {
  const int lane = c.lane;
  v4u v[8];
  const u32 base = W_Q + (16 * c.grp + 4 * c.w + (lane >> 5)) * 512 + (lane & 31) * 16;
  gather16<8, 4>(S, c.ws, base, [](int i) { return (u32)(64 * (i & 3) + 2 * (i >> 2)) * 512; }, v, c.tag);
#pragma unroll
  for (int i = 0; i < 8; i += 4)
    stl<u32>(L_QS + (128 * c.w + 16 * i + lane) * 4,
             pk((asf(v[i].x) + asf(v[i + 1].x)) + (asf(v[i + 2].x) + asf(v[i + 3].x)), (asf(v[i].z) + asf(v[i + 1].z)) + (asf(v[i + 2].z) + asf(v[i + 3].z))));
  S.add1<C_Q, 1>();
}

DI float combine_part(const Params& p, const Ctx& c, Sync& S) {
  const int lane = c.lane, a = 16 * c.grp + c.s, sl = 8 * c.w + (lane >> 3);
  const u32 base = opq(W_P + (a * 64 + sl) * 512 + (lane & 7) * 16);
  const int dim = 2 * (lane & 7) + 32 * (lane >> 5) + 16 * ((lane >> 4) & 1) + ((lane >> 3) & 1);
  const u32 ra = opq(L_QS + (c.w * 64 + dim) * 4);
  if (c.w == 5) {
    v4u ml[1];
    gather16<1, 2>(S, c.ws, W_ML + (c.grp * 64 + lane) * 128 + (c.s >> 1) * 16, [](int) { return 0u; }, ml, c.tag);
    const float e = ex2((asf(ml[0].x) - max64(asf(ml[0].x))) * LOG2E);
    stl<float>(L_HS + 256 + lane * 4, e * (1.f / sum64(e * asf(ml[0].z))));
    S.set<C_W>(1);
    STAMP(26);
  }
  S.wait<C_W>(1);
  const float wt = ldl<float>(L_HS + 256 + sl * 4);
  nap<NAP1>();
  v4u v[4];
  gather16<4, 1>(S, c.ws, base, [](int i) { return (u32)i * 128; }, v, c.tag);
  STAMP(8);
  float z0 = asf(v[0].x) * wt, z1 = asf(v[0].z) * wt, z2 = asf(v[1].x) * wt, z3 = asf(v[1].z) * wt;
  z0 += sw32(z0, asf(v[2].x) * wt); z1 += sw32(z1, asf(v[2].z) * wt);
  z2 += sw32(z2, asf(v[3].x) * wt); z3 += sw32(z3, asf(v[3].z) * wt);
  z0 += sw16(z0, z2); z1 += sw16(z1, z3);
  const bool up = lanes(M_HI8);
  stl<float>(ra, sel(up, z1, z0) + dpp<ROR8>(sel(up, z0, z1)));
  LGKM0();
  __builtin_amdgcn_s_barrier();
  CBAR();
  return 0.f;
}
DI float sum8(u32 a) {
  float r[8];
#pragma unroll
  for (int i = 0; i < 8; ++i) r[i] = ldl<float>(a + i * 256);
  return ((r[0] + r[1]) + (r[2] + r[3])) + ((r[4] + r[5]) + (r[6] + r[7]));
}

DI void stage_B(const Params& p, const Ctx& c, Sync& S, int m, int kb, int cb, u32 n0) {
  const int lane = c.lane, w = c.w;
  S.wait<C_FULL>(n0 + 16);
  v4u wf[16];
  read_rows<0>(n0, w, lane, wf);
  S.wait<C_CS>(1);
  const v2u cs = ldl<v2u>(L_CS + lane * 8);
  const Masks M = mk_masks();
  const int P = 64 * kb + lane, tile = w + 8 * (lane & 3);
  const u32 pa = opq(L_PRIV + w * 512 + lane * 4), xa = opq(L_PRIV + w * 512 + (lane >> 4) * 8);
  const u32 o0 = opq(W_X + (P >> 3) * 128 + (P & 7) * 8), o1 = opq(W_X + lane * 128 + 64);
  const u32 oo = opq((lane & 4) ? OOB : W_GU + ((8 * cb + (tile >> 2)) * 16 + 2 * kb + m) * 512 + (tile & 3) * 128 + (lane >> 4) * 32 + (lane & 8) * 2);
  LGKM0();
  STAMP(12);
  v2u xg; v4u st;
  for (u32 n = 0;;) {
    CBAR();
    xg = gl8(c.ws, o0);
    st = gl16(c.ws, o1);
    if (all_ok((xg.y ^ c.tag) | (st.y ^ c.tag) | (st.w ^ c.tag)) || ++n > 8192u) break;
    nap<3>();
  }
  CBAR();
  STAMP(13);
  const float mu0 = asf(rfl(st.x));
  float d = asf(st.x) - mu0;
  d += sw32(d, asf(st.z) + 16.f * d * d);
  d = sum_lo3(d); d += dpp<ROR8>(d); d = sum16(d);
  const float e = sw32(d, d) * (1.f / 1024.f);
  d *= 1.f / 64.f;
  const float mb = rbf(mu0 + d), inv = rcp(rbf(__builtin_amdgcn_sqrtf(rbf(rbf(fmx(e - d * d, 0.f)) + 1e-5f))));
  const u32 xm = pk(lo16(xg.x) - mb, hi16(xg.x) - mb), ln = pk(lo16(xm) * inv, hi16(xm) * inv);
  const u32 sc = pk(lo16(ln) * lo16(cs.x), hi16(ln) * lo16(cs.y));
  stl<u32>(pa, pk(lo16(sc) + hi16(cs.x), hi16(sc) + hi16(cs.y)));
  CBAR();
  v4f acc[4];
#pragma unroll
  for (int u = 0; u < 4; ++u) acc[u] = v4f{0.f, 0.f, 0.f, 0.f};
#pragma unroll
  for (int k = 0; k < 4; ++k) {
    const v4u xf = xfrag(xa, k);
#pragma unroll
    for (int u = 0; u < 4; ++u) acc[u] = mfma(wf[k * 4 + u], xf, acc[u]);
  }
  const bool hi = lanes(M_HI8);
  float r0 = sel(hi, acc[0].z, acc[0].x), r1 = sel(hi, acc[0].w, acc[0].y);
#pragma unroll
  for (int u = 1; u < 4; ++u) {
    r0 = sel(lanes(M.m[u]), sel(hi, acc[u].z, acc[u].x), r0);
    r1 = sel(lanes(M.m[u]), sel(hi, acc[u].w, acc[u].y), r1);
  }
  gs16(c.ws, oo, v4u{asu(r0), c.tag, asu(r1), c.tag});
  STAMP(14);
}

DI void stage_D(const Params& p, const Ctx& c, Sync& S, int t, u32 n0) {
  const int lane = c.lane, w = c.w;
  S.wait<C_FULL>(n0 + 16);
  v4u wf[16];
  read_rows<1>(n0, w, lane, wf);
  const Masks M = mk_masks();
  const u32 pa = opq(L_PRIV + w * 512 + lane * 2), xa = opq(L_PRIV + w * 512 + (lane >> 4) * 8);
  const u32 oi = opq(W_GU + (t * 16 + 2 * w) * 512 + lane * 16);
  const u32 wa = opq(L_CS + w * 512 + (lane & 31) * 16 + (lane >> 5) * 4), ra = opq(L_CS + lane * 8);
  const u32 oo = opq(W_D + (tile_of(lane, w) * 56 + t) * 128 + (lane >> 4) * 32 + (lane & 8) * 2);
  LGKM0();
  STAMP(27);
  v4u q[1];
  gather16<1, 3>(S, c.ws, oi, [](int) { return 0u; }, q, c.tag);
  STAMP(28);
  stl<u32>(wa, q[0].x);
  stl<u32>(wa + 8, q[0].z);
  LGKM0();
  __builtin_amdgcn_s_barrier();
  CBAR();
  v2f r[8];
#pragma unroll
  for (int i = 0; i < 8; ++i) r[i] = ldl<v2f>(ra + i * 512);
  const v2f y = ((r[0] + r[1]) + (r[2] + r[3])) + ((r[4] + r[5]) + (r[6] + r[7]));
  const u32 gu = pk(y.x, y.y);
  const float g = lo16(gu);
  const float z = (-2.f * LOG2E * 0.7978845608028654f) * (g + 0.044715f * g * g * g);
  const float h = rbf(g * rcp(1.f + ex2(z))) * hi16(gu);
  stl<u16>(pa, (u16)pk(h, h));
  CBAR();
  gs16(c.ws, oo, tiles8(wf, xfrag(xa, 0), xfrag(xa, 1), M, c.tag));
  STAMP(29);
}

template <int N> DI float col_total(const v4u (&q)[N]) {
  float s0 = asf(q[0].x), s1 = asf(q[0].z);
#pragma unroll
  for (int i = 1; i < N; ++i) { s0 += asf(q[i].x); s1 += asf(q[i].z); }
  s0 += sw32(s0, s1);
  s0 = sum16(s0);
  return s0 + dpp<ROR8>(s0);
}

__global__ void __launch_bounds__(512) fused_block(Params p) {
  const int tid = threadIdx.x, lane = tid & 63, w = rfl(tid >> 6), wg = blockIdx.x;
  const int grp = (wg >> 3) & 3, s = (wg & 7) + ((wg >> 5) << 3);
  const bool isA = s < 16;
  const int lam = (wg >> 3) - 8;
  const int role = isA ? 0 : lam < 14 ? 1 : lam < 21 ? 2 : 3;
  const int tt = (wg & 7) + 8 * (lam % 7), r = 16 * grp + s, v = w & 3;
  const u32 nkv = isA ? 26 : 10, NT = isA ? 26 : 34, ntail = nkv + 2 * NT;
  const u64 tstart = now(), cstart = __builtin_amdgcn_s_memtime();
  Sync S{opq(L_CTRL)};
  if (w == 0) { STAMP(0); S.set_at<0>(S.base + 4 * lane, 0u); }
  Ctx c;
  c.ws = mk(p.ws, W_END);
  c.lane = lane; c.w = w; c.wg = wg; c.grp = grp; c.s = s;
  v4u wa[16], wb[16];
  auto take_wo = [&]() {
    read_rows<1>(10, v, lane, wa);
    read_rows<1>(10, v + 4, lane, wb);
    LGKM0();
    S.add1<C_FREE + 10, 8>();
    S.add1<C_FREE, 8>();
  };

  if (w == 7) {
    __builtin_amdgcn_s_setprio(3);
    Ring R{S, 0, 0, 0, 0, 0, ntail, 0, 0};
    const rsrc_t rm = mk(p.wmod, 1024 * 6144 * 2);
    ld_slab<DEPTH>(R, rm, (wg >> 5) * 128 * 12288 + (wg & 31) * 128, 12288, 2, lane);
    STAMP(21);
    __builtin_amdgcn_s_barrier();
    ld_slab<DEPTH>(R, mk(p.wq, 1024 * 4096 * 2), (wg >> 6) * 256 * 8192 + (wg & 63) * 128, 8192, 4, lane);
    ld_slab<DEPTH>(R, rm, (wg >> 6) * 256 * 12288 + 4096 + (wg & 63) * 128, 12288, 4, lane);
    if (isA) ld_rows<DEPTH, 2048, 1>(R, mk(p.wo, 4096 * 1024 * 2), r * 64 * 2048, lane);
    const rsrc_t rk = mk(p.K, 1u << 26), rv = mk(p.V, 1u << 26);
    const u32 rw = lane >> 4, ps = lane & 15;
    u32 vk[4], vv[2];
#pragma unroll
    for (int i = 0; i < 4; ++i) vk[i] = rw * 256 + ((ps ^ (4 * i + rw)) << 4);
#pragma unroll
    for (int i = 0; i < 2; ++i) vv[i] = rw * 256 + ((ps ^ ((4 * i + rw) << 1)) << 4);
    auto tile_off = [&](int tau) { return ((u32)grp << 24) + (((tau < 8 ? tau * 48 + s - 16 : 384 + (tau - 8) * 64 + s) + ROT * grp) & 2047) * 8192; };
    auto ldk = [&](int tau) {
      const u32 so = tile_off(tau), ld = R.open();
      dma4<DEPTH>(ld, vk[0], vk[1], vk[2], vk[3], rk, so);
      dma4<DEPTH>(ld + 4096, vk[0], vk[1], vk[2], vk[3], rk, so + 4096);
      R.close<DEPTH>();
    };
    auto ldv = [&](int tau) {
      const u32 so = tile_off(tau), ld = R.open();
      dma4<DEPTH>(ld, vv[0], vv[1], vv[0], vv[1], rv, so);
      dma4<DEPTH>(ld + 4096, vv[0], vv[1], vv[0], vv[1], rv, so + 4096);
      R.close<DEPTH>();
    };
    int tau = isA ? 8 : 0;
    if (isA) {
      ldk(8); ldv(8);
      vmwait<16>();
      R.publish(26);
      tau = 9;
      take_wo();
    }
#pragma unroll 1
    for (; tau < 31; ++tau) { ldk(tau); ldv(tau); }
#pragma unroll 1
    for (; tau < 34; ++tau) ldk(tau);
#pragma unroll 1
    for (tau = 31; tau < 34; ++tau) ldv(tau);
    STAMP(3);
    if (role == 1) ld_rows<DEPTH2, 7168, 0>(R, mk(lam < 7 ? p.wg : p.wu, 1024 * 3584 * 2), (wg & 7) * 128 * 7168 + (lam % 7) * 1024, lane);
    else if (role == 2) ld_rows<DEPTH2, 2048, 1>(R, mk(p.wd, 3584 * 1024 * 2), tt * 64 * 2048, lane);
    vmwait<32>(); R.publish(R.n - 4);
    vmwait<24>(); R.publish(R.n - 3);
    vmwait<16>(); R.publish(R.n - 2);
    vmwait<8>(); R.publish(R.n - 1);
    vmwait<0>(); R.publish(R.n);
    STAMP(4);
    STAMPV(31, R.stall + 1);
    __builtin_amdgcn_s_setprio(0);
    c.tag = rfl(*(volatile LAS u32*)(S.base + 4 * C_TAG));
  } else if (w < 4) {
    const u32 ep = __hip_atomic_load((GAS u32*)(p.ws + W_EPOCH), __ATOMIC_RELAXED, __HIP_MEMORY_SCOPE_AGENT);
    const u32 c1 = *(const GAS u32*)(p.c + (wg >> 5) * 128 + 2 * lane);
    const v2u c2 = *(const GAS v2u*)(p.c + (wg >> 6) * 256 + 4 * lane);
    const XIn X = load_x(p, c);
    const u32 pa = L_PRIV + w * 1024;
    LGKM0();
    __builtin_amdgcn_s_barrier();
    CBAR();
    auto silu2 = [](u32 a) { const float x = lo16(a), y = hi16(a); return pk(x / (1.f + ex2(-LOG2E * x)), y / (1.f + ex2(-LOG2E * y))); };
    stl<u32>(pa + lane * 4, silu2(c1));
    stl<v2u>(pa + 256 + lane * 8, v2u{silu2(c2.x), silu2(c2.y)});
    c.tag = rfl(ep) + 1u;
    S.set<C_TAG>(c.tag);
    if (w == 0) { STAMP(22); }
    S.wait<C_FULL>(2);
    if (w == 0) { STAMP(30); }
    put_tile(c.ws, W_MOD1 + wg * 512 + w * 128, gemv_cols<4>(0, pa, w, lane), c.tag, lane);
    S.add1<C_FREE, 2>();
    if (w == 0) { STAMP(1); }
    make_h(p, c, S, make_ln(X));
    S.wait<C_FULL>(6);
    S.wait<C_H>(4);
    if (w == 0) { STAMP(18); }
    put_tile(c.ws, W_Q + wg * 512 + w * 128, gemv_cols<8>(2, L_HS, w, lane), c.tag, lane);
    S.add1<C_FREE + 2, 4>();
    if (w == 0) { STAMP(19); }
    S.wait<C_FULL>(10);
    put_tile(c.ws, W_MOD2 + wg * 512 + w * 128, gemv_cols<8>(6, pa + 256, w, lane), c.tag, lane);
    S.add1<C_FREE + 6, 4>();
    if (w == 0) { STAMP(20); }
    nap<NAP0B>();
    make_q(p, c, S);
    S.wait<C_Q>(4);
    if (w == 0) { STAMP(2); }
    const u32 g = lane >> 4, j = lane & 15;
    v4u qf[4];
#pragma unroll
    for (int k = 0; k < 4; ++k) qf[k] = ldl<v4u>(L_QS + (j & 7) * 256 + k * 64 + g * 16);
    const u32 kb = j * 256 + ((g ^ j) << 4);
    const u32 r16 = 4 * g + (j >> 2), vb = r16 * 256 + ((r16 & 7) << 5) + (lane & 3) * 8;
    float m = -INFINITY, l = 0.f;
    v4f acc[8];
#pragma unroll
    for (int b = 0; b < 8; ++b) acc[b] = v4f{0.f, 0.f, 0.f, 0.f};
    const u32 ma = opq(L_MRG + lane * 16), mla = opq(L_MRG + 4096 + lane * 8);
    auto raw = [&](u32 n, float (&sv)[8]) {
      const u32 pos = n % NH, ka = pos * 8192;
      S.wait<C_FULL>(n + 1);
      v4u kf[8];
#pragma unroll
      for (int k = 0; k < 4; ++k) { const u32 a = ka + (kb ^ (k << 6)); kf[k] = ldl<v4u>(a); kf[4 + k] = ldl<v4u>(a + 4096); }
      v4f s0 = {0.f, 0.f, 0.f, 0.f}, s1 = s0;
#pragma unroll
      for (int k = 0; k < 4; ++k) { s0 = mfma(kf[k], qf[k], s0); s1 = mfma(kf[4 + k], qf[k], s1); }
      S.set_at<4 * C_FREE>(S.base + 4 * pos, n / NH * 4 + 4);
#pragma unroll
      for (int i = 0; i < 4; ++i) {
        const u32 a = i < 2 ? pk(s0[2 * i], s0[2 * i + 1]) : pk(s1[2 * i - 4], s1[2 * i - 3]);
        const u32 b = pk(lo16(a) * 0.08838834764831845f, hi16(a) * 0.08838834764831845f);
        sv[2 * i] = lo16(b); sv[2 * i + 1] = hi16(b);
      }
      return fmx(fmx(fmx(sv[0], sv[1]), fmx(sv[2], sv[3])), fmx(fmx(sv[4], sv[5]), fmx(sv[6], sv[7])));
    };
    auto probs = [&](const float (&sv)[8]) {
      float e[8];
#pragma unroll
      for (int i = 0; i < 8; ++i) e[i] = ex2((sv[i] - m) * LOG2E);
      l += ((e[0] + e[1]) + (e[2] + e[3])) + ((e[4] + e[5]) + (e[6] + e[7]));
      return v4u{pk(e[0], e[1]), pk(e[2], e[3]), pk(e[4], e[5]), pk(e[6], e[7])};
    };
    auto values = [&](u32 n, v4u pf) {
      const u32 pos = n % NH;
      u32 a[8];
#pragma unroll
      for (int b = 0; b < 8; ++b) a[b] = opq(pos * 8192 + (vb ^ (b << 5)));
      S.wait<C_FULL>(n + 1);
      v4u vf[8];
#pragma unroll
      for (int b = 0; b < 8; ++b) vf[b] = cat(trd(a[b]), trd(a[b] + 4096));
#pragma unroll
      for (int b = 0; b < 8; ++b) acc[b] = mfma(vf[b], pf, acc[b]);
      S.set_at<4 * C_FREE>(S.base + 4 * pos, n / NH * 4 + 4);
    };
    auto fold = [&]() {
      const v2f ml = ldl<v2f>(mla);
      const float mx = fmx(m, ml.x), al = ex2((m - mx) * LOG2E), be = ex2((ml.x - mx) * LOG2E);
      l = l * al + be * ml.y;
      m = mx;
#pragma unroll
      for (int b2 = 0; b2 < 4; ++b2) {
        const v4f o = ldl<v4f>(ma + b2 * 1024) * be;
        acc[2 * b2] = acc[2 * b2] * al + v4f{o.x, o.y, o.x, o.y};
        acc[2 * b2 + 1] = acc[2 * b2 + 1] * al + v4f{o.z, o.w, o.z, o.w};
      }
    };
    auto rescale = [&](float mx) {
      mx = fmx(m, max32(max16(mx)));
      if (__builtin_amdgcn_ballot_w64(mx > m) != 0ull) {
        const float al = ex2((m - mx) * LOG2E);
        l *= al;
#pragma unroll
        for (int b = 0; b < 8; ++b) acc[b] *= al;
        m = mx;
      }
    };
    const int order = (w - (int)NT + 1) & 3;
#pragma unroll 1
    for (u32 it = w; it < NT - 2; it += 4) {
      float sv[8];
      rescale(raw(nkv + 2 * it, sv));
      values(nkv + 2 * it + (it == NT - 3 ? 3 : 1), probs(sv));
    }
    const bool hi = lanes(M_HI8);
    if (order < 3) {
      if (order > 0) { S.wait<C_M>(order); fold(); }
#pragma unroll
      for (int b2 = 0; b2 < 4; ++b2) {
        const v4f a = acc[2 * b2], b = acc[2 * b2 + 1];
        stl<v4f>(ma + b2 * 1024, v4f{sel(hi, a.z, a.x), sel(hi, a.w, a.y), sel(hi, b.z, b.x), sel(hi, b.w, b.y)});
      }
      stl<v2f>(mla, v2f{m, l});
      S.set<C_M>(order + 1);
    } else {
      const u32 n = ntail - 5;
      float sa[8], sb[8];
      const float mxa = raw(n, sa), mxb = raw(n + 1, sb);
      rescale(fmx(mxa, mxb));
      const v4u p0 = probs(sa), p1 = probs(sb);
      const u32 o = opq(W_P + ((16 * grp + 2 * (j & 7)) * 64 + s) * 512 + g * 32 + (j & 8) * 2), o2 = opq(o + 64 * 512);
      const u32 om = opq(lane < 8 ? W_ML + (64 * grp + s) * 128 + lane * 16 : OOB);
      STAMP(23);
      values(n + 3, p0);
      S.wait<C_M>(3);
      STAMP(24);
      const v2f ml = ldl<v2f>(mla);
      const float mx = fmx(m, ml.x), al = ex2((m - mx) * LOG2E), be = ex2((ml.x - mx) * LOG2E);
      gs16(c.ws, om, v4u{asu(mx), c.tag, asu(sum32(sum16(l * al + be * ml.y))), c.tag});
      v4f y[4];
#pragma unroll
      for (int b2 = 0; b2 < 4; ++b2) { y[b2] = ldl<v4f>(ma + b2 * 1024) * be; asm volatile("" : "+v"(y[b2])); }
      STAMP(25);
      values(n + 4, p1);
      STAMP(5);
#pragma unroll
      for (int b = 0; b < 8; ++b)
        gs16(c.ws, (b < 4 ? o : o2) + (b & 3) * 128,
             v4u{asu(sel(hi, acc[b].z, acc[b].x) * al + y[b >> 1][2 * (b & 1)]), c.tag, asu(sel(hi, acc[b].w, acc[b].y) * al + y[b >> 1][2 * (b & 1) + 1]), c.tag});
      STAMP(6);
    }
  } else {
    __builtin_amdgcn_s_barrier();
    if (role == 0) {
      S.wait<C_FULL>(26);
      take_wo();
    }
    S.wait<C_Q>(4);
    c.tag = rfl(*(volatile LAS u32*)(S.base + 4 * C_TAG));
    if (role == 0) {
      if (w == 4) {
      v4u q[2];
      const u32 xp = *(const GAS u32*)(p.x + 16 * r + 2 * (lane & 7));
      gather16<2, 8>(S, c.ws, W_MOD2 + (64 * ((lane >> 3) & 3) + 32 * (lane >> 5) + (r >> 2)) * 512 + ((r & 3) * 16 + 2 * (lane & 7)) * 8,
                     [](int i) { return (u32)i * 16 * 512; }, q, c.tag);
      float y[4] = {asf(q[0].x), asf(q[0].z), asf(q[1].x), asf(q[1].z)}, o[4];
#pragma unroll
      for (int i = 0; i < 4; ++i) { y[i] += dpp<ROR8>(y[i]); y[i] = rbf(sum16(y[i])); o[i] = sw32(y[i], y[i]); }
      stl<v4f>(L_HS + (lane & 7) * 16, v4f{y[0], o[2], lo16(xp), 0.f});
      stl<v4f>(L_HS + 128 + (lane & 7) * 16, v4f{y[1], o[3], hi16(xp), 0.f});
      gs16(c.ws, lane < 8 ? W_CS + r * 128 + lane * 16 : OOB, v4u{pk(1.f + o[0], y[2]), c.tag, pk(1.f + o[1], y[3]), c.tag});
      }
    } else if (role == 1 && w == 4) {
      v4u q[1];
      gather16<1, 32>(S, c.ws, W_CS + (128 * (wg & 7) + 2 * lane) * 8, [](int) { return 0u; }, q, c.tag);
      stl<v2u>(L_CS + lane * 8, v2u{q[0].x, q[0].z});
      S.set<C_CS>(1);
    }
  }

  if (role == 0) {
    const Masks M = mk_masks();
    const u32 pa = L_PRIV + w * 512;
    const u32 xa = opq(pa + (lane >> 4) * 8), oo = opq(W_A + (tile_of(lane, v) * 64 + r) * 128 + (lane >> 4) * 32 + (lane & 8) * 2);
    const u32 ra = opq(L_QS + lane * 4), wr = opq(pa + lane * 2);
    S.wait<C_FULL>(ntail);
    if (w == 4) { STAMP(7); }
    combine_part(p, c, S);
    if (w < 4) return;
    const float o = sum8(ra);
    stl<u16>(wr, (u16)pk(o, o));
    CBAR();
    const v4u x0 = xfrag(xa, 0), x1 = xfrag(xa, 1);
    gs16(c.ws, oo, tiles8(wa, x0, x1, M, c.tag));
    gs16(c.ws, oo + 4 * 64 * 128, tiles8(wb, x0, x1, M, c.tag));
    if (v == 0) {
      STAMP(9);
      const v4f kp = ldl<v4f>(L_HS + (lane >> 5) * 128 + (lane & 7) * 16);
      const u32 ox = opq(lane < 10 ? W_X + r * 128 + lane * 8 : OOB);
      const u32 ia = opq(W_A + (r * 64 + (lane >> 3)) * 128 + (lane & 7) * 16);
      nap<NAP2>();
      v4u q[8];
      gather16<8, 1>(S, c.ws, ia, [](int i) { return (u32)i * 1024; }, q, c.tag);
      STAMP(10);
      const float a = rbf(kp.z + rbf(kp.x * rbf(col_total(q))));
      float a0 = a;
      const float a1 = sw32(a0, a0);
      const float mu = sum_lo3(a0 + a1) * (1.f / 16.f);
      const float m2 = sum_lo3((a0 - mu) * (a0 - mu) + (a1 - mu) * (a1 - mu));
      gs8(c.ws, ox, v2u{asu(sel(lanes(0xff), asf(pk(a0, a1)), sel(lanes(0x100), mu, m2))), c.tag});
      STAMP(11);
      const u32 id = opq(W_D + (r * 56 + (lane >> 3)) * 128 + (lane & 7) * 16);
      const u32 oy = opq(lane < 8 ? 32 * r + 4 * lane : OOB);
      nap<NAP5>();
      v4u q7[7];
      gather16<7, 2>(S, c.ws, id, [](int i) { return (u32)i * 1024; }, q7, c.tag);
      STAMP(15);
      float y0 = a + rbf(kp.y * rbf(col_total(q7)));
      const float y1 = sw32(y0, y0);
      __builtin_amdgcn_raw_buffer_store_b32(pk(y0, y1), mk(p.out, 2048), oy, 0, 0);
      __builtin_amdgcn_raw_buffer_store_b32(c.tag, c.ws, (r | lane) ? OOB : W_EPOCH, 0, 16);
      STAMP(16);
      if (r == 0) {
        const u64 te = now(), ce = __builtin_amdgcn_s_memtime();
        while (now() - te < 300) {}
        u64 t0 = ~0ull, mx = 0, b[4], cnt = 0;
        for (int i = 0; i < 4; ++i) {
          const u64 a = __hip_atomic_load((GAS u64*)(p.ws + W_STAMP + ((size_t)(lane + 64 * i) * 32) * 8), __ATOMIC_RELAXED, __HIP_MEMORY_SCOPE_AGENT);
          b[i] = __hip_atomic_load((GAS u64*)(p.ws + W_STAMP + ((size_t)(lane + 64 * i) * 32 + (XK < 32 ? XK : 0)) * 8), __ATOMIC_RELAXED, __HIP_MEMORY_SCOPE_AGENT);
          t0 = a < t0 ? a : t0;
        }
        for (int o = 32; o > 0; o >>= 1) { const u64 a = __shfl_xor(t0, o); t0 = a < t0 ? a : t0; }
        for (int i = 0; i < 4; ++i) { const u64 d = b[i] - t0; if (d < 20000) { mx += d; cnt++; } }
        for (int o = 32; o > 0; o >>= 1) { mx += __shfl_xor(mx, o); cnt += __shfl_xor(cnt, o); }
        mx /= (cnt ? cnt : 1);
        u64 val = XK == 100 ? te - t0 : XK == 101 ? (ce - cstart) * 100 / (te - tstart) : XK == 102 ? 10 : mx;
        if (val > 20000) val = 20000;
        while (now() - te < val * 100) {}
      }
    }
  } else if (role == 1) {
    stage_B(p, c, S, lam >= 7, wg & 7, lam % 7, ntail);
  } else if (role == 2) {
    stage_D(p, c, S, tt, ntail);
  }
}

extern "C" void kernel_launch(void* const* d_in, const int* in_sizes, int n_in, void* d_out, int out_size, void* d_ws,
                              size_t ws_size, hipStream_t stream) {
  Params p;
  p.x = (const u16*)d_in[0]; p.K = (const u16*)d_in[1]; p.V = (const u16*)d_in[2];
  p.wq = (const u16*)d_in[3]; p.wo = (const u16*)d_in[4];
  p.wg = (const u16*)d_in[6]; p.wu = (const u16*)d_in[7]; p.wd = (const u16*)d_in[8];
  p.c = (const u16*)d_in[9]; p.wmod = (const u16*)d_in[10];
  p.out = (u16*)d_out; p.ws = (char*)d_ws;
  (void)hipFuncSetAttribute((const void*)fused_block, hipFuncAttributeMaxDynamicSharedMemorySize, 163840);
  hipLaunchKernelGGL(fused_block, dim3(256), dim3(512), 163840, stream, p);
}
```

```cpp
#include <hip/hip_runtime.h>
#include <stdint.h>
#include <type_traits>

typedef unsigned short u16;
typedef unsigned u32;
typedef unsigned long long u64;
typedef u32 v4u __attribute__((ext_vector_type(4)));
typedef u32 v2u __attribute__((ext_vector_type(2)));
typedef float v4f __attribute__((ext_vector_type(4)));
typedef float v2f __attribute__((ext_vector_type(2)));
typedef __bf16 v8bf __attribute__((ext_vector_type(8)));
typedef __bf16 v2bf __attribute__((ext_vector_type(2)));
typedef short v4s __attribute__((ext_vector_type(4)));
#define LAS __attribute__((address_space(3)))
#define GAS __attribute__((address_space(1)))
#define DI __device__ __forceinline__
typedef __amdgpu_buffer_rsrc_t rsrc_t;

#define STAMPS 0
#define ROT 37
#define NAP0A 20
#define NAP0B 28
#define NAP1 1
#define NAP2 16
#define NAPR 10
#define NAP3 14
#define NAP4 48
#define PBF 1
#define T2 150
#define T3 (T2 + 88)
#define T4 (T3 + 108)
#define T5 (T4 + 120)
#define NAP5 22
#define CODEPF 9
#define PACE0 40
#define PUBNAP 0
#define PACE 0
#define DEPTH 24
#define DEPTH3 24
#define DEPTH4 40
#define H2_ 0
#define H2D 8
#define H3D 12
#define H3_ 4
#define DEPTH2 16

constexpr u32 W_EPOCH = 0;
constexpr u32 W_MOD1 = 4096;
constexpr u32 W_Q = W_MOD1 + 256 * 512;
constexpr u32 W_MOD2 = W_Q + 256 * 512;
constexpr u32 W_T0 = 1024;
constexpr u32 W_CS = W_MOD2 + 256 * 512;
constexpr u32 W_X = W_CS + 64 * 128;
constexpr u32 W_ML = W_X + 64 * 128;
constexpr u32 W_GU = W_ML + 32 * 64 * 16;
constexpr u32 W_A = W_GU + 56 * 16 * 512;
constexpr u32 W_D = W_A + 64 * 64 * 128;
constexpr u32 W_P = W_D + 64 * 64 * 128;
constexpr u32 W_MR = W_P + 64 * 64 * 512;
constexpr u32 W_BELL = W_MR + 64 * 64 * 128;
constexpr u32 W_END = W_BELL + 7 * 16 * 128;
constexpr u32 W_STAMP = 8u  __H0__ ;

constexpr u32 NH = 18;
constexpr u32 L_CTRL = NH * 8192;
constexpr u32 L_MRG = L_CTRL + 256;
constexpr u32 L_QS = L_MRG + 4608;
constexpr u32 L_HS = L_QS + 2048;
constexpr u32 L_CS = L_HS + 512;
constexpr u32 L_PRIV = L_CS + 4096;
constexpr u32 L_END = L_PRIV + 4096;
static_assert(L_END <= 163840, "LDS");
constexpr u32 C_FULL = 0, C_H = 2, C_Q = 3, C_M = 4, C_CS = 5, C_TAG = 6, C_W = 7, C_T0 = 8, C_WO = 9, C_FREE = 16;
constexpr float LOG2E = 1.4426950408889634f;

struct Params {
  const u16 *x, *K, *V, *wq, *wo, *wg, *wu, *wd, *c, *wmod;
  u16* out;
  char* ws;
};

template <class T> DI T ldl(u32 a) { return *(LAS T*)a; }
template <class T> DI void stl(u32 a, T v) { *(LAS T*)a = v; }
DI u32 rfl(u32 v) { return __builtin_amdgcn_readfirstlane(v); }
DI float asf(u32 v) { return __builtin_bit_cast(float, v); }
DI u32 asu(float v) { return __builtin_bit_cast(u32, v); }
DI float lo16(u32 v) { return asf(v << 16); }
DI float hi16(u32 v) { return asf(v & 0xffff0000u); }
DI u32 pk(float a, float b) { v2f t = {a, b}; return __builtin_bit_cast(u32, __builtin_convertvector(t, v2bf)); }
DI float rbf(float a) { return hi16(pk(a, a)); }
#define CBAR() asm volatile("" ::: "memory")
#define LGKM0() asm volatile("s_waitcnt lgkmcnt(0)" ::: "memory")
DI v4f mfma(v4u a, v4u b, v4f c) {
  return __builtin_amdgcn_mfma_f32_16x16x32_bf16(__builtin_bit_cast(v8bf, a), __builtin_bit_cast(v8bf, b), c, 0, 0, 0);
}
DI v2u trd(u32 addr) { return __builtin_bit_cast(v2u, __builtin_amdgcn_ds_read_tr16_b64_v4i16((LAS v4s*)addr)); }
DI v4u cat(v2u a, v2u b) { return v4u{a.x, a.y, b.x, b.y}; }
template <int CTRL> DI float dpp(float x) { return asf(__builtin_amdgcn_update_dpp(0, asu(x), CTRL, 0xf, 0xf, true)); }
constexpr int XOR1 = 0xB1, XOR2 = 0x4E, HMIR = 0x141, ROR8 = 0x128;
DI float fmx(float a, float b) { float r; asm("v_max_f32 %0, %1, %2" : "=v"(r) : "v"(a), "v"(b)); return r; }
DI float fmx3(float a, float b, float c) { float r; asm("v_max3_f32 %0, %1, %2, %3" : "=v"(r) : "v"(a), "v"(b), "v"(c)); return r; }
#define DPPMAX(NAME, CTRL) \
  DI float NAME(float x) { float r; asm("s_nop 1\n\tv_max_f32_dpp %0, %1, %1 " CTRL " row_mask:0xf bank_mask:0xf" : "=v"(r) : "v"(x)); return r; }
DPPMAX(max_x1, "quad_perm:[1,0,3,2]") DPPMAX(max_x2, "quad_perm:[2,3,0,1]") DPPMAX(max_hm, "row_half_mirror") DPPMAX(max_r8, "row_ror:8")
DI float sw16(float& a, float b) { auto s = __builtin_amdgcn_permlane16_swap(asu(a), asu(b), false, false); a = asf(s[0]); return asf(s[1]); }
DI float sw32(float& a, float b) { auto s = __builtin_amdgcn_permlane32_swap(asu(a), asu(b), false, false); a = asf(s[0]); return asf(s[1]); }
DI float sum16(float x) { const float o = sw16(x, x); return x + o; }
DI float sum32(float x) { const float o = sw32(x, x); return x + o; }
DI float max16(float x) { const float o = sw16(x, x); return fmx(x, o); }
DI float max32(float x) { const float o = sw32(x, x); return fmx(x, o); }
DI float sum_lo3(float x) { x += dpp<XOR1>(x); x += dpp<XOR2>(x); return x + dpp<HMIR>(x); }
DI float sum64(float x) { x = sum_lo3(x); x += dpp<ROR8>(x); return sum32(sum16(x)); }
DI float sel(bool c, float a, float b) { asm("" : "+v"(a)); asm("" : "+v"(b)); return c ? a : b; }
DI float ex2(float x) { return __builtin_amdgcn_exp2f(x); }
DI float rcp(float x) { return __builtin_amdgcn_rcpf(x); }
DI u32 silu2(u32 a) { const float x = lo16(a), y = hi16(a); return pk(x / (1.f + ex2(-LOG2E * x)), y / (1.f + ex2(-LOG2E * y))); }
DI u32 opq(u32 v) { asm volatile("" : "+v"(v)); return v; }
DI u64 opqs(u64 v) { asm volatile("" : "+s"(v)); return v; }
DI bool lanes(u64 m) { return __builtin_amdgcn_inverse_ballot_w64(m); }
constexpr u64 M_HI8 = 0xff00ff00ff00ff00ull, M_HI32 = 0xffffffff00000000ull;
constexpr u32 OOB = 0x80000000u;

DI u64 now() { return __builtin_amdgcn_s_memrealtime(); }
#define STAMPV(i, val) do {} while (0)
#define STAMP(i) STAMPV(i, now())

#define TS(name) do {} while (0)
#define TSW(i, val) do {} while (0)
struct Sync {
  u32 base;
  template <u32 OFF> DI void wait_at(u32 addr, u32 v) {
    u32 t, s, n;
    asm volatile("s_movk_i32 %2, 0x7fff\n1:\n\tds_read_b32 %0, %3 offset:%c5\n\ts_waitcnt lgkmcnt(0)\n\tv_readfirstlane_b32 %1, %0\n\t"
                 "s_cmp_ge_u32 %1, %4\n\ts_cbranch_scc1 2f\n\ts_sleep 1\n\ts_sub_u32 %2, %2, 1\n\ts_cbranch_scc0 1b\n2:"
                 : "=&v"(t), "=&s"(s), "=&s"(n) : "v"(addr), "s"(v), "i"(OFF) : "memory", "scc");
  }
  template <u32 W> DI void wait(u32 v) { wait_at<4 * W>(base, v); }
  template <u32 W, int N> DI void add1() {
    u64 e;
    asm volatile("s_mov_b64 %0, exec\n\ts_mov_b64 exec, %4\n\tds_add_u32 %1, %2 offset:%c3\n\ts_mov_b64 exec, %0"
                 : "=&s"(e) : "v"(base + (N > 1 ? 4 * (threadIdx.x & 63) : 0)), "v"(1u), "i"(4 * W), "s"((u64)((1ull << N) - 1)) : "memory");
  }
  template <u32 OFF> DI void set_at(u32 addr, u32 v) {
    asm volatile("ds_write_b32 %0, %1 offset:%c2" :: "v"(addr), "v"(v), "i"(OFF) : "memory");
  }
  template <u32 W> DI void set(u32 v) { set_at<4 * W>(base, v); }
};
DI v4u gl16(rsrc_t r, u32 off, u32 so = 0) { return __builtin_amdgcn_raw_buffer_load_b128(r, off, so, 16); }
DI v2u gl8(rsrc_t r, u32 off) { return __builtin_amdgcn_raw_buffer_load_b64(r, off, 0, 16); }
DI void gs16(rsrc_t r, u32 off, v4u v) { __builtin_amdgcn_raw_buffer_store_b128(v, r, off, 0, 16); }
DI void gs8(rsrc_t r, u32 off, v2u v) { __builtin_amdgcn_raw_buffer_store_b64(v, r, off, 0, 16); }
template <int K> DI void nap() { if constexpr (K > 0) __builtin_amdgcn_s_sleep(K); }
DI bool all_ok(u32 bad) { return __builtin_amdgcn_ballot_w64(bad != 0u) == 0ull; }
template <int N, int NAP, class F> DI void gather16(Sync& S, rsrc_t r, u32 base, F f, v4u (&v)[N], u32 tag) {
#pragma unroll 1
  for (u32 n = 0;;) {
    CBAR();
#pragma unroll
    for (int i = 0; i < N; ++i) v[i] = gl16(r, base + (f(i) & 4095u), f(i) & ~4095u);
    u32 bad = 0;
#pragma unroll
    for (int i = 0; i < N; ++i) bad |= (v[i].y ^ tag) | (v[i].w ^ tag);
    if (all_ok(opq(bad)) || ++n > 8192u) break;
    nap<NAP>();
  }
  CBAR();
}

template <int NAP> DI void gather1(rsrc_t r, u32 a, v4u (&v)[1], u32 tag) {
  u64 m[1], o;
  u32 n;
  asm volatile(
               "s_movk_i32 %[n], 0x3fff\n\t"
               "buffer_load_dwordx4 v[224:227], %[a], %[r], 0 offen offset:0 sc1\n\t"
               "1:\n\t"
               "s_waitcnt vmcnt(0)\n\t"
               "v_bitop3_b32 v223, v225, v227, %[t] bitop3:0x7e\n\t"
               "v_cmp_ne_u32 %[m0], 0, v223\n\t"
               "s_or_b64 %[o], %[m0], %[m0]\n\t"
               "s_cbranch_scc0 2f\n\t"
               "s_sleep %c[z]\n\t"
               "s_mov_b64 exec, %[m0]\n\t"
               "s_cbranch_execz 3f\n\t"
               "buffer_load_dwordx4 v[224:227], %[a], %[r], 0 offen offset:0 sc1\n\t"
               "3:\n\t"
               "s_mov_b64 exec, -1\n\t"
               "s_waitcnt vmcnt(0)\n\t"
               "s_sub_u32 %[n], %[n], 1\n\t"
               "s_cbranch_scc0 1b\n\t"
               "2:\n\t"
               : "=&{v[224:227]}"(v[0]), [m0] "=&s"(m[0]), [o] "=&s"(o), [n] "=&s"(n)
               : [a] "v"(a), [r] "s"(r), [t] "s"(tag), [z] "i"(NAP) : "memory", "scc", "v223");
}
template <int NAP> DI void gather2(rsrc_t r, u32 a, u32 b, v4u (&v)[2], u32 tag) {
  u64 m[2], o;
  u32 n;
  asm volatile(
               "s_movk_i32 %[n], 0x3fff\n\t"
               "buffer_load_dwordx4 v[224:227], %[a], %[r], 0 offen sc1\n\t"
               "buffer_load_dwordx4 v[228:231], %[b], %[r], 0 offen sc1\n\t"
               "1:\n\t"
               "s_waitcnt vmcnt(1)\n\t"
               "v_bitop3_b32 v223, v225, v227, %[t] bitop3:0x7e\n\t"
               "v_cmp_ne_u32 %[m0], 0, v223\n\t"
               "s_waitcnt vmcnt(0)\n\t"
               "v_bitop3_b32 v223, v229, v231, %[t] bitop3:0x7e\n\t"
               "v_cmp_ne_u32 %[m1], 0, v223\n\t"
               "s_or_b64 %[o], %[m0], %[m1]\n\t"
               "s_cbranch_scc0 2f\n\t"
               "s_sleep %c[z]\n\t"
               "s_mov_b64 exec, %[m0]\n\t"
               "s_cbranch_execz 3f\n\t"
               "buffer_load_dwordx4 v[224:227], %[a], %[r], 0 offen sc1\n\t"
               "3:\n\t"
               "s_mov_b64 exec, %[m1]\n\t"
               "s_cbranch_execz 3f\n\t"
               "buffer_load_dwordx4 v[228:231], %[b], %[r], 0 offen sc1\n\t"
               "3:\n\t"
               "s_mov_b64 exec, -1\n\t"
               "s_waitcnt vmcnt(0)\n\t"
               "s_sub_u32 %[n], %[n], 1\n\t"
               "s_cbranch_scc0 1b\n\t"
               "2:\n\t"
               : "=&{v[224:227]}"(v[0]), "=&{v[228:231]}"(v[1]), [m0] "=&s"(m[0]), [m1] "=&s"(m[1]), [o] "=&s"(o), [n] "=&s"(n)
               : [a] "v"(a), [b] "v"(b), [r] "s"(r), [t] "s"(tag), [z] "i"(NAP) : "memory", "scc", "v223");
}
template <int NAP> DI void gather4(rsrc_t r, u32 a, v4u (&v)[4], u32 tag) {
  u64 m[4], o;
  u32 n;
  asm volatile(
               "s_movk_i32 %[n], 0x3fff\n\t"
               "buffer_load_dwordx4 v[224:227], %[a], %[r], 0 offen offset:0 sc1\n\t"
               "buffer_load_dwordx4 v[228:231], %[a], %[r], 0 offen offset:128 sc1\n\t"
               "buffer_load_dwordx4 v[232:235], %[a], %[r], 0 offen offset:256 sc1\n\t"
               "buffer_load_dwordx4 v[236:239], %[a], %[r], 0 offen offset:384 sc1\n\t"
               "1:\n\t"
               "s_waitcnt vmcnt(3)\n\t"
               "v_bitop3_b32 v223, v225, v227, %[t] bitop3:0x7e\n\t"
               "v_cmp_ne_u32 %[m0], 0, v223\n\t"
               "s_waitcnt vmcnt(2)\n\t"
               "v_bitop3_b32 v223, v229, v231, %[t] bitop3:0x7e\n\t"
               "v_cmp_ne_u32 %[m1], 0, v223\n\t"
               "s_or_b64 %[o], %[m0], %[m1]\n\t"
               "s_waitcnt vmcnt(1)\n\t"
               "v_bitop3_b32 v223, v233, v235, %[t] bitop3:0x7e\n\t"
               "v_cmp_ne_u32 %[m2], 0, v223\n\t"
               "s_or_b64 %[o], %[o], %[m2]\n\t"
               "s_waitcnt vmcnt(0)\n\t"
               "v_bitop3_b32 v223, v237, v239, %[t] bitop3:0x7e\n\t"
               "v_cmp_ne_u32 %[m3], 0, v223\n\t"
               "s_or_b64 %[o], %[o], %[m3]\n\t"
               "s_cbranch_scc0 2f\n\t"
               "s_sleep %c[z]\n\t"
               "s_mov_b64 exec, %[m0]\n\t"
               "s_cbranch_execz 3f\n\t"
               "buffer_load_dwordx4 v[224:227], %[a], %[r], 0 offen offset:0 sc1\n\t"
               "3:\n\t"
               "s_mov_b64 exec, %[m1]\n\t"
               "s_cbranch_execz 3f\n\t"
               "buffer_load_dwordx4 v[228:231], %[a], %[r], 0 offen offset:128 sc1\n\t"
               "3:\n\t"
               "s_mov_b64 exec, %[m2]\n\t"
               "s_cbranch_execz 3f\n\t"
               "buffer_load_dwordx4 v[232:235], %[a], %[r], 0 offen offset:256 sc1\n\t"
               "3:\n\t"
               "s_mov_b64 exec, %[m3]\n\t"
               "s_cbranch_execz 3f\n\t"
               "buffer_load_dwordx4 v[236:239], %[a], %[r], 0 offen offset:384 sc1\n\t"
               "3:\n\t"
               "s_mov_b64 exec, -1\n\t"
               "s_waitcnt vmcnt(0)\n\t"
               "s_sub_u32 %[n], %[n], 1\n\t"
               "s_cbranch_scc0 1b\n\t"
               "2:\n\t"
               : "=&{v[224:227]}"(v[0]), "=&{v[228:231]}"(v[1]), "=&{v[232:235]}"(v[2]), "=&{v[236:239]}"(v[3]), [m0] "=&s"(m[0]), [m1] "=&s"(m[1]), [m2] "=&s"(m[2]), [m3] "=&s"(m[3]), [o] "=&s"(o), [n] "=&s"(n)
               : [a] "v"(a), [r] "s"(r), [t] "s"(tag), [z] "i"(NAP) : "memory", "scc", "v223");
}
template <int NAP> DI void gather7(rsrc_t r, u32 a, v4u (&v)[7], u32 tag) {
  u64 m[7], o;
  u32 n;
  asm volatile(
               "s_movk_i32 %[n], 0x3fff\n\t"
               "buffer_load_dwordx4 v[224:227], %[a], %[r], 0 offen offset:0 sc1\n\t"
               "buffer_load_dwordx4 v[228:231], %[a], %[r], 0 offen offset:1024 sc1\n\t"
               "buffer_load_dwordx4 v[232:235], %[a], %[r], 0 offen offset:2048 sc1\n\t"
               "buffer_load_dwordx4 v[236:239], %[a], %[r], 0 offen offset:3072 sc1\n\t"
               "buffer_load_dwordx4 v[240:243], %[a], %[r], %[hi] offen offset:0 sc1\n\t"
               "buffer_load_dwordx4 v[244:247], %[a], %[r], %[hi] offen offset:1024 sc1\n\t"
               "buffer_load_dwordx4 v[248:251], %[a], %[r], %[hi] offen offset:2048 sc1\n\t"
               "1:\n\t"
               "s_waitcnt vmcnt(6)\n\t"
               "v_bitop3_b32 v223, v225, v227, %[t] bitop3:0x7e\n\t"
               "v_cmp_ne_u32 %[m0], 0, v223\n\t"
               "s_waitcnt vmcnt(5)\n\t"
               "v_bitop3_b32 v223, v229, v231, %[t] bitop3:0x7e\n\t"
               "v_cmp_ne_u32 %[m1], 0, v223\n\t"
               "s_or_b64 %[o], %[m0], %[m1]\n\t"
               "s_waitcnt vmcnt(4)\n\t"
               "v_bitop3_b32 v223, v233, v235, %[t] bitop3:0x7e\n\t"
               "v_cmp_ne_u32 %[m2], 0, v223\n\t"
               "s_or_b64 %[o], %[o], %[m2]\n\t"
               "s_waitcnt vmcnt(3)\n\t"
               "v_bitop3_b32 v223, v237, v239, %[t] bitop3:0x7e\n\t"
               "v_cmp_ne_u32 %[m3], 0, v223\n\t"
               "s_or_b64 %[o], %[o], %[m3]\n\t"
               "s_waitcnt vmcnt(2)\n\t"
               "v_bitop3_b32 v223, v241, v243, %[t] bitop3:0x7e\n\t"
               "v_cmp_ne_u32 %[m4], 0, v223\n\t"
               "s_or_b64 %[o], %[o], %[m4]\n\t"
               "s_waitcnt vmcnt(1)\n\t"
               "v_bitop3_b32 v223, v245, v247, %[t] bitop3:0x7e\n\t"
               "v_cmp_ne_u32 %[m5], 0, v223\n\t"
               "s_or_b64 %[o], %[o], %[m5]\n\t"
               "s_waitcnt vmcnt(0)\n\t"
               "v_bitop3_b32 v223, v249, v251, %[t] bitop3:0x7e\n\t"
               "v_cmp_ne_u32 %[m6], 0, v223\n\t"
               "s_or_b64 %[o], %[o], %[m6]\n\t"
               "s_cbranch_scc0 2f\n\t"
               "s_sleep %c[z]\n\t"
               "s_mov_b64 exec, %[m0]\n\t"
               "s_cbranch_execz 3f\n\t"
               "buffer_load_dwordx4 v[224:227], %[a], %[r], 0 offen offset:0 sc1\n\t"
               "3:\n\t"
               "s_mov_b64 exec, %[m1]\n\t"
               "s_cbranch_execz 3f\n\t"
               "buffer_load_dwordx4 v[228:231], %[a], %[r], 0 offen offset:1024 sc1\n\t"
               "3:\n\t"
               "s_mov_b64 exec, %[m2]\n\t"
               "s_cbranch_execz 3f\n\t"
               "buffer_load_dwordx4 v[232:235], %[a], %[r], 0 offen offset:2048 sc1\n\t"
               "3:\n\t"
               "s_mov_b64 exec, %[m3]\n\t"
               "s_cbranch_execz 3f\n\t"
               "buffer_load_dwordx4 v[236:239], %[a], %[r], 0 offen offset:3072 sc1\n\t"
               "3:\n\t"
               "s_mov_b64 exec, %[m4]\n\t"
               "s_cbranch_execz 3f\n\t"
               "buffer_load_dwordx4 v[240:243], %[a], %[r], %[hi] offen offset:0 sc1\n\t"
               "3:\n\t"
               "s_mov_b64 exec, %[m5]\n\t"
               "s_cbranch_execz 3f\n\t"
               "buffer_load_dwordx4 v[244:247], %[a], %[r], %[hi] offen offset:1024 sc1\n\t"
               "3:\n\t"
               "s_mov_b64 exec, %[m6]\n\t"
               "s_cbranch_execz 3f\n\t"
               "buffer_load_dwordx4 v[248:251], %[a], %[r], %[hi] offen offset:2048 sc1\n\t"
               "3:\n\t"
               "s_mov_b64 exec, -1\n\t"
               "s_waitcnt vmcnt(0)\n\t"
               "s_sub_u32 %[n], %[n], 1\n\t"
               "s_cbranch_scc0 1b\n\t"
               "2:\n\t"
               : "=&{v[224:227]}"(v[0]), "=&{v[228:231]}"(v[1]), "=&{v[232:235]}"(v[2]), "=&{v[236:239]}"(v[3]), "=&{v[240:243]}"(v[4]), "=&{v[244:247]}"(v[5]), "=&{v[248:251]}"(v[6]), [m0] "=&s"(m[0]), [m1] "=&s"(m[1]), [m2] "=&s"(m[2]), [m3] "=&s"(m[3]), [m4] "=&s"(m[4]), [m5] "=&s"(m[5]), [m6] "=&s"(m[6]), [o] "=&s"(o), [n] "=&s"(n)
               : [a] "v"(a), [r] "s"(r), [t] "s"(tag), [z] "i"(NAP), [hi] "s"(4096u) : "memory", "scc", "v223");
}
template <int NAP> DI void gather8(rsrc_t r, u32 a, v4u (&v)[8], u32 tag) {
  u64 m[8], o;
  u32 n;
  asm volatile(
               "s_movk_i32 %[n], 0x3fff\n\t"
               "buffer_load_dwordx4 v[224:227], %[a], %[r], 0 offen offset:0 sc1\n\t"
               "buffer_load_dwordx4 v[228:231], %[a], %[r], 0 offen offset:1024 sc1\n\t"
               "buffer_load_dwordx4 v[232:235], %[a], %[r], 0 offen offset:2048 sc1\n\t"
               "buffer_load_dwordx4 v[236:239], %[a], %[r], 0 offen offset:3072 sc1\n\t"
               "buffer_load_dwordx4 v[240:243], %[a], %[r], %[hi] offen offset:0 sc1\n\t"
               "buffer_load_dwordx4 v[244:247], %[a], %[r], %[hi] offen offset:1024 sc1\n\t"
               "buffer_load_dwordx4 v[248:251], %[a], %[r], %[hi] offen offset:2048 sc1\n\t"
               "buffer_load_dwordx4 v[252:255], %[a], %[r], %[hi] offen offset:3072 sc1\n\t"
               "1:\n\t"
               "s_waitcnt vmcnt(7)\n\t"
               "v_bitop3_b32 v223, v225, v227, %[t] bitop3:0x7e\n\t"
               "v_cmp_ne_u32 %[m0], 0, v223\n\t"
               "s_waitcnt vmcnt(6)\n\t"
               "v_bitop3_b32 v223, v229, v231, %[t] bitop3:0x7e\n\t"
               "v_cmp_ne_u32 %[m1], 0, v223\n\t"
               "s_or_b64 %[o], %[m0], %[m1]\n\t"
               "s_waitcnt vmcnt(5)\n\t"
               "v_bitop3_b32 v223, v233, v235, %[t] bitop3:0x7e\n\t"
               "v_cmp_ne_u32 %[m2], 0, v223\n\t"
               "s_or_b64 %[o], %[o], %[m2]\n\t"
               "s_waitcnt vmcnt(4)\n\t"
               "v_bitop3_b32 v223, v237, v239, %[t] bitop3:0x7e\n\t"
               "v_cmp_ne_u32 %[m3], 0, v223\n\t"
               "s_or_b64 %[o], %[o], %[m3]\n\t"
               "s_waitcnt vmcnt(3)\n\t"
               "v_bitop3_b32 v223, v241, v243, %[t] bitop3:0x7e\n\t"
               "v_cmp_ne_u32 %[m4], 0, v223\n\t"
               "s_or_b64 %[o], %[o], %[m4]\n\t"
               "s_waitcnt vmcnt(2)\n\t"
               "v_bitop3_b32 v223, v245, v247, %[t] bitop3:0x7e\n\t"
               "v_cmp_ne_u32 %[m5], 0, v223\n\t"
               "s_or_b64 %[o], %[o], %[m5]\n\t"
               "s_waitcnt vmcnt(1)\n\t"
               "v_bitop3_b32 v223, v249, v251, %[t] bitop3:0x7e\n\t"
               "v_cmp_ne_u32 %[m6], 0, v223\n\t"
               "s_or_b64 %[o], %[o], %[m6]\n\t"
               "s_waitcnt vmcnt(0)\n\t"
               "v_bitop3_b32 v223, v253, v255, %[t] bitop3:0x7e\n\t"
               "v_cmp_ne_u32 %[m7], 0, v223\n\t"
               "s_or_b64 %[o], %[o], %[m7]\n\t"
               "s_cbranch_scc0 2f\n\t"
               "s_sleep %c[z]\n\t"
               "s_mov_b64 exec, %[m0]\n\t"
               "s_cbranch_execz 3f\n\t"
               "buffer_load_dwordx4 v[224:227], %[a], %[r], 0 offen offset:0 sc1\n\t"
               "3:\n\t"
               "s_mov_b64 exec, %[m1]\n\t"
               "s_cbranch_execz 3f\n\t"
               "buffer_load_dwordx4 v[228:231], %[a], %[r], 0 offen offset:1024 sc1\n\t"
               "3:\n\t"
               "s_mov_b64 exec, %[m2]\n\t"
               "s_cbranch_execz 3f\n\t"
               "buffer_load_dwordx4 v[232:235], %[a], %[r], 0 offen offset:2048 sc1\n\t"
               "3:\n\t"
               "s_mov_b64 exec, %[m3]\n\t"
               "s_cbranch_execz 3f\n\t"
               "buffer_load_dwordx4 v[236:239], %[a], %[r], 0 offen offset:3072 sc1\n\t"
               "3:\n\t"
               "s_mov_b64 exec, %[m4]\n\t"
               "s_cbranch_execz 3f\n\t"
               "buffer_load_dwordx4 v[240:243], %[a], %[r], %[hi] offen offset:0 sc1\n\t"
               "3:\n\t"
               "s_mov_b64 exec, %[m5]\n\t"
               "s_cbranch_execz 3f\n\t"
               "buffer_load_dwordx4 v[244:247], %[a], %[r], %[hi] offen offset:1024 sc1\n\t"
               "3:\n\t"
               "s_mov_b64 exec, %[m6]\n\t"
               "s_cbranch_execz 3f\n\t"
               "buffer_load_dwordx4 v[248:251], %[a], %[r], %[hi] offen offset:2048 sc1\n\t"
               "3:\n\t"
               "s_mov_b64 exec, %[m7]\n\t"
               "s_cbranch_execz 3f\n\t"
               "buffer_load_dwordx4 v[252:255], %[a], %[r], %[hi] offen offset:3072 sc1\n\t"
               "3:\n\t"
               "s_mov_b64 exec, -1\n\t"
               "s_waitcnt vmcnt(0)\n\t"
               "s_sub_u32 %[n], %[n], 1\n\t"
               "s_cbranch_scc0 1b\n\t"
               "2:\n\t"
               : "=&{v[224:227]}"(v[0]), "=&{v[228:231]}"(v[1]), "=&{v[232:235]}"(v[2]), "=&{v[236:239]}"(v[3]), "=&{v[240:243]}"(v[4]), "=&{v[244:247]}"(v[5]), "=&{v[248:251]}"(v[6]), "=&{v[252:255]}"(v[7]), [m0] "=&s"(m[0]), [m1] "=&s"(m[1]), [m2] "=&s"(m[2]), [m3] "=&s"(m[3]), [m4] "=&s"(m[4]), [m5] "=&s"(m[5]), [m6] "=&s"(m[6]), [m7] "=&s"(m[7]), [o] "=&s"(o), [n] "=&s"(n)
               : [a] "v"(a), [r] "s"(r), [t] "s"(tag), [z] "i"(NAP), [hi] "s"(4096u) : "memory", "scc", "v223");
}

#define ML_FETCH \
      "buffer_load_dwordx4 v[228:231], %[b], %[r], 0 offen sc1\n\t" \
      "buffer_load_dwordx4 v[232:235], %[b], %[r], 0 offen offset:128 sc1\n"
#define ML_CHECK \
      "v_bitop3_b32 v223, v229, v231, %[t] bitop3:0x7e\n\t" \
      "v_bitop3_b32 v222, v233, v235, %[t] bitop3:0x7e\n\t" \
      "v_or_b32 v223, v223, v222\n\t" \
      "v_cmp_ne_u32 vcc, 0, v223\n\t"
DI void gather_ml1(rsrc_t r, u32 a, u32 b, v4u& ml, v4u (&v)[4], u32 tag) {
  u64 P, N, x;
  u32 n;
  asm volatile(
      "s_movk_i32 %[n], 0x3fff\n\ts_mov_b64 %[P], -1\n\ts_mov_b64 %[N], 0\n"
      "1:\n\t"
      "s_mov_b64 exec, %[P]\n\t"
      "buffer_load_dwordx4 v[224:227], %[a], %[r], 0 offen sc1\n\t"
      "s_mov_b64 exec, -1\n\ts_waitcnt vmcnt(0)\n\t"
      ML_CHECK
      "v_bitop3_b32 v221, v225, v227, %[t] bitop3:0x7e\n\t"
      "s_and_b64 %[N], %[N], vcc\n\t"
      "v_cmp_ne_u32 %[x], 0, v221\n\t"
      "s_andn2_b64 vcc, %[P], %[x]\n\t"
      "s_or_b64 %[N], %[N], vcc\n\t"
      "s_mov_b64 exec, %[N]\n\ts_cbranch_execz 3f\n\t"
      ML_FETCH
      "3:\n\t"
      "s_and_b64 %[P], %[P], %[x]\n\t"
      "s_cbranch_scc0 2f\n\t"
      "s_sub_u32 %[n], %[n], 1\n\ts_cbranch_scc0 1b\n"
      "2:\n\ts_mov_b64 exec, -1"
      : "=&{v[224:227]}"(ml), "=&{v[228:231]}"(v[0]), "=&{v[232:235]}"(v[1]), "=&{v[236:239]}"(v[2]), "=&{v[240:243]}"(v[3]),
        [P] "=&s"(P), [N] "=&s"(N), [x] "=&s"(x), [n] "=&s"(n)
      : [a] "v"(a), [b] "v"(b), [r] "s"(r), [t] "s"(tag) : "memory", "scc", "vcc", "v221", "v222", "v223");
}
DI void gather_ml2(rsrc_t r, u32 b, v4u (&v)[4], u32 tag) {
  u32 n;
  asm volatile(
      "s_movk_i32 %[n], 0x3fff\n"
      "1:\n\ts_waitcnt vmcnt(0)\n\t"
      ML_CHECK
      "s_cbranch_vccz 2f\n\t"
      "s_mov_b64 exec, vcc\n\t"
      ML_FETCH
      "\ts_mov_b64 exec, -1\n\ts_sub_u32 %[n], %[n], 1\n\ts_cbranch_scc0 1b\n\ts_waitcnt vmcnt(0)\n"
      "2:"
      : "+{v[228:231]}"(v[0]), "+{v[232:235]}"(v[1]), "+{v[236:239]}"(v[2]), "+{v[240:243]}"(v[3]), [n] "=&s"(n)
      : [b] "v"(b), [r] "s"(r), [t] "s"(tag) : "memory", "scc", "vcc", "v221", "v222", "v223");
}

DI u32 hear(rsrc_t r, u32 line, u32 tag) {
  v2u t;
  u32 n;
  asm volatile("s_movk_i32 %1, 0x3fff\n1:\n\tbuffer_load_dwordx2 v[224:225], %2, %3, 0 offen sc1\n\ts_waitcnt vmcnt(0)\n\t"
               "v_cmp_ne_u32 vcc, %4, v225\n\ts_cbranch_vccz 2f\n\ts_sub_u32 %1, %1, 1\n\ts_cbranch_scc0 1b\n2:"
               : "=&{v[224:225]}"(t), "=&s"(n) : "v"(W_BELL + line * 128), "s"(r), "s"(tag) : "memory", "scc", "vcc");
  return t.x;
}
DI u32 hear4(rsrc_t r, u32 copy, int lane, u32 tag) {
  const u32 t = hear(r, (lane & 3) * 16 + copy, tag);
  u32 m = __builtin_amdgcn_readlane(t, 0);
#pragma unroll
  for (int i = 1; i < 4; ++i) { const u32 x = __builtin_amdgcn_readlane(t, i); m = (int)(x - m) > 0 ? x : m; }
  return m;
}
DI void ring(rsrc_t r, u32 bell, int lane, u32 time, u32 tag) { gs8(r, lane < 16 ? W_BELL + (bell * 16 + lane) * 128 : OOB, v2u{time, tag}); }
DI void until(u32 t) {
  if ((int)(t - (u32)now()) < 1000) while ((int)((u32)now() - t) < 0) {}
}

template <int D> DI void dma4(u32 ld, u32 v0, u32 v1, u32 v2, u32 v3, rsrc_t r, u32 so) {
  asm volatile("s_mov_b32 m0, %0\n\ts_nop 0\n\t"
               "buffer_load_dwordx4 %1, %5, %6 offen nt lds\n\ts_waitcnt vmcnt(%c7)\n\t"
               "buffer_load_dwordx4 %2, %5, %6 offen offset:1024 nt lds\n\ts_waitcnt vmcnt(%c7)\n\t"
               "buffer_load_dwordx4 %3, %5, %6 offen offset:2048 nt lds\n\ts_waitcnt vmcnt(%c7)\n\t"
               "buffer_load_dwordx4 %4, %5, %6 offen offset:3072 nt lds\n\ts_waitcnt vmcnt(%c7)"
               :: "s"(ld), "v"(v0), "v"(v1), "v"(v2), "v"(v3), "s"(r), "s"(so), "i"(D) : "memory");
}
template <int D> DI void dma1(u32 ld, u32 v, rsrc_t r, u32 so) {
  asm volatile("s_mov_b32 m0, %0\n\ts_nop 0\n\tbuffer_load_dwordx4 %1, %2, %3 offen nt lds\n\ts_waitcnt vmcnt(%c4)"
               :: "s"(ld), "v"(v), "s"(r), "s"(so), "i"(D) : "memory");
}
template <int D> DI void vmwait() { asm volatile("s_waitcnt vmcnt(%c0)" :: "i"(D) : "memory"); }
struct Ring {
  Sync S;
  u32 n, pos, need, full, next, lim;
  u64 stall, first;
  DI void publish(u32 f) { if ((int)(f - full) > 0) { full = f; nap<PUBNAP>(); S.set<C_FULL>(f); } }
  DI u32 open() {
    if (n >= NH) {
      const u32 fa = S.base + 4 * pos;
      if (rfl(*(volatile LAS u32*)(fa + 4 * C_FREE)) < need) {
        vmwait<0>();
        publish(n);
        S.wait_at<4 * C_FREE>(fa, need);
      }
    }
    return pos * 8192u;
  }
  template <int D> DI void close() {
    ++n; ++pos;
    if (pos == NH) { pos = 0; need += 4; }
    publish(n - D / 8);
  }
};
DI rsrc_t mk(const void* p, u32 bytes) { return __builtin_amdgcn_make_buffer_rsrc((void*)p, 0, bytes, 0x00020000); }
template <int D> DI void ld_slab(Ring& R, rsrc_t r, u32 base, u32 pitch, int nh, int lane) {
  const u32 row = lane >> 3, v = row * pitch + (((lane & 7) ^ (((row >> 1) & 3) << 1)) << 4);
#pragma unroll 1
  for (int h = 0; h < nh; ++h) {
    const u32 ld = R.open();
#pragma unroll
    for (int i = 0; i < 8; ++i) { dma1<D>(ld + i * 1024, v, r, base); base += 8 * pitch; }
    R.close<D>();
  }
}
template <int D, int D2, int D3> DI void ld_rows(Ring& R, rsrc_t r, u32 base, u32 pitch, int sh, int lane, int H2, int H3) {
  u32 v[8];
#pragma unroll
  for (int i = 0; i < 8; ++i) v[i] = ((lane ^ (i << 1)) << 4) + i * pitch - (i & 3) * 1024;
  int h = 0;
  auto run = [&](auto dd, int end) {
    constexpr int DD = decltype(dd)::value;
#pragma unroll 1
    for (; h < end; ++h) {
      const u32 ld = R.open(), so = base + (h >> sh) * 8 * pitch + (h & sh) * 1024;
      dma4<DD>(ld, v[0], v[1], v[2], v[3], r, so);
      dma4<DD>(ld + 4096, v[4], v[5], v[6], v[7], r, so);
      R.template close<DD>();
    }
  };
  run(std::integral_constant<int, D>{}, H2);
  if constexpr (D2 > 0) run(std::integral_constant<int, D2>{}, H3);
  if constexpr (D3 > 0) run(std::integral_constant<int, D3>{}, 16);
}

DI v4u xfrag(u32 addr, int k) { return cat(ldl<v2u>(addr + k * 64), ldl<v2u>(addr + k * 64 + 32)); }
template <int NK> DI v4f gemv_cols(u32 n0, u32 xaddr, int c, int lane) {
  const u32 g = lane >> 4, r16 = 4 * g + ((lane & 15) >> 2);
  const u32 a = opq(n0 * 8192 + r16 * 128 + ((c ^ ((r16 >> 1) & 3)) << 5) + (lane & 3) * 8), xa = opq(xaddr + g * 8);
  v4f acc = {0.f, 0.f, 0.f, 0.f};
#pragma unroll
  for (int k = 0; k < NK; ++k) acc = mfma(xfrag(xa, k), cat(trd(a + k * 4096), trd(a + k * 4096 + 2048)), acc);
  return acc;
}
DI void put_tile(rsrc_t ws, u32 off, v4f acc, u32 tag, int lane) {
  gs8(ws, lane < 16 ? off + lane * 8 : OOB, v2u{asu(acc.x), tag});
}
DI u32 hs_addr(u32 n) { return (n % NH) * 8192u; }
template <int SH, int O = 0, int NW = 16> DI void read_rows(u32 n0, int v, int lane, v4u (&wf)[NW]) {
  const u32 g = lane >> 4, r8 = (4 * g + ((lane & 15) >> 2)) & 7;
  const u32 lo = r8 * 1024 + ((v ^ r8) << 5) + (lane & 3) * 8;
#pragma unroll
  for (int q = 0; q < 4; ++q) {
    const int half = SH ? q >> 1 : 0, k = SH ? q & 1 : q;
    u32 a[2];
#pragma unroll
    for (int t = 0; t < 2; ++t) {
      const u32 h0 = hs_addr(n0 + ((4 * k + 2 * t) << SH) + half), h1 = hs_addr(n0 + ((4 * k + 2 * t + 1) << SH) + half);
      a[t] = lo + (lanes(M_HI32) ? h1 : h0);
    }
#pragma unroll
    for (int u = 0; u < 4; ++u) wf[O + (SH ? (half * 4 + u) * 2 + k : 4 * k + u)] = cat(trd(a[0] + u * 256), trd(a[1] + u * 256));
  }
}
DI float pick4(float a0, float a1, float a2, float a3) {
  const bool odd = lanes(0xffff0000ffff0000ull);
  return sel(lanes(M_HI32), sel(odd, a3, a2), sel(odd, a1, a0));
}
template <int N> DI void put_tiles(rsrc_t ws, const u32 (&off)[N / 4], const v4u (&wf)[2 * N], v4u x0, v4u x1, u32 tag) {
  v4f acc[N];
#pragma unroll
  for (int i = 0; i < N; ++i) acc[i] = mfma(x0, wf[2 * i], v4f{0.f, 0.f, 0.f, 0.f});
  __builtin_amdgcn_sched_barrier(0);
#pragma unroll
  for (int q = 0; q <= N / 4; ++q) {
    if (q < N / 4) {
#pragma unroll
      for (int i = 4 * q; i < 4 * q + 4; ++i) acc[i] = mfma(x1, wf[2 * i + 1], acc[i]);
    }
    if (q > 0) gs8(ws, off[q - 1], v2u{asu(pick4(acc[4 * q - 4].x, acc[4 * q - 3].x, acc[4 * q - 2].x, acc[4 * q - 1].x)), tag});
    __builtin_amdgcn_sched_barrier(0);
  }
}

struct Ctx { rsrc_t ws; u32 tag; int lane, w, wg, grp, s; };

struct XIn { v4u xa, xb; u32 xp; };
DI XIn load_x(const Params& p, const Ctx& c) {
  return XIn{*(const GAS v4u*)(p.x + c.lane * 16), *(const GAS v4u*)(p.x + c.lane * 16 + 8),
             *(const GAS u32*)(p.x + 256 * (c.wg >> 6) + 64 * c.w + 2 * (c.lane & 31))};
}
DI float make_ln(const XIn& X) {
  const v4u xa = X.xa, xb = X.xb;
  float sm = 0.f;
#pragma unroll
  for (int i = 0; i < 4; ++i) sm += (lo16(xa[i]) + hi16(xa[i])) + (lo16(xb[i]) + hi16(xb[i]));
  const float mean = sum64(sm) * (1.f / 1024.f);
  float q = 0.f;
#pragma unroll
  for (int i = 0; i < 4; ++i) {
    const float a = lo16(xa[i]) - mean, b = hi16(xa[i]) - mean, d = lo16(xb[i]) - mean, e = hi16(xb[i]) - mean;
    q += (a * a + b * b) + (d * d + e * e);
  }
  const float var = sum64(q) * (1.f / 1024.f);
  const float mb = rbf(mean), den = rbf(sqrtf(rbf(rbf(var) + 1e-5f)));
  return asf(opq(asu(rbf(rbf((lanes(M_HI32) ? hi16(X.xp) : lo16(X.xp)) - mb) / den))));
}
DI void make_h(const Params& p, const Ctx& c, Sync& S, float ln) {
  const int lane = c.lane, kq = c.wg >> 6;
  const u32 base = opq(W_MOD1 + (32 * (lane >> 5) + 4 * kq + c.w) * 512 + (lane & 31) * 16);
  const u32 ha = opq(L_HS + (64 * c.w + 2 * (lane & 31) + (lane >> 5)) * 2);
  v4u v[8];
  nap<NAP0A>();
  gather16<8, 4>(S, c.ws, base, [](int i) { return (u32)(64 * (i & 3) + 16 * (i >> 2)) * 512; }, v, c.tag);
  if (c.w == 0) { STAMP(17); }
  float s0 = (asf(v[0].x) + asf(v[1].x)) + (asf(v[2].x) + asf(v[3].x)), c0 = (asf(v[4].x) + asf(v[5].x)) + (asf(v[6].x) + asf(v[7].x));
  s0 += sw32(s0, (asf(v[0].z) + asf(v[1].z)) + (asf(v[2].z) + asf(v[3].z)));
  c0 += sw32(c0, (asf(v[4].z) + asf(v[5].z)) + (asf(v[6].z) + asf(v[7].z)));
  const float h = rbf(ln * rbf(1.f + rbf(c0))) + rbf(s0);
  stl<u16>(ha, (u16)pk(h, h));
  S.add1<C_H, 1>();
}
DI void make_q(const Params& p, const Ctx& c, Sync& S) {
  const int lane = c.lane;
  v4u v[8];
  const u32 base = W_Q + (16 * c.grp + 4 * c.w + (lane >> 5)) * 512 + (lane & 31) * 16;
  gather16<8, 4>(S, c.ws, base, [](int i) { return (u32)(64 * (i & 3) + 2 * (i >> 2)) * 512; }, v, c.tag);
#pragma unroll
  for (int i = 0; i < 8; i += 4)
    stl<u32>(L_QS + (128 * c.w + 16 * i + lane) * 4,
             pk((asf(v[i].x) + asf(v[i + 1].x)) + (asf(v[i + 2].x) + asf(v[i + 3].x)), (asf(v[i].z) + asf(v[i + 1].z)) + (asf(v[i + 2].z) + asf(v[i + 3].z))));
  S.add1<C_Q, 1>();
}

DI void combine_part(const Params& p, const Ctx& c, Sync& S) {
  const int lane = c.lane, a = 16 * c.grp + c.s, sl = 8 * c.w + (lane >> 3);
  const u32 base = opq(W_P + (a * 64 + sl) * 512 + (lane & 7) * 16), mla = opq(W_MR + (a * 64 + sl) * 128);
  const int dim = 2 * (lane & 7) + 32 * (lane >> 5) + 16 * ((lane >> 4) & 1) + ((lane >> 3) & 1);
  const u32 ra = opq(L_QS + (c.w * 64 + dim) * 4), wa = opq(L_CS + sl * 8), wb = opq(L_CS + lane * 8);
  v4u ml, v[4];
  gather_ml1(c.ws, mla, base, ml, v, c.tag);
  stl<v2u>(wa, v2u{ml.x, ml.z});
  LGKM0();
  __builtin_amdgcn_s_barrier();
  CBAR();
  const u32 tb = (u32)now();
  const v2f all = ldl<v2f>(wb);
  const float M = max32(max16(max_r8(max_hm(max_x2(max_x1(all.x))))));
  const float wt = asf(opq(asu(ex2((asf(ml.x) - M) * LOG2E) * rcp(sum64(ex2((all.x - M) * LOG2E) * all.y)))));
  gather_ml2(c.ws, base, v, c.tag);
  TS(t8);
  if (c.w == 1 && c.s == 0) ring(c.ws, c.grp, lane, tb, c.tag);
  STAMP(8);
  float z0 = lo16(v[0].x) * wt, z1 = hi16(v[0].x) * wt, z2 = lo16(v[0].z) * wt, z3 = hi16(v[0].z) * wt;
  const float y0 = lo16(v[1].x) * wt, y1 = hi16(v[1].x) * wt, y2 = lo16(v[1].z) * wt, y3 = hi16(v[1].z) * wt;
  z0 += sw32(z0, y0); z1 += sw32(z1, y1); z2 += sw32(z2, y2); z3 += sw32(z3, y3);
  z0 += sw16(z0, z2); z1 += sw16(z1, z3);
  const bool up = lanes(M_HI8);
  stl<float>(ra, sel(up, z1, z0) + dpp<ROR8>(sel(up, z0, z1)));
  LGKM0();
  __builtin_amdgcn_s_barrier();
  CBAR();
}
DI float sum8(u32 a) {
  float r[8];
#pragma unroll
  for (int i = 0; i < 8; ++i) r[i] = ldl<float>(a + i * 256);
  return ((r[0] + r[1]) + (r[2] + r[3])) + ((r[4] + r[5]) + (r[6] + r[7]));
}

DI void stage_B(const Params& p, const Ctx& c, Sync& S, int m, int kb, int cb, u32 n0) {
  const int lane = c.lane, w = c.w;
  S.wait<C_FULL>(n0 + 16);
  v4u wf[16];
  read_rows<0>(n0, w, lane, wf);
  const int tile = w + 8 * (lane >> 4);
  const u32 xa = opq(L_PRIV + (lane >> 4) * 8);
  const u32 oo = opq(W_GU + ((8 * cb + (tile >> 2)) * 16 + 2 * kb + m) * 512 + (tile & 3) * 128 + (lane & 15) * 8);
  if (w == 4) {
    const v2u cs = ldl<v2u>(L_CS + lane * 8);
    const int P = 64 * kb + lane;
    const u32 pa = opq(L_PRIV + lane * 4);
    const u32 o0 = opq(W_X + (P & ~1) * 8), o1 = opq(W_X + 4096 + lane * 16);
    LGKM0();
    TS(t12);
    STAMP(12);
    until(rfl(hear(c.ws, 4 * 16 + ((c.wg >> 3) & 15), c.tag)) + T3);
    v4u q[2];
    gather2<1>(c.ws, o1, o0, q, c.tag);
    TS(t13);
    STAMP(13);
    const u32 xg = asu(sel(lanes(0xaaaaaaaaaaaaaaaaull), asf(q[1].z), asf(q[1].x)));
    const float mu0 = asf(rfl(q[0].x));
    float d = asf(q[0].x) - mu0;
    d += sw32(d, asf(q[0].z) + 16.f * d * d);
    d = sum_lo3(d); d += dpp<ROR8>(d); d = sum16(d);
    const float e = sw32(d, d) * (1.f / 1024.f);
    d *= 1.f / 64.f;
    const float mb = rbf(mu0 + d), inv = rcp(rbf(__builtin_amdgcn_sqrtf(rbf(rbf(fmaxf(e - d * d, 0.f)) + 1e-5f))));
    const u32 xm = pk(lo16(xg) - mb, hi16(xg) - mb), ln = pk(lo16(xm) * inv, hi16(xm) * inv);
    const u32 sc = pk(lo16(ln) * lo16(cs.x), hi16(ln) * lo16(cs.y));
    stl<u32>(pa, pk(lo16(sc) + hi16(cs.x), hi16(sc) + hi16(cs.y)));
  }
  LGKM0();
  __builtin_amdgcn_s_barrier();
  CBAR();
  v4f acc[4];
#pragma unroll
  for (int u = 0; u < 4; ++u) acc[u] = v4f{0.f, 0.f, 0.f, 0.f};
#pragma unroll
  for (int k = 0; k < 4; ++k) {
    const v4u xf = xfrag(xa, k);
#pragma unroll
    for (int u = 0; u < 4; ++u) acc[u] = mfma(xf, wf[k * 4 + u], acc[u]);
  }
  gs8(c.ws, oo, v2u{asu(pick4(acc[0].x, acc[1].x, acc[2].x, acc[3].x)), c.tag});
  TS(t14);
  STAMP(14);
}

DI void stage_D(const Params& p, const Ctx& c, Sync& S, int t, u32 n0) {
  const int lane = c.lane, w = c.w;
  S.wait<C_FULL>(n0 + 16);
  v4u wf[16];
  read_rows<1>(n0, w, lane, wf);
  const u32 pa = opq(L_PRIV + w * 512 + lane * 2), xa = opq(L_PRIV + w * 512 + (lane >> 4) * 8);
  const u32 oi = opq(W_GU + (t * 16 + 2 * w) * 512 + lane * 16);
  const u32 wa = opq(L_CS + w * 512 + (lane & 31) * 16 + (lane >> 5) * 4), ra = opq(L_CS + lane * 8);
  const u32 oo = opq(W_D + ((w + 8 * (lane >> 4)) * 56 + t) * 128 + (lane & 15) * 8);
  const u32 of[2] = {oo, opq(oo + 32 * 56 * 128)};
  LGKM0();
  TS(t27);
  STAMP(27);
  if (w == 4) until(rfl(hear(c.ws, 5 * 16 + (t & 15), c.tag)) + T4);
  __builtin_amdgcn_s_barrier();
  v4u q[1];
  gather1<2>(c.ws, oi, q, c.tag);
  TS(t28);
  STAMP(28);
  stl<u32>(wa, q[0].x);
  stl<u32>(wa + 8, q[0].z);
  LGKM0();
  __builtin_amdgcn_s_barrier();
  CBAR();
  v2f r[8];
#pragma unroll
  for (int i = 0; i < 8; ++i) r[i] = ldl<v2f>(ra + i * 512);
  const v2f y = ((r[0] + r[1]) + (r[2] + r[3])) + ((r[4] + r[5]) + (r[6] + r[7]));
  const u32 gu = pk(y.x, y.y);
  const float g = lo16(gu);
  const float z = (-2.f * LOG2E * 0.7978845608028654f) * (g + 0.044715f * g * g * g);
  const float h = rbf(g * rcp(1.f + ex2(z))) * hi16(gu);
  stl<u16>(pa, (u16)pk(h, h));
  CBAR();
  put_tiles<8>(c.ws, of, wf, xfrag(xa, 0), xfrag(xa, 1), c.tag);
  TS(t29);
  STAMP(29);
}

struct Att {
  v4u qf[4];
  v4f acc[8];
  float m, l;
  u32 kb, vb, ma, mla;
  DI void init(int lane) {
    const u32 g = lane >> 4, j = lane & 15, r16 = 4 * g + (j >> 2);
    kb = j * 256 + ((g ^ j) << 4);
    vb = r16 * 256 + ((r16 & 7) << 5) + (lane & 3) * 8;
    ma = opq(L_MRG + lane * 16); mla = opq(L_MRG + 4096 + lane * 8);
    m = -INFINITY; l = 0.f;
#pragma unroll
    for (int b = 0; b < 8; ++b) acc[b] = v4f{0.f, 0.f, 0.f, 0.f};
  }
  DI void load_q(int lane) {
#pragma unroll
    for (int k = 0; k < 4; ++k) qf[k] = ldl<v4u>(L_QS + (lane & 7) * 256 + k * 64 + (lane >> 4) * 16);
  }
  DI static void release(Sync& S, u32 n) { S.set_at<4 * C_FREE>(S.base + 4 * (n % NH), n / NH * 4 + 4); }
  DI void read_k(Sync& S, u32 n, v4u (&kf)[8]) {
    const u32 ka = n % NH * 8192;
    S.wait<C_FULL>(n + 1);
#pragma unroll
    for (int k = 0; k < 4; ++k) { const u32 a = ka + (kb ^ (k << 6)); kf[k] = ldl<v4u>(a); kf[4 + k] = ldl<v4u>(a + 4096); }
    release(S, n);
  }
  DI void read_v(Sync& S, u32 n, v4u (&vf)[8]) {
    u32 a[8];
#pragma unroll
    for (int b = 0; b < 8; ++b) a[b] = opq(n % NH * 8192 + (vb ^ (b << 5)));
    S.wait<C_FULL>(n + 1);
#pragma unroll
    for (int b = 0; b < 8; ++b) vf[b] = cat(trd(a[b]), trd(a[b] + 4096));
    release(S, n);
  }
  DI float scores(const v4u (&kf)[8], float (&sv)[8]) {
    v4f s0 = {0.f, 0.f, 0.f, 0.f}, s1 = s0;
#pragma unroll
    for (int k = 0; k < 4; ++k) { s0 = mfma(kf[k], qf[k], s0); s1 = mfma(kf[4 + k], qf[k], s1); }
#pragma unroll
    for (int i = 0; i < 4; ++i) {
      const u32 a = i < 2 ? pk(s0[2 * i], s0[2 * i + 1]) : pk(s1[2 * i - 4], s1[2 * i - 3]);
      const u32 b = pk(lo16(a) * 0.08838834764831845f, hi16(a) * 0.08838834764831845f);
      sv[2 * i] = lo16(b); sv[2 * i + 1] = hi16(b);
    }
    return fmx3(fmx3(sv[0], sv[1], sv[2]), fmx3(sv[3], sv[4], sv[5]), fmx(sv[6], sv[7]));
  }
  DI void rescale(float mx) {
    mx = fmx(m, max32(max16(mx)));
    if (__builtin_amdgcn_ballot_w64(mx > m) != 0ull) {
      const float al = ex2((m - mx) * LOG2E);
      l *= al;
#pragma unroll
      for (int b = 0; b < 8; ++b) acc[b] *= al;
      m = mx;
    }
  }
  DI v4u probs(const float (&sv)[8]) {
    float e[8];
#pragma unroll
    for (int i = 0; i < 8; ++i) e[i] = ex2((sv[i] - m) * LOG2E);
    l += ((e[0] + e[1]) + (e[2] + e[3])) + ((e[4] + e[5]) + (e[6] + e[7]));
    return v4u{pk(e[0], e[1]), pk(e[2], e[3]), pk(e[4], e[5]), pk(e[6], e[7])};
  }
  DI void pv(const v4u (&vf)[8], v4u pf) {
#pragma unroll
    for (int b = 0; b < 8; ++b) acc[b] = mfma(vf[b], pf, acc[b]);
  }
  DI void hand_on(Sync& S, u32 order) {
    if (order > 0) {
      S.wait<C_M>(order);
      const v2f ml = ldl<v2f>(mla);
      const float mx = fmx(m, ml.x), al = ex2((m - mx) * LOG2E), be = ex2((ml.x - mx) * LOG2E);
      l = l * al + be * ml.y;
      m = mx;
#pragma unroll
      for (int b2 = 0; b2 < 4; ++b2) {
        const v4f o = ldl<v4f>(ma + b2 * 1024) * be;
        acc[2 * b2] = acc[2 * b2] * al + v4f{o.x, o.y, o.x, o.y};
        acc[2 * b2 + 1] = acc[2 * b2 + 1] * al + v4f{o.z, o.w, o.z, o.w};
      }
    }
    const bool hi = lanes(M_HI8);
#pragma unroll
    for (int b2 = 0; b2 < 4; ++b2) {
      const v4f a = acc[2 * b2], b = acc[2 * b2 + 1];
      stl<v4f>(ma + b2 * 1024, v4f{sel(hi, a.z, a.x), sel(hi, a.w, a.y), sel(hi, b.z, b.x), sel(hi, b.w, b.y)});
    }
    stl<v2f>(mla, v2f{m, l});
    S.set<C_M>(order + 1);
  }
};

template <int N> DI float col_total(const v4u (&q)[N]) {
  float s0 = asf(q[0].x), s1 = asf(q[0].z);
#pragma unroll
  for (int i = 1; i < N; ++i) { s0 += asf(q[i].x); s1 += asf(q[i].z); }
  s0 += sw32(s0, s1);
  s0 = sum16(s0);
  return s0 + dpp<ROR8>(s0);
}

__global__ void __launch_bounds__(512) fused_block(Params p) {
  const u64 pc0 = __builtin_amdgcn_s_getpc() & ~255ull;
  constexpr u32 CODE_BYTES = 36864;
  const int tid = threadIdx.x, lane = tid & 63, w = rfl(tid >> 6), wg = blockIdx.x;
  const int grp = (wg >> 3) & 3, s = (wg & 7) + ((wg >> 5) << 3);
  const bool isA = s < 16;
  const int lam = (wg >> 3) - 8;
  const int role = isA ? 0 : lam < 14 ? 1 : lam < 21 ? 2 : 3;
  const int tt = (wg & 7) + 8 * (lam % 7), r = 16 * grp + s, v = w & 3;
  const u32 nkv = isA ? 26 : 10, NT = isA ? 26 : 34, ntail = nkv + 2 * NT;
  Sync S{opq(L_CTRL)};
  if (w == 0) { STAMP(0); S.set_at<0>(S.base + 4 * lane, 0u); }
  Ctx c;
  c.ws = mk(p.ws, W_END);
  c.lane = lane; c.w = w; c.wg = wg; c.grp = grp; c.s = s;
  v4u wa[32];
  auto take_wo = [&]() {
    read_rows<1, 0, 32>(10, v, lane, wa);
    read_rows<1, 16, 32>(10, v + 4, lane, wa);
    S.add1<C_FREE + 10, 8>();
    S.add1<C_FREE, 8>();
  };

  if (w == 7) {
    __builtin_amdgcn_s_setprio(3);
    __builtin_amdgcn_s_waitcnt(0x0F70);
    Ring R{S, 0, 0, 0, 0, 0, ntail, 0, 0};
    const rsrc_t rm = mk(p.wmod, 1024 * 6144 * 2);
    ld_slab<DEPTH>(R, rm, (wg >> 5) * 128 * 12288 + (wg & 31) * 128, 12288, 2, lane);
    __builtin_amdgcn_s_barrier();
    ld_slab<DEPTH>(R, mk(p.wq, 1024 * 4096 * 2), (wg >> 6) * 256 * 8192 + (wg & 63) * 128, 8192, 4, lane);
    ld_slab<DEPTH>(R, rm, (wg >> 6) * 256 * 12288 + 4096 + (wg & 63) * 128, 12288, 4, lane);
    if (isA) ld_rows<DEPTH, 0, 0>(R, mk(p.wo, 4096 * 1024 * 2), r * 64 * 2048, 2048, 1, lane, 16, 16);
    const rsrc_t rk = mk(p.K, 1u << 26), rv = mk(p.V, 1u << 26);
    const u32 rw = lane >> 4, ps = lane & 15;
    u32 vk[4], vv[2];
#pragma unroll
    for (int i = 0; i < 4; ++i) vk[i] = rw * 256 + ((ps ^ (4 * i + rw)) << 4);
#pragma unroll
    for (int i = 0; i < 2; ++i) vv[i] = rw * 256 + ((ps ^ ((4 * i + rw) << 1)) << 4);
    auto tile_off = [&](int tau) { return ((u32)grp << 24) + (((tau < 8 ? tau * 48 + s - 16 : 384 + (tau - 8) * 64 + s) + ROT * grp) & 2047) * 8192; };
    auto ldk = [&](int tau) {
      const u32 so = tile_off(tau), ld = R.open();
      dma4<DEPTH>(ld, vk[0], vk[1], vk[2], vk[3], rk, so);
      dma4<DEPTH>(ld + 4096, vk[0], vk[1], vk[2], vk[3], rk, so + 4096);
      R.close<DEPTH>();
    };
    auto ldv = [&](int tau) {
      const u32 so = tile_off(tau), ld = R.open();
      dma4<DEPTH>(ld, vv[0], vv[1], vv[0], vv[1], rv, so);
      dma4<DEPTH>(ld + 4096, vv[0], vv[1], vv[0], vv[1], rv, so + 4096);
      R.close<DEPTH>();
    };
    int tau = isA ? 8 : 0;
    if (isA) {
      ldk(8); ldv(8);
      vmwait<16>();
      R.publish(26);
      tau = 9;
      take_wo();
    }
#pragma unroll 1
    for (; tau < 31; ++tau) { ldk(tau); ldv(tau); }
#pragma unroll 1
    for (; tau < 34; ++tau) ldk(tau);
#pragma unroll 1
    for (tau = 31; tau < 34; ++tau) ldv(tau);
    STAMP(3);
    if (role == 1 || role == 2) {
      const bool gu = role == 1;
      ld_rows<DEPTH2, DEPTH3, DEPTH4>(R, mk(gu ? (lam < 7 ? p.wg : p.wu) : p.wd, 1024 * 3584 * 2),
                                                gu ? (wg & 7) * 128 * 7168 + (lam % 7) * 1024 : tt * 64 * 2048, gu ? 7168 : 2048, gu ? 0 : 1, lane, gu ? H2_ : H2D, gu ? H3_ : H3D);
    }
    vmwait<32>(); R.publish(R.n - 4);
    vmwait<24>(); R.publish(R.n - 3);
    vmwait<16>(); R.publish(R.n - 2);
    vmwait<8>(); R.publish(R.n - 1);
    vmwait<0>(); R.publish(R.n);
    STAMP(4);
    STAMPV(22, R.first);
    STAMPV(31, R.stall + 1);
    __builtin_amdgcn_s_setprio(0);
    c.tag = rfl(*(volatile LAS u32*)(S.base + 4 * C_TAG));
  } else if (w < 4) {
    const u32 ep = __hip_atomic_load((GAS u32*)(p.ws + W_EPOCH), __ATOMIC_RELAXED, __HIP_MEMORY_SCOPE_AGENT);
    const u32 c1 = *(const GAS u32*)(p.c + (wg >> 5) * 128 + 2 * lane);
    const XIn X = load_x(p, c);
    const u32 pa = L_PRIV + w * 256;
    LGKM0();
    __builtin_amdgcn_s_barrier();
    CBAR();
    stl<u32>(pa + lane * 4, silu2(c1));
    c.tag = rfl(ep) + 1u;
    S.set<C_TAG>(c.tag);
    S.wait<C_FULL>(2);
    put_tile(c.ws, W_MOD1 + wg * 512 + w * 128, gemv_cols<4>(0, pa, w, lane), c.tag, lane);
    S.add1<C_FREE, 2>();
    if (w == 0) { STAMP(1); }
    make_h(p, c, S, make_ln(X));
    S.wait<C_FULL>(6);
    S.wait<C_H>(4);
    if (w == 0) { STAMP(18); }
    put_tile(c.ws, W_Q + wg * 512 + w * 128, gemv_cols<8>(2, L_HS, w, lane), c.tag, lane);
    S.add1<C_FREE + 2, 4>();
    if (w == 0) { STAMP(19); }
    nap<NAP0B>();
    make_q(p, c, S);
    S.wait<C_Q>(4);
    if (w == 0) { STAMP(2); }
    Att A;
    A.init(lane);
    A.load_q(lane);
    const u32 it0 = isA ? 0 : 6, ord0 = isA ? 0 : 3;
    const u32 order = (w + it0 - NT + 1) & 3;
#pragma unroll 1
    for (u32 it = it0 + w; it < NT - 2; it += 4) {
      v4u f[8];
      float sv[8];
      A.read_k(S, nkv + 2 * it, f);
      A.rescale(A.scores(f, sv));
      const v4u pf = A.probs(sv);
      A.read_v(S, nkv + 2 * it + (it == NT - 3 ? 3 : 1), f);
      A.pv(f, pf);
    }
    if (order < 3) {
      A.hand_on(S, ord0 + order);
    } else {
      const u32 n = ntail - 5, g = lane >> 4, j = lane & 15;
      const bool hi = lanes(M_HI8);
      v4u f[8];
      float sa[8], sb[8];
      A.read_k(S, n, f);
      const float mxa = A.scores(f, sa);
      A.read_k(S, n + 1, f);
      A.rescale(fmx(mxa, A.scores(f, sb)));
      const v4u p0 = A.probs(sa), p1 = A.probs(sb);
      const u32 o = opq(W_P + ((16 * grp + 2 * (j & 7)) * 64 + s) * 512 + g * 32 + (j & 8) * 2), o2 = opq(o + 64 * 512);
      const u32 om = opq(lane < 16 ? W_MR + ((16 * grp + 2 * (lane & 7) + (lane >> 3)) * 64 + s) * 128 : OOB);
      STAMP(23);
      A.read_v(S, n + 3, f);
      A.pv(f, p0);
      S.wait<C_M>(ord0 + 3);
      STAMP(24);
      const v2f ml = ldl<v2f>(A.mla);
      const float mx = fmx(A.m, ml.x), al = ex2((A.m - mx) * LOG2E), be = ex2((ml.x - mx) * LOG2E);
      TS(t25);
      gs16(c.ws, om, v4u{asu(mx), c.tag, asu(sum32(sum16(A.l * al + be * ml.y))), c.tag});
      v4f y[4];
#pragma unroll
      for (int b2 = 0; b2 < 4; ++b2) { y[b2] = ldl<v4f>(A.ma + b2 * 1024) * be; asm volatile("" : "+v"(y[b2])); }
      STAMP(25);
      A.read_v(S, n + 4, f);
      A.pv(f, p1);
      STAMP(5);
#pragma unroll
      for (int b = 0; b < 8; b += 2)
        gs16(c.ws, (b < 4 ? o : o2) + (b & 2) * 64,
             v4u{pk(sel(hi, A.acc[b].z, A.acc[b].x) * al + y[b >> 1].x, sel(hi, A.acc[b].w, A.acc[b].y) * al + y[b >> 1].y), c.tag,
                 pk(sel(hi, A.acc[b + 1].z, A.acc[b + 1].x) * al + y[b >> 1].z, sel(hi, A.acc[b + 1].w, A.acc[b + 1].y) * al + y[b >> 1].w), c.tag});
      TS(t6);
      TSW(25, t25); TSW(6, t6);
      STAMP(6);
    }
  } else {
    if (w == 6) {
      const u32 line = ((wg >> 3) * CODEPF + lane) * 128;
      u32 t;
      asm volatile("buffer_load_dword %0, %1, %2, 0 offen\n\ts_waitcnt vmcnt(0)" : "=&v"(t) : "v"(lane < CODEPF ? line : OOB), "s"(mk((const void*)pc0, CODE_BYTES)) : "memory");
    }
    const u32 ep = __hip_atomic_load((GAS u32*)(p.ws + W_EPOCH), __ATOMIC_RELAXED, __HIP_MEMORY_SCOPE_AGENT);
    const v2u c2 = *(const GAS v2u*)(p.c + (wg >> 6) * 256 + 4 * lane);
    const u32 pa = L_PRIV + 1024 + v * 512;
    stl<v2u>(pa + lane * 8, v2u{silu2(c2.x), silu2(c2.y)});
    __builtin_amdgcn_s_barrier();
    c.tag = rfl(ep) + 1u;
    S.wait<C_FULL>(10);
    put_tile(c.ws, W_MOD2 + wg * 512 + v * 128, gemv_cols<8>(6, pa, v, lane), c.tag, lane);
    if (v == 0) { put_tile(c.ws, W_MOD2 + wg * 512 + 3 * 128, gemv_cols<8>(6, pa, 3, lane), c.tag, lane); S.add1<C_FREE + 6, 4>(); }
    S.add1<C_FREE + 6, 4>();
    if (role == 0) {
      S.wait<C_FULL>(26);
      take_wo();
      S.wait<C_Q>(4);
    } else {
      Att A;
      A.init(lane);
      v4u k0[8], v0[8], k1[8], v1[8];
      A.read_k(S, 10 + 2 * v, k0);
      A.read_v(S, 11 + 2 * v, v0);
      A.read_k(S, 16 + 2 * v, k1);
      A.read_v(S, 17 + 2 * v, v1);
      S.wait<C_Q>(4);
      A.load_q(lane);
      float sa[8], sb[8];
      const float mxa = A.scores(k0, sa);
      A.m = max32(max16(fmx(mxa, A.scores(k1, sb))));
      A.pv(v0, A.probs(sa));
      A.pv(v1, A.probs(sb));
      A.hand_on(S, v);
    }
    if (role == 0) {
      if (w == 4) {
      v4u q[2];
      const u32 xp = *(const GAS u32*)(p.x + 16 * r + 2 * (lane & 7));
      gather16<2, 8>(S, c.ws, W_MOD2 + (64 * ((lane >> 3) & 3) + 32 * (lane >> 5) + (r >> 2)) * 512 + ((r & 3) * 16 + 2 * (lane & 7)) * 8,
                     [](int i) { return (u32)i * 16 * 512; }, q, c.tag);
      float y[4] = {asf(q[0].x), asf(q[0].z), asf(q[1].x), asf(q[1].z)}, o[4];
#pragma unroll
      for (int i = 0; i < 4; ++i) { y[i] += dpp<ROR8>(y[i]); y[i] = rbf(sum16(y[i])); o[i] = sw32(y[i], y[i]); }
      stl<v4f>(L_HS + (lane & 7) * 16, v4f{y[0], o[2], lo16(xp), 0.f});
      stl<v4f>(L_HS + 128 + (lane & 7) * 16, v4f{y[1], o[3], hi16(xp), 0.f});
      gs16(c.ws, lane < 8 ? W_CS + r * 128 + lane * 16 : OOB, v4u{pk(1.f + o[0], y[2]), c.tag, pk(1.f + o[1], y[3]), c.tag});
      }
    } else if (role == 1 && w == 4) {
      v4u q[1];
      gather16<1, 32>(S, c.ws, W_CS + (128 * (wg & 7) + 2 * lane) * 8, [](int) { return 0u; }, q, c.tag);
      stl<v2u>(L_CS + lane * 8, v2u{q[0].x, q[0].z});
    }
  }

  if (role == 0) {
    const u32 pa = L_PRIV + w * 512;
    const u32 xa = opq(pa + (lane >> 4) * 8), oo = opq(W_A + ((v + 8 * (lane >> 4)) * 64 + r) * 128 + (lane & 15) * 8);
    const u32 o1 = opq(oo + 32 * 8192), o2 = opq(oo + 4 * 8192), o3 = opq(oo + 36 * 8192);
    const u32 ra = opq(L_QS + lane * 4), wr = opq(pa + lane * 2);
    S.wait<C_FULL>(ntail);
    if (w == 4) { STAMP(7); }
    combine_part(p, c, S);
    if (w >= 4) {
      const float o = sum8(ra);
      stl<u16>(wr, (u16)pk(o, o));
      CBAR();
      const u32 of[4] = {oo, o1, o2, o3};
      put_tiles<16>(c.ws, of, wa, xfrag(xa, 0), xfrag(xa, 1), c.tag);
      TS(t9); if (w == 7) TSW(9, t9);
      STAMP(9);
    } else if (w == 2) {
      if (r < 2) ring(c.ws, 4 + r, lane, hear4(c.ws, 0, lane, c.tag), c.tag);
    } else if (w == 0) {
      const v4f kp = ldl<v4f>(L_HS + (lane >> 5) * 128 + (lane & 7) * 16);
      const u32 ox = opq(lane < 8 ? W_X + r * 64 + lane * 8 : lane < 10 ? W_X + 4096 + r * 16 + (lane - 8) * 8 : OOB);
      const u32 ia = opq(W_A + (r * 64 + (lane >> 3)) * 128 + (lane & 7) * 16);
      const u32 t0 = hear4(c.ws, r >> 2, lane, c.tag);
      TS(t21);
      until(t0 + T2);
      TS(t20);
      v4u q[8];
      gather8<1>(c.ws, ia, q, c.tag);
      TS(t10);
      STAMP(10);
      const float a = rbf(kp.z + rbf(kp.x * rbf(col_total(q))));
      float a0 = a;
      const float a1 = sw32(a0, a0);
      const float mu = sum_lo3(a0 + a1) * (1.f / 16.f);
      const float m2 = sum_lo3((a0 - mu) * (a0 - mu) + (a1 - mu) * (a1 - mu));
      gs8(c.ws, ox, v2u{asu(sel(lanes(0xff), asf(pk(a0, a1)), sel(lanes(0x100), mu, m2))), c.tag});
      TS(t11);
      STAMP(11);
      const u32 id = opq(W_D + (r * 56 + (lane >> 3)) * 128 + (lane & 7) * 16);
      const u32 oy = opq(lane < 8 ? 32 * r + 4 * lane : OOB);
      until(t0 + T5);
      v4u q7[7];
      gather7<1>(c.ws, id, q7, c.tag);
      TS(t15);
      STAMP(15);
      float y0 = a + rbf(kp.y * rbf(col_total(q7)));
      const float y1 = sw32(y0, y0);
      __builtin_amdgcn_raw_buffer_store_b32(pk(y0, y1), mk(p.out, 2048), oy, 0, 0);
      __builtin_amdgcn_raw_buffer_store_b32(c.tag, c.ws, (r | lane) ? OOB : W_EPOCH, 0, 16);
      STAMP(16);
      TSW(21, t21); TSW(20, t20); TSW(10, t10); TSW(11, t11); TSW(15, t15); TSW(30, (t20 & ~0xffffffffull) | t0);
    }
  } else if (role == 1) {
    stage_B(p, c, S, lam >= 7, wg & 7, lam % 7, ntail);
  } else if (role == 2) {
    stage_D(p, c, S, tt, ntail);
  }
}

extern "C" void kernel_launch(void* const* d_in, const int* in_sizes, int n_in, void* d_out, int out_size, void* d_ws,
                              size_t ws_size, hipStream_t stream) {
  Params p;
  p.x = (const u16*)d_in[0]; p.K = (const u16*)d_in[1]; p.V = (const u16*)d_in[2];
  p.wq = (const u16*)d_in[3]; p.wo = (const u16*)d_in[4];
  p.wg = (const u16*)d_in[6]; p.wu = (const u16*)d_in[7]; p.wd = (const u16*)d_in[8];
  p.c = (const u16*)d_in[9]; p.wmod = (const u16*)d_in[10];
  p.out = (u16*)d_out; p.ws = (char*)d_ws;
  (void)hipFuncSetAttribute((const void*)fused_block, hipFuncAttributeMaxDynamicSharedMemorySize, 163840);
  hipLaunchKernelGGL(fused_block, dim3(256), dim3(512), 163840, stream, p);
}
```

```cpp
#include <hip/hip_runtime.h>
#include <stdint.h>
#define STAMPS 1
#define XK 101

typedef unsigned short u16;
typedef unsigned u32;
typedef unsigned long long u64;
typedef u32 v4u __attribute__((ext_vector_type(4)));
typedef u32 v2u __attribute__((ext_vector_type(2)));
typedef float v4f __attribute__((ext_vector_type(4)));
typedef float v2f __attribute__((ext_vector_type(2)));
typedef __bf16 v8bf __attribute__((ext_vector_type(8)));
typedef __bf16 v2bf __attribute__((ext_vector_type(2)));
typedef short v4s __attribute__((ext_vector_type(4)));
#define LAS __attribute__((address_space(3)))
#define GAS __attribute__((address_space(1)))
#define DI __device__ __forceinline__
typedef __amdgpu_buffer_rsrc_t rsrc_t;

#define ROT 37
#define NAP0A 10
#define NAP0B 10
#define NAP1 8
#define NAP2 10
#define NAP5 64
#define PACE0 4
#define PUBNAP 0
#define PACE 0
#define DEPTH 24
#define DEPTH2 16

constexpr u32 W_EPOCH = 0;
constexpr u32 W_MOD1 = 4096;
constexpr u32 W_Q = W_MOD1 + 256 * 512;
constexpr u32 W_MOD2 = W_Q + 256 * 512;
constexpr u32 W_CS = W_MOD2 + 256 * 512;
constexpr u32 W_X = W_CS + 64 * 128;
constexpr u32 W_ML = W_X + 64 * 128;
constexpr u32 W_GU = W_ML + 32 * 64 * 16;
constexpr u32 W_A = W_GU + 56 * 16 * 512;
constexpr u32 W_D = W_A + 64 * 64 * 128;
constexpr u32 W_P = W_D + 64 * 64 * 128;
constexpr u32 W_END = W_P + 64 * 64 * 512;
constexpr u32 W_STAMP = W_END;

constexpr u32 NH = 18;
constexpr u32 L_CTRL = NH * 8192;
constexpr u32 L_MRG = L_CTRL + 256;
constexpr u32 L_QS = L_MRG + 4608;
constexpr u32 L_HS = L_QS + 2048;
constexpr u32 L_CS = L_HS + 512;
constexpr u32 L_PRIV = L_CS + 4096;
constexpr u32 L_END = L_PRIV + 4096;
static_assert(L_END <= 163840, "LDS");
constexpr u32 C_FULL = 0, C_H = 2, C_Q = 3, C_M = 4, C_CS = 5, C_TAG = 6, C_W = 7, C_FREE = 16;
constexpr float LOG2E = 1.4426950408889634f;

struct Params {
  const u16 *x, *K, *V, *wq, *wo, *wg, *wu, *wd, *c, *wmod;
  u16* out;
  char* ws;
};

template <class T> DI T ldl(u32 a) { return *(LAS T*)a; }
template <class T> DI void stl(u32 a, T v) { *(LAS T*)a = v; }
DI u32 rfl(u32 v) { return __builtin_amdgcn_readfirstlane(v); }
DI float asf(u32 v) { return __builtin_bit_cast(float, v); }
DI u32 asu(float v) { return __builtin_bit_cast(u32, v); }
DI float lo16(u32 v) { return asf(v << 16); }
DI float hi16(u32 v) { return asf(v & 0xffff0000u); }
DI u32 pk(float a, float b) { v2f t = {a, b}; return __builtin_bit_cast(u32, __builtin_convertvector(t, v2bf)); }
DI float rbf(float a) { return hi16(pk(a, a)); }
#define CBAR() asm volatile("" ::: "memory")
#define LGKM0() asm volatile("s_waitcnt lgkmcnt(0)" ::: "memory")
DI v4f mfma(v4u a, v4u b, v4f c) {
  return __builtin_amdgcn_mfma_f32_16x16x32_bf16(__builtin_bit_cast(v8bf, a), __builtin_bit_cast(v8bf, b), c, 0, 0, 0);
}
DI v2u trd(u32 addr) { return __builtin_bit_cast(v2u, __builtin_amdgcn_ds_read_tr16_b64_v4i16((LAS v4s*)addr)); }
DI v4u cat(v2u a, v2u b) { return v4u{a.x, a.y, b.x, b.y}; }
template <int CTRL> DI float dpp(float x) { return asf(__builtin_amdgcn_update_dpp(0, asu(x), CTRL, 0xf, 0xf, true)); }
constexpr int XOR1 = 0xB1, XOR2 = 0x4E, HMIR = 0x141, ROR8 = 0x128;
DI float fmx(float a, float b) { return __builtin_amdgcn_fmed3f(a, b, INFINITY); }
DI float sw16(float& a, float b) { auto s = __builtin_amdgcn_permlane16_swap(asu(a), asu(b), false, false); a = asf(s[0]); return asf(s[1]); }
DI float sw32(float& a, float b) { auto s = __builtin_amdgcn_permlane32_swap(asu(a), asu(b), false, false); a = asf(s[0]); return asf(s[1]); }
DI float sum16(float x) { const float o = sw16(x, x); return x + o; }
DI float sum32(float x) { const float o = sw32(x, x); return x + o; }
DI float max16(float x) { const float o = sw16(x, x); return fmx(x, o); }
DI float max32(float x) { const float o = sw32(x, x); return fmx(x, o); }
DI float sum_lo3(float x) { x += dpp<XOR1>(x); x += dpp<XOR2>(x); return x + dpp<HMIR>(x); }
DI float sum64(float x) { x = sum_lo3(x); x += dpp<ROR8>(x); return sum32(sum16(x)); }
DI float max64(float x) {
  x = fmx(x, dpp<XOR1>(x)); x = fmx(x, dpp<XOR2>(x)); x = fmx(x, dpp<HMIR>(x)); x = fmx(x, dpp<ROR8>(x));
  return max32(max16(x));
}
DI float sel(bool c, float a, float b) { asm("" : "+v"(a)); asm("" : "+v"(b)); return c ? a : b; }
DI float ex2(float x) { return __builtin_amdgcn_exp2f(x); }
DI float rcp(float x) { return __builtin_amdgcn_rcpf(x); }
DI float sigm(float z) { return rcp(1.f + ex2(-LOG2E * z)); }
DI u32 opq(u32 v) { asm volatile("" : "+v"(v)); return v; }
DI u64 opqs(u64 v) { asm volatile("" : "+s"(v)); return v; }
DI bool lanes(u64 m) { return __builtin_amdgcn_inverse_ballot_w64(m); }
constexpr u64 M_HI8 = 0xff00ff00ff00ff00ull, M_HI32 = 0xffffffff00000000ull;
constexpr u32 OOB = 0x80000000u;

DI u64 now() { return __builtin_amdgcn_s_memrealtime(); }
#define STAMPV(i, val) do { if ((threadIdx.x & 63) == 0) __hip_atomic_store((GAS u64*)(p.ws + W_STAMP + ((size_t)blockIdx.x * 32 + (i)) * 8), (u64)(val), __ATOMIC_RELAXED, __HIP_MEMORY_SCOPE_AGENT); } while (0)
#define STAMP(i) STAMPV(i, now())

struct Sync {
  u32 base;
  template <u32 OFF> DI void wait_at(u32 addr, u32 v) {
    u32 t, s, n;
    asm volatile("s_movk_i32 %2, 0x7fff\n1:\n\tds_read_b32 %0, %3 offset:%c5\n\ts_waitcnt lgkmcnt(0)\n\tv_readfirstlane_b32 %1, %0\n\t"
                 "s_cmp_ge_u32 %1, %4\n\ts_cbranch_scc1 2f\n\ts_sleep 1\n\ts_sub_u32 %2, %2, 1\n\ts_cbranch_scc0 1b\n2:"
                 : "=&v"(t), "=&s"(s), "=&s"(n) : "v"(addr), "s"(v), "i"(OFF) : "memory", "scc");
  }
  template <u32 W> DI void wait(u32 v) { wait_at<4 * W>(base, v); }
  template <u32 W, int N> DI void add1() {
    u64 e;
    asm volatile("s_mov_b64 %0, exec\n\ts_mov_b64 exec, %4\n\tds_add_u32 %1, %2 offset:%c3\n\ts_mov_b64 exec, %0"
                 : "=&s"(e) : "v"(base + (N > 1 ? 4 * (threadIdx.x & 63) : 0)), "v"(1u), "i"(4 * W), "s"((u64)((1ull << N) - 1)) : "memory");
  }
  template <u32 OFF> DI void set_at(u32 addr, u32 v) {
    asm volatile("ds_write_b32 %0, %1 offset:%c2" :: "v"(addr), "v"(v), "i"(OFF) : "memory");
  }
  template <u32 W> DI void set(u32 v) { set_at<4 * W>(base, v); }
};
DI v4u gl16(rsrc_t r, u32 off, u32 so = 0) { return __builtin_amdgcn_raw_buffer_load_b128(r, off, so, 16); }
DI v2u gl8(rsrc_t r, u32 off) { return __builtin_amdgcn_raw_buffer_load_b64(r, off, 0, 16); }
DI void gs16(rsrc_t r, u32 off, v4u v) { __builtin_amdgcn_raw_buffer_store_b128(v, r, off, 0, 16); }
DI void gs8(rsrc_t r, u32 off, v2u v) { __builtin_amdgcn_raw_buffer_store_b64(v, r, off, 0, 16); }
template <int K> DI void nap() { if constexpr (K > 0) __builtin_amdgcn_s_sleep(K); }
DI bool all_ok(u32 bad) { return __builtin_amdgcn_ballot_w64(bad != 0u) == 0ull; }
template <int N, int NAP, class F> DI void gather16(Sync& S, rsrc_t r, u32 base, F f, v4u (&v)[N], u32 tag) {
  for (u32 n = 0;;) {
    CBAR();
#pragma unroll
    for (int i = 0; i < N; ++i) v[i] = gl16(r, base + (f(i) & 4095u), f(i) & ~4095u);
    u32 bad = 0;
#pragma unroll
    for (int i = 0; i < N; ++i) bad |= (v[i].y ^ tag) | (v[i].w ^ tag);
    if (all_ok(bad) || ++n > 8192u) break;
    nap<NAP>();
  }
  CBAR();
}

template <int D> DI void dma4(u32 ld, u32 v0, u32 v1, u32 v2, u32 v3, rsrc_t r, u32 so) {
  asm volatile("s_mov_b32 m0, %0\n\ts_nop 0\n\t"
               "buffer_load_dwordx4 %1, %5, %6 offen nt lds\n\ts_waitcnt vmcnt(%c7)\n\t"
               "buffer_load_dwordx4 %2, %5, %6 offen offset:1024 nt lds\n\ts_waitcnt vmcnt(%c7)\n\t"
               "buffer_load_dwordx4 %3, %5, %6 offen offset:2048 nt lds\n\ts_waitcnt vmcnt(%c7)\n\t"
               "buffer_load_dwordx4 %4, %5, %6 offen offset:3072 nt lds\n\ts_waitcnt vmcnt(%c7)"
               :: "s"(ld), "v"(v0), "v"(v1), "v"(v2), "v"(v3), "s"(r), "s"(so), "i"(D) : "memory");
}
template <int D> DI void dma1(u32 ld, u32 v, rsrc_t r, u32 so) {
  asm volatile("s_mov_b32 m0, %0\n\ts_nop 0\n\tbuffer_load_dwordx4 %1, %2, %3 offen nt lds\n\ts_waitcnt vmcnt(%c4)"
               :: "s"(ld), "v"(v), "s"(r), "s"(so), "i"(D) : "memory");
}
template <int D> DI void vmwait() { asm volatile("s_waitcnt vmcnt(%c0)" :: "i"(D) : "memory"); }
struct Ring {
  Sync S;
  u32 n, pos, need, full, next, lim;
  u64 stall, first;
  DI void publish(u32 f) { if ((int)(f - full) > 0) { full = f; nap<PUBNAP>(); S.set<C_FULL>(f); } }
  DI u32 open() {
    if (n >= NH) {
      const u32 fa = S.base + 4 * pos;
      if (rfl(*(volatile LAS u32*)(fa + 4 * C_FREE)) < need) {
        const u64 t0 = now(); if (!first) first = t0;
        vmwait<0>();
        publish(n);
        S.wait_at<4 * C_FREE>(fa, need);
        stall += now() - t0;
      }
    }
    return pos * 8192u;
  }
  template <int D> DI void close() {
    ++n; ++pos;
    if (pos == NH) { pos = 0; need += 4; }
    publish(n - D / 8);
  }
};
DI rsrc_t mk(const void* p, u32 bytes) { return __builtin_amdgcn_make_buffer_rsrc((void*)p, 0, bytes, 0x00020000); }
template <int D> DI void ld_slab(Ring& R, rsrc_t r, u32 base, u32 pitch, int nh, int lane) {
  const u32 row = lane >> 3, v = row * pitch + (((lane & 7) ^ (((row >> 1) & 3) << 1)) << 4);
#pragma unroll 1
  for (int h = 0; h < nh; ++h) {
    const u32 ld = R.open();
#pragma unroll
    for (int i = 0; i < 8; ++i) { dma1<D>(ld + i * 1024, v, r, base); base += 8 * pitch; }
    R.close<D>();
  }
}
template <int D, u32 PITCH, int SH> DI void ld_rows(Ring& R, rsrc_t r, u32 base, int lane) {
  u32 v[8];
#pragma unroll
  for (int i = 0; i < 8; ++i) v[i] = ((lane ^ (i << 1)) << 4) + i * PITCH - (i & 3) * 1024;
#pragma unroll 1
  for (int h = 0; h < 16; ++h) {
    const u32 ld = R.open(), so = base + (h >> SH) * 8 * PITCH + (h & ((1 << SH) - 1)) * 1024;
    dma4<D>(ld, v[0], v[1], v[2], v[3], r, so);
    dma4<D>(ld + 4096, v[4], v[5], v[6], v[7], r, so);
    R.close<D>();
  }
}

DI v4u xfrag(u32 addr, int k) { return cat(ldl<v2u>(addr + k * 64), ldl<v2u>(addr + k * 64 + 32)); }
template <int NK> DI v4f gemv_cols(u32 n0, u32 xaddr, int c, int lane) {
  const u32 g = lane >> 4, r16 = 4 * g + ((lane & 15) >> 2);
  const u32 a = opq(n0 * 8192 + r16 * 128 + ((c ^ ((r16 >> 1) & 3)) << 5) + (lane & 3) * 8), xa = opq(xaddr + g * 8);
  v4f acc = {0.f, 0.f, 0.f, 0.f};
#pragma unroll
  for (int k = 0; k < NK; ++k) acc = mfma(cat(trd(a + k * 4096), trd(a + k * 4096 + 2048)), xfrag(xa, k), acc);
  return acc;
}
DI void put_tile(rsrc_t ws, u32 off, v4f acc, u32 tag, int lane) {
  const bool hi = lanes(M_HI8);
  gs16(ws, (lane & 7) ? OOB : off + (lane >> 4) * 32 + (lane & 8) * 2, v4u{asu(sel(hi, acc.z, acc.x)), tag, asu(sel(hi, acc.w, acc.y)), tag});
}
DI u32 hs_addr(u32 n) { return (n % NH) * 8192u; }
template <int SH> DI void read_rows(u32 n0, int v, int lane, v4u (&wf)[16]) {
  const u32 g = lane >> 4, r8 = (4 * g + ((lane & 15) >> 2)) & 7;
  const u32 lo = r8 * 1024 + ((v ^ r8) << 5) + (lane & 3) * 8;
#pragma unroll
  for (int q = 0; q < 4; ++q) {
    const int half = SH ? q >> 1 : 0, k = SH ? q & 1 : q;
    u32 a[2];
#pragma unroll
    for (int t = 0; t < 2; ++t) {
      const u32 h0 = hs_addr(n0 + ((4 * k + 2 * t) << SH) + half), h1 = hs_addr(n0 + ((4 * k + 2 * t + 1) << SH) + half);
      a[t] = lo + (lanes(M_HI32) ? h1 : h0);
    }
#pragma unroll
    for (int u = 0; u < 4; ++u) wf[SH ? (half * 4 + u) * 2 + k : 4 * k + u] = cat(trd(a[0] + u * 256), trd(a[1] + u * 256));
  }
}
struct Masks { u64 m[8]; };
DI Masks mk_masks() {
  Masks M;
#pragma unroll
  for (int i = 0; i < 8; ++i) M.m[i] = opqs(0x0101010101010101ull << i);
  return M;
}
DI v4u tiles8(const v4u (&wf)[16], v4u x0, v4u x1, const Masks& M, u32 tag) {
  v4f acc[8];
#pragma unroll
  for (int i = 0; i < 8; ++i) acc[i] = mfma(wf[2 * i], x0, v4f{0.f, 0.f, 0.f, 0.f});
#pragma unroll
  for (int i = 0; i < 8; ++i) acc[i] = mfma(wf[2 * i + 1], x1, acc[i]);
  const bool hi = lanes(M_HI8);
  float o0 = sel(hi, acc[0].z, acc[0].x), o1 = sel(hi, acc[0].w, acc[0].y);
#pragma unroll
  for (int i = 1; i < 8; ++i) {
    o0 = sel(lanes(M.m[i]), sel(hi, acc[i].z, acc[i].x), o0);
    o1 = sel(lanes(M.m[i]), sel(hi, acc[i].w, acc[i].y), o1);
  }
  return v4u{asu(o0), tag, asu(o1), tag};
}
DI u32 tile_of(int lane, int v) { return 32 * ((lane >> 2) & 1) + 8 * (lane & 3) + v; }

struct Ctx { rsrc_t ws; u32 tag; int lane, w, wg, grp, s; };

struct XIn { v4u xa, xb; u32 xp; };
DI XIn load_x(const Params& p, const Ctx& c) {
  return XIn{*(const GAS v4u*)(p.x + c.lane * 16), *(const GAS v4u*)(p.x + c.lane * 16 + 8),
             *(const GAS u32*)(p.x + 256 * (c.wg >> 6) + 64 * c.w + 2 * (c.lane & 31))};
}
DI float make_ln(const XIn& X) {
  const v4u xa = X.xa, xb = X.xb;
  float sm = 0.f;
#pragma unroll
  for (int i = 0; i < 4; ++i) sm += (lo16(xa[i]) + hi16(xa[i])) + (lo16(xb[i]) + hi16(xb[i]));
  const float mean = sum64(sm) * (1.f / 1024.f);
  float q = 0.f;
#pragma unroll
  for (int i = 0; i < 4; ++i) {
    const float a = lo16(xa[i]) - mean, b = hi16(xa[i]) - mean, d = lo16(xb[i]) - mean, e = hi16(xb[i]) - mean;
    q += (a * a + b * b) + (d * d + e * e);
  }
  const float var = sum64(q) * (1.f / 1024.f);
  const float mb = rbf(mean), den = rbf(sqrtf(rbf(rbf(var) + 1e-5f)));
  return asf(opq(asu(rbf(rbf((lanes(M_HI32) ? hi16(X.xp) : lo16(X.xp)) - mb) / den))));
}
DI void make_h(const Params& p, const Ctx& c, Sync& S, float ln) {
  const int lane = c.lane, kq = c.wg >> 6;
  const u32 base = opq(W_MOD1 + (32 * (lane >> 5) + 4 * kq + c.w) * 512 + (lane & 31) * 16);
  const u32 ha = opq(L_HS + (64 * c.w + 2 * (lane & 31) + (lane >> 5)) * 2);
  v4u v[8];
  nap<NAP0A>();
  gather16<8, 4>(S, c.ws, base, [](int i) { return (u32)(64 * (i & 3) + 16 * (i >> 2)) * 512; }, v, c.tag);
  if (c.w == 0) { STAMP(17); }
  float s0 = (asf(v[0].x) + asf(v[1].x)) + (asf(v[2].x) + asf(v[3].x)), c0 = (asf(v[4].x) + asf(v[5].x)) + (asf(v[6].x) + asf(v[7].x));
  s0 += sw32(s0, (asf(v[0].z) + asf(v[1].z)) + (asf(v[2].z) + asf(v[3].z)));
  c0 += sw32(c0, (asf(v[4].z) + asf(v[5].z)) + (asf(v[6].z) + asf(v[7].z)));
  const float h = rbf(ln * rbf(1.f + rbf(c0))) + rbf(s0);
  stl<u16>(ha, (u16)pk(h, h));
  S.add1<C_H, 1>();
}
DI void make_q(const Params& p, const Ctx& c, Sync& S) {
  const int lane = c.lane;
  v4u v[8];
  const u32 base = W_Q + (16 * c.grp + 4 * c.w + (lane >> 5)) * 512 + (lane & 31) * 16;
  gather16<8, 4>(S, c.ws, base, [](int i) { return (u32)(64 * (i & 3) + 2 * (i >> 2)) * 512; }, v, c.tag);
#pragma unroll
  for (int i = 0; i < 8; i += 4)
    stl<u32>(L_QS + (128 * c.w + 16 * i + lane) * 4,
             pk((asf(v[i].x) + asf(v[i + 1].x)) + (asf(v[i + 2].x) + asf(v[i + 3].x)), (asf(v[i].z) + asf(v[i + 1].z)) + (asf(v[i + 2].z) + asf(v[i + 3].z))));
  S.add1<C_Q, 1>();
}

DI float combine_part(const Params& p, const Ctx& c, Sync& S) {
  const int lane = c.lane, a = 16 * c.grp + c.s, sl = 8 * c.w + (lane >> 3);
  const u32 base = opq(W_P + (a * 64 + sl) * 512 + (lane & 7) * 16);
  const int dim = 2 * (lane & 7) + 32 * (lane >> 5) + 16 * ((lane >> 4) & 1) + ((lane >> 3) & 1);
  const u32 ra = opq(L_QS + (c.w * 64 + dim) * 4);
  if (c.w == 5) {
    v4u ml[1];
    gather16<1, 2>(S, c.ws, W_ML + (c.grp * 64 + lane) * 128 + (c.s >> 1) * 16, [](int) { return 0u; }, ml, c.tag);
    const float e = ex2((asf(ml[0].x) - max64(asf(ml[0].x))) * LOG2E);
    stl<float>(L_HS + 256 + lane * 4, e * (1.f / sum64(e * asf(ml[0].z))));
    S.set<C_W>(1);
    STAMP(26);
  }
  S.wait<C_W>(1);
  const float wt = ldl<float>(L_HS + 256 + sl * 4);
  nap<NAP1>();
  v4u v[4];
  gather16<4, 1>(S, c.ws, base, [](int i) { return (u32)i * 128; }, v, c.tag);
  STAMP(8);
  float z0 = asf(v[0].x) * wt, z1 = asf(v[0].z) * wt, z2 = asf(v[1].x) * wt, z3 = asf(v[1].z) * wt;
  z0 += sw32(z0, asf(v[2].x) * wt); z1 += sw32(z1, asf(v[2].z) * wt);
  z2 += sw32(z2, asf(v[3].x) * wt); z3 += sw32(z3, asf(v[3].z) * wt);
  z0 += sw16(z0, z2); z1 += sw16(z1, z3);
  const bool up = lanes(M_HI8);
  stl<float>(ra, sel(up, z1, z0) + dpp<ROR8>(sel(up, z0, z1)));
  LGKM0();
  __builtin_amdgcn_s_barrier();
  CBAR();
  return 0.f;
}
DI float sum8(u32 a) {
  float r[8];
#pragma unroll
  for (int i = 0; i < 8; ++i) r[i] = ldl<float>(a + i * 256);
  return ((r[0] + r[1]) + (r[2] + r[3])) + ((r[4] + r[5]) + (r[6] + r[7]));
}

DI void stage_B(const Params& p, const Ctx& c, Sync& S, int m, int kb, int cb, u32 n0) {
  const int lane = c.lane, w = c.w;
  S.wait<C_FULL>(n0 + 16);
  v4u wf[16];
  read_rows<0>(n0, w, lane, wf);
  S.wait<C_CS>(1);
  const v2u cs = ldl<v2u>(L_CS + lane * 8);
  const Masks M = mk_masks();
  const int P = 64 * kb + lane, tile = w + 8 * (lane & 3);
  const u32 pa = opq(L_PRIV + w * 512 + lane * 4), xa = opq(L_PRIV + w * 512 + (lane >> 4) * 8);
  const u32 o0 = opq(W_X + (P >> 3) * 128 + (P & 7) * 8), o1 = opq(W_X + lane * 128 + 64);
  const u32 oo = opq((lane & 4) ? OOB : W_GU + ((8 * cb + (tile >> 2)) * 16 + 2 * kb + m) * 512 + (tile & 3) * 128 + (lane >> 4) * 32 + (lane & 8) * 2);
  LGKM0();
  STAMP(12);
  v2u xg; v4u st;
  for (u32 n = 0;;) {
    CBAR();
    xg = gl8(c.ws, o0);
    st = gl16(c.ws, o1);
    if (all_ok((xg.y ^ c.tag) | (st.y ^ c.tag) | (st.w ^ c.tag)) || ++n > 8192u) break;
    nap<3>();
  }
  CBAR();
  STAMP(13);
  const float mu0 = asf(rfl(st.x));
  float d = asf(st.x) - mu0;
  d += sw32(d, asf(st.z) + 16.f * d * d);
  d = sum_lo3(d); d += dpp<ROR8>(d); d = sum16(d);
  const float e = sw32(d, d) * (1.f / 1024.f);
  d *= 1.f / 64.f;
  const float mb = rbf(mu0 + d), inv = rcp(rbf(__builtin_amdgcn_sqrtf(rbf(rbf(fmx(e - d * d, 0.f)) + 1e-5f))));
  const u32 xm = pk(lo16(xg.x) - mb, hi16(xg.x) - mb), ln = pk(lo16(xm) * inv, hi16(xm) * inv);
  const u32 sc = pk(lo16(ln) * lo16(cs.x), hi16(ln) * lo16(cs.y));
  stl<u32>(pa, pk(lo16(sc) + hi16(cs.x), hi16(sc) + hi16(cs.y)));
  CBAR();
  v4f acc[4];
#pragma unroll
  for (int u = 0; u < 4; ++u) acc[u] = v4f{0.f, 0.f, 0.f, 0.f};
#pragma unroll
  for (int k = 0; k < 4; ++k) {
    const v4u xf = xfrag(xa, k);
#pragma unroll
    for (int u = 0; u < 4; ++u) acc[u] = mfma(wf[k * 4 + u], xf, acc[u]);
  }
  const bool hi = lanes(M_HI8);
  float r0 = sel(hi, acc[0].z, acc[0].x), r1 = sel(hi, acc[0].w, acc[0].y);
#pragma unroll
  for (int u = 1; u < 4; ++u) {
    r0 = sel(lanes(M.m[u]), sel(hi, acc[u].z, acc[u].x), r0);
    r1 = sel(lanes(M.m[u]), sel(hi, acc[u].w, acc[u].y), r1);
  }
  gs16(c.ws, oo, v4u{asu(r0), c.tag, asu(r1), c.tag});
  STAMP(14);
}

DI void stage_D(const Params& p, const Ctx& c, Sync& S, int t, u32 n0) {
  const int lane = c.lane, w = c.w;
  S.wait<C_FULL>(n0 + 16);
  v4u wf[16];
  read_rows<1>(n0, w, lane, wf);
  const Masks M = mk_masks();
  const u32 pa = opq(L_PRIV + w * 512 + lane * 2), xa = opq(L_PRIV + w * 512 + (lane >> 4) * 8);
  const u32 oi = opq(W_GU + (t * 16 + 2 * w) * 512 + lane * 16);
  const u32 wa = opq(L_CS + w * 512 + (lane & 31) * 16 + (lane >> 5) * 4), ra = opq(L_CS + lane * 8);
  const u32 oo = opq(W_D + (tile_of(lane, w) * 56 + t) * 128 + (lane >> 4) * 32 + (lane & 8) * 2);
  LGKM0();
  STAMP(27);
  v4u q[1];
  gather16<1, 3>(S, c.ws, oi, [](int) { return 0u; }, q, c.tag);
  STAMP(28);
  stl<u32>(wa, q[0].x);
  stl<u32>(wa + 8, q[0].z);
  LGKM0();
  __builtin_amdgcn_s_barrier();
  CBAR();
  v2f r[8];
#pragma unroll
  for (int i = 0; i < 8; ++i) r[i] = ldl<v2f>(ra + i * 512);
  const v2f y = ((r[0] + r[1]) + (r[2] + r[3])) + ((r[4] + r[5]) + (r[6] + r[7]));
  const u32 gu = pk(y.x, y.y);
  const float g = lo16(gu);
  const float z = (-2.f * LOG2E * 0.7978845608028654f) * (g + 0.044715f * g * g * g);
  const float h = rbf(g * rcp(1.f + ex2(z))) * hi16(gu);
  stl<u16>(pa, (u16)pk(h, h));
  CBAR();
  gs16(c.ws, oo, tiles8(wf, xfrag(xa, 0), xfrag(xa, 1), M, c.tag));
  STAMP(29);
}

template <int N> DI float col_total(const v4u (&q)[N]) {
  float s0 = asf(q[0].x), s1 = asf(q[0].z);
#pragma unroll
  for (int i = 1; i < N; ++i) { s0 += asf(q[i].x); s1 += asf(q[i].z); }
  s0 += sw32(s0, s1);
  s0 = sum16(s0);
  return s0 + dpp<ROR8>(s0);
}

__global__ void __launch_bounds__(512) fused_block(Params p) {
  const int tid = threadIdx.x, lane = tid & 63, w = rfl(tid >> 6), wg = blockIdx.x;
  const int grp = (wg >> 3) & 3, s = (wg & 7) + ((wg >> 5) << 3);
  const bool isA = s < 16;
  const int lam = (wg >> 3) - 8;
  const int role = isA ? 0 : lam < 14 ? 1 : lam < 21 ? 2 : 3;
  const int tt = (wg & 7) + 8 * (lam % 7), r = 16 * grp + s, v = w & 3;
  const u32 nkv = isA ? 26 : 10, NT = isA ? 26 : 34, ntail = nkv + 2 * NT;
  const u64 tstart = now(), cstart = __builtin_amdgcn_s_memtime();
  Sync S{opq(L_CTRL)};
  if (w == 0) { STAMP(0); S.set_at<0>(S.base + 4 * lane, 0u); }
  Ctx c;
  c.ws = mk(p.ws, W_END);
  c.lane = lane; c.w = w; c.wg = wg; c.grp = grp; c.s = s;
  v4u wa[16], wb[16];
  auto take_wo = [&]() {
    read_rows<1>(10, v, lane, wa);
    read_rows<1>(10, v + 4, lane, wb);
    LGKM0();
    S.add1<C_FREE + 10, 8>();
    S.add1<C_FREE, 8>();
  };

  if (w == 7) {
    __builtin_amdgcn_s_setprio(3);
    Ring R{S, 0, 0, 0, 0, 0, ntail, 0, 0};
    const rsrc_t rm = mk(p.wmod, 1024 * 6144 * 2);
    ld_slab<DEPTH>(R, rm, (wg >> 5) * 128 * 12288 + (wg & 31) * 128, 12288, 2, lane);
    __builtin_amdgcn_s_barrier();
    ld_slab<DEPTH>(R, mk(p.wq, 1024 * 4096 * 2), (wg >> 6) * 256 * 8192 + (wg & 63) * 128, 8192, 4, lane);
    ld_slab<DEPTH>(R, rm, (wg >> 6) * 256 * 12288 + 4096 + (wg & 63) * 128, 12288, 4, lane);
    if (isA) ld_rows<DEPTH, 2048, 1>(R, mk(p.wo, 4096 * 1024 * 2), r * 64 * 2048, lane);
    const rsrc_t rk = mk(p.K, 1u << 26), rv = mk(p.V, 1u << 26);
    const u32 rw = lane >> 4, ps = lane & 15;
    u32 vk[4], vv[2];
#pragma unroll
    for (int i = 0; i < 4; ++i) vk[i] = rw * 256 + ((ps ^ (4 * i + rw)) << 4);
#pragma unroll
    for (int i = 0; i < 2; ++i) vv[i] = rw * 256 + ((ps ^ ((4 * i + rw) << 1)) << 4);
    auto tile_off = [&](int tau) { return ((u32)grp << 24) + (((tau < 8 ? tau * 48 + s - 16 : 384 + (tau - 8) * 64 + s) + ROT * grp) & 2047) * 8192; };
    auto ldk = [&](int tau) {
      const u32 so = tile_off(tau), ld = R.open();
      dma4<DEPTH>(ld, vk[0], vk[1], vk[2], vk[3], rk, so);
      dma4<DEPTH>(ld + 4096, vk[0], vk[1], vk[2], vk[3], rk, so + 4096);
      R.close<DEPTH>();
    };
    auto ldv = [&](int tau) {
      const u32 so = tile_off(tau), ld = R.open();
      dma4<DEPTH>(ld, vv[0], vv[1], vv[0], vv[1], rv, so);
      dma4<DEPTH>(ld + 4096, vv[0], vv[1], vv[0], vv[1], rv, so + 4096);
      R.close<DEPTH>();
    };
    int tau = isA ? 8 : 0;
    if (isA) {
      ldk(8); ldv(8);
      vmwait<16>();
      R.publish(26);
      tau = 9;
      take_wo();
    }
#pragma unroll 1
    for (; tau < 31; ++tau) { ldk(tau); ldv(tau); }
#pragma unroll 1
    for (; tau < 34; ++tau) ldk(tau);
#pragma unroll 1
    for (tau = 31; tau < 34; ++tau) ldv(tau);
    STAMP(3);
    if (role == 1) ld_rows<DEPTH2, 7168, 0>(R, mk(lam < 7 ? p.wg : p.wu, 1024 * 3584 * 2), (wg & 7) * 128 * 7168 + (lam % 7) * 1024, lane);
    else if (role == 2) ld_rows<DEPTH2, 2048, 1>(R, mk(p.wd, 3584 * 1024 * 2), tt * 64 * 2048, lane);
    vmwait<32>(); R.publish(R.n - 4);
    vmwait<24>(); R.publish(R.n - 3);
    vmwait<16>(); R.publish(R.n - 2);
    vmwait<8>(); R.publish(R.n - 1);
    vmwait<0>(); R.publish(R.n);
    STAMP(4);
    STAMPV(22, R.first);
    STAMPV(31, R.stall + 1);
    __builtin_amdgcn_s_setprio(0);
    c.tag = rfl(*(volatile LAS u32*)(S.base + 4 * C_TAG));
  } else if (w < 4) {
    const u32 ep = __hip_atomic_load((GAS u32*)(p.ws + W_EPOCH), __ATOMIC_RELAXED, __HIP_MEMORY_SCOPE_AGENT);
    const u32 c1 = *(const GAS u32*)(p.c + (wg >> 5) * 128 + 2 * lane);
    const v2u c2 = *(const GAS v2u*)(p.c + (wg >> 6) * 256 + 4 * lane);
    const XIn X = load_x(p, c);
    const u32 pa = L_PRIV + w * 1024;
    LGKM0();
    __builtin_amdgcn_s_barrier();
    CBAR();
    auto silu2 = [](u32 a) { const float x = lo16(a), y = hi16(a); return pk(x * sigm(x), y * sigm(y)); };
    stl<u32>(pa + lane * 4, silu2(c1));
    stl<v2u>(pa + 256 + lane * 8, v2u{silu2(c2.x), silu2(c2.y)});
    c.tag = rfl(ep) + 1u;
    S.set<C_TAG>(c.tag);
    S.wait<C_FULL>(2);
    put_tile(c.ws, W_MOD1 + wg * 512 + w * 128, gemv_cols<4>(0, pa, w, lane), c.tag, lane);
    S.add1<C_FREE, 2>();
    if (w == 0) { STAMP(1); }
    make_h(p, c, S, make_ln(X));
    S.wait<C_FULL>(6);
    S.wait<C_H>(4);
    if (w == 0) { STAMP(18); }
    put_tile(c.ws, W_Q + wg * 512 + w * 128, gemv_cols<8>(2, L_HS, w, lane), c.tag, lane);
    S.add1<C_FREE + 2, 4>();
    if (w == 0) { STAMP(19); }
    S.wait<C_FULL>(10);
    put_tile(c.ws, W_MOD2 + wg * 512 + w * 128, gemv_cols<8>(6, pa + 256, w, lane), c.tag, lane);
    S.add1<C_FREE + 6, 4>();
    if (w == 0) { STAMP(20); }
    nap<NAP0B>();
    make_q(p, c, S);
    S.wait<C_Q>(4);
    if (w == 0) { STAMP(2); }
    const u32 g = lane >> 4, j = lane & 15;
    v4u qf[4];
#pragma unroll
    for (int k = 0; k < 4; ++k) qf[k] = ldl<v4u>(L_QS + (j & 7) * 256 + k * 64 + g * 16);
    const u32 kb = j * 256 + ((g ^ j) << 4);
    const u32 r16 = 4 * g + (j >> 2), vb = r16 * 256 + ((r16 & 7) << 5) + (lane & 3) * 8;
    float m = -INFINITY, l = 0.f;
    v4f acc[8];
#pragma unroll
    for (int b = 0; b < 8; ++b) acc[b] = v4f{0.f, 0.f, 0.f, 0.f};
    const u32 ma = opq(L_MRG + lane * 16), mla = opq(L_MRG + 4096 + lane * 8);
    auto raw = [&](u32 n, float (&sv)[8]) {
      const u32 pos = n % NH, ka = pos * 8192;
      S.wait<C_FULL>(n + 1);
      v4u kf[8];
#pragma unroll
      for (int k = 0; k < 4; ++k) { const u32 a = ka + (kb ^ (k << 6)); kf[k] = ldl<v4u>(a); kf[4 + k] = ldl<v4u>(a + 4096); }
      v4f s0 = {0.f, 0.f, 0.f, 0.f}, s1 = s0;
#pragma unroll
      for (int k = 0; k < 4; ++k) { s0 = mfma(kf[k], qf[k], s0); s1 = mfma(kf[4 + k], qf[k], s1); }
      S.set_at<4 * C_FREE>(S.base + 4 * pos, n / NH * 4 + 4);
#pragma unroll
      for (int i = 0; i < 4; ++i) {
        const u32 a = i < 2 ? pk(s0[2 * i], s0[2 * i + 1]) : pk(s1[2 * i - 4], s1[2 * i - 3]);
        const u32 b = pk(lo16(a) * 0.08838834764831845f, hi16(a) * 0.08838834764831845f);
        sv[2 * i] = lo16(b); sv[2 * i + 1] = hi16(b);
      }
      return fmx(fmx(fmx(sv[0], sv[1]), fmx(sv[2], sv[3])), fmx(fmx(sv[4], sv[5]), fmx(sv[6], sv[7])));
    };
    auto probs = [&](const float (&sv)[8]) {
      float e[8];
#pragma unroll
      for (int i = 0; i < 8; ++i) e[i] = ex2((sv[i] - m) * LOG2E);
      l += ((e[0] + e[1]) + (e[2] + e[3])) + ((e[4] + e[5]) + (e[6] + e[7]));
      return v4u{pk(e[0], e[1]), pk(e[2], e[3]), pk(e[4], e[5]), pk(e[6], e[7])};
    };
    auto values = [&](u32 n, v4u pf) {
      const u32 pos = n % NH;
      u32 a[8];
#pragma unroll
      for (int b = 0; b < 8; ++b) a[b] = opq(pos * 8192 + (vb ^ (b << 5)));
      S.wait<C_FULL>(n + 1);
      v4u vf[8];
#pragma unroll
      for (int b = 0; b < 8; ++b) vf[b] = cat(trd(a[b]), trd(a[b] + 4096));
#pragma unroll
      for (int b = 0; b < 8; ++b) acc[b] = mfma(vf[b], pf, acc[b]);
      S.set_at<4 * C_FREE>(S.base + 4 * pos, n / NH * 4 + 4);
    };
    auto fold = [&]() {
      const v2f ml = ldl<v2f>(mla);
      const float mx = fmx(m, ml.x), al = ex2((m - mx) * LOG2E), be = ex2((ml.x - mx) * LOG2E);
      l = l * al + be * ml.y;
      m = mx;
#pragma unroll
      for (int b2 = 0; b2 < 4; ++b2) {
        const v4f o = ldl<v4f>(ma + b2 * 1024) * be;
        acc[2 * b2] = acc[2 * b2] * al + v4f{o.x, o.y, o.x, o.y};
        acc[2 * b2 + 1] = acc[2 * b2 + 1] * al + v4f{o.z, o.w, o.z, o.w};
      }
    };
    auto rescale = [&](float mx) {
      mx = fmx(m, max32(max16(mx)));
      if (__builtin_amdgcn_ballot_w64(mx > m) != 0ull) {
        const float al = ex2((m - mx) * LOG2E);
        l *= al;
#pragma unroll
        for (int b = 0; b < 8; ++b) acc[b] *= al;
        m = mx;
      }
    };
    const int order = (w - (int)NT + 1) & 3;
#pragma unroll 1
    for (u32 it = w; it < NT - 2; it += 4) {
      float sv[8];
      rescale(raw(nkv + 2 * it, sv));
      values(nkv + 2 * it + (it == NT - 3 ? 3 : 1), probs(sv));
    }
    const bool hi = lanes(M_HI8);
    if (order < 3) {
      if (order > 0) { S.wait<C_M>(order); fold(); }
#pragma unroll
      for (int b2 = 0; b2 < 4; ++b2) {
        const v4f a = acc[2 * b2], b = acc[2 * b2 + 1];
        stl<v4f>(ma + b2 * 1024, v4f{sel(hi, a.z, a.x), sel(hi, a.w, a.y), sel(hi, b.z, b.x), sel(hi, b.w, b.y)});
      }
      stl<v2f>(mla, v2f{m, l});
      S.set<C_M>(order + 1);
    } else {
      const u32 n = ntail - 5;
      float sa[8], sb[8];
      const float mxa = raw(n, sa), mxb = raw(n + 1, sb);
      rescale(fmx(mxa, mxb));
      const v4u p0 = probs(sa), p1 = probs(sb);
      const u32 o = opq(W_P + ((16 * grp + 2 * (j & 7)) * 64 + s) * 512 + g * 32 + (j & 8) * 2), o2 = opq(o + 64 * 512);
      const u32 om = opq(lane < 8 ? W_ML + (64 * grp + s) * 128 + lane * 16 : OOB);
      STAMP(23);
      values(n + 3, p0);
      S.wait<C_M>(3);
      STAMP(24);
      const v2f ml = ldl<v2f>(mla);
      const float mx = fmx(m, ml.x), al = ex2((m - mx) * LOG2E), be = ex2((ml.x - mx) * LOG2E);
      gs16(c.ws, om, v4u{asu(mx), c.tag, asu(sum32(sum16(l * al + be * ml.y))), c.tag});
      v4f y[4];
#pragma unroll
      for (int b2 = 0; b2 < 4; ++b2) { y[b2] = ldl<v4f>(ma + b2 * 1024) * be; asm volatile("" : "+v"(y[b2])); }
      STAMP(25);
      values(n + 4, p1);
      STAMP(5);
#pragma unroll
      for (int b = 0; b < 8; ++b)
        gs16(c.ws, (b < 4 ? o : o2) + (b & 3) * 128,
             v4u{asu(sel(hi, acc[b].z, acc[b].x) * al + y[b >> 1][2 * (b & 1)]), c.tag, asu(sel(hi, acc[b].w, acc[b].y) * al + y[b >> 1][2 * (b & 1) + 1]), c.tag});
      STAMP(6);
    }
  } else {
    __builtin_amdgcn_s_barrier();
    if (role == 0) {
      S.wait<C_FULL>(26);
      take_wo();
    }
    S.wait<C_Q>(4);
    c.tag = rfl(*(volatile LAS u32*)(S.base + 4 * C_TAG));
    if (role == 0) {
      if (w == 4) {
      v4u q[2];
      const u32 xp = *(const GAS u32*)(p.x + 16 * r + 2 * (lane & 7));
      gather16<2, 8>(S, c.ws, W_MOD2 + (64 * ((lane >> 3) & 3) + 32 * (lane >> 5) + (r >> 2)) * 512 + ((r & 3) * 16 + 2 * (lane & 7)) * 8,
                     [](int i) { return (u32)i * 16 * 512; }, q, c.tag);
      float y[4] = {asf(q[0].x), asf(q[0].z), asf(q[1].x), asf(q[1].z)}, o[4];
#pragma unroll
      for (int i = 0; i < 4; ++i) { y[i] += dpp<ROR8>(y[i]); y[i] = rbf(sum16(y[i])); o[i] = sw32(y[i], y[i]); }
      stl<v4f>(L_HS + (lane & 7) * 16, v4f{y[0], o[2], lo16(xp), 0.f});
      stl<v4f>(L_HS + 128 + (lane & 7) * 16, v4f{y[1], o[3], hi16(xp), 0.f});
      gs16(c.ws, lane < 8 ? W_CS + r * 128 + lane * 16 : OOB, v4u{pk(1.f + o[0], y[2]), c.tag, pk(1.f + o[1], y[3]), c.tag});
      }
    } else if (role == 1 && w == 4) {
      v4u q[1];
      gather16<1, 32>(S, c.ws, W_CS + (128 * (wg & 7) + 2 * lane) * 8, [](int) { return 0u; }, q, c.tag);
      stl<v2u>(L_CS + lane * 8, v2u{q[0].x, q[0].z});
      S.set<C_CS>(1);
    }
  }

  if (role == 0) {
    const Masks M = mk_masks();
    const u32 pa = L_PRIV + w * 512;
    const u32 xa = opq(pa + (lane >> 4) * 8), oo = opq(W_A + (tile_of(lane, v) * 64 + r) * 128 + (lane >> 4) * 32 + (lane & 8) * 2);
    const u32 ra = opq(L_QS + lane * 4), wr = opq(pa + lane * 2);
    S.wait<C_FULL>(ntail);
    if (w == 4) { STAMP(7); }
    combine_part(p, c, S);
    if (w < 4) return;
    const float o = sum8(ra);
    stl<u16>(wr, (u16)pk(o, o));
    CBAR();
    const v4u x0 = xfrag(xa, 0), x1 = xfrag(xa, 1);
    gs16(c.ws, oo, tiles8(wa, x0, x1, M, c.tag));
    gs16(c.ws, oo + 4 * 64 * 128, tiles8(wb, x0, x1, M, c.tag));
    if (v == 0) {
      STAMP(9);
      const v4f kp = ldl<v4f>(L_HS + (lane >> 5) * 128 + (lane & 7) * 16);
      const u32 ox = opq(lane < 10 ? W_X + r * 128 + lane * 8 : OOB);
      const u32 ia = opq(W_A + (r * 64 + (lane >> 3)) * 128 + (lane & 7) * 16);
      nap<NAP2>();
      v4u q[8];
      gather16<8, 1>(S, c.ws, ia, [](int i) { return (u32)i * 1024; }, q, c.tag);
      STAMP(10);
      const float a = rbf(kp.z + rbf(kp.x * rbf(col_total(q))));
      float a0 = a;
      const float a1 = sw32(a0, a0);
      const float mu = sum_lo3(a0 + a1) * (1.f / 16.f);
      const float m2 = sum_lo3((a0 - mu) * (a0 - mu) + (a1 - mu) * (a1 - mu));
      gs8(c.ws, ox, v2u{asu(sel(lanes(0xff), asf(pk(a0, a1)), sel(lanes(0x100), mu, m2))), c.tag});
      STAMP(11);
      const u32 id = opq(W_D + (r * 56 + (lane >> 3)) * 128 + (lane & 7) * 16);
      const u32 oy = opq(lane < 8 ? 32 * r + 4 * lane : OOB);
      nap<NAP5>();
      v4u q7[7];
      gather16<7, 2>(S, c.ws, id, [](int i) { return (u32)i * 1024; }, q7, c.tag);
      STAMP(15);
      float y0 = a + rbf(kp.y * rbf(col_total(q7)));
      const float y1 = sw32(y0, y0);
      __builtin_amdgcn_raw_buffer_store_b32(pk(y0, y1), mk(p.out, 2048), oy, 0, 0);
      __builtin_amdgcn_raw_buffer_store_b32(c.tag, c.ws, (r | lane) ? OOB : W_EPOCH, 0, 16);
      STAMP(16);
      if (r == 0) {
        const u64 te = now(), ce = __builtin_amdgcn_s_memtime();
        while (now() - te < 300) {}
        u64 t0 = ~0ull, mx = 0;
        for (int i = 0; i < 4; ++i) {
          const u64 a = __hip_atomic_load((GAS u64*)(p.ws + W_STAMP + ((size_t)(lane + 64 * i) * 32) * 8), __ATOMIC_RELAXED, __HIP_MEMORY_SCOPE_AGENT);
          const u64 b = __hip_atomic_load((GAS u64*)(p.ws + W_STAMP + ((size_t)(lane + 64 * i) * 32 + (XK < 32 ? XK : 0)) * 8), __ATOMIC_RELAXED, __HIP_MEMORY_SCOPE_AGENT);
          t0 = a < t0 ? a : t0; mx = b > mx ? b : mx;
        }
        for (int o = 32; o > 0; o >>= 1) { const u64 a = __shfl_xor(t0, o), b = __shfl_xor(mx, o); t0 = a < t0 ? a : t0; mx = b > mx ? b : mx; }
        u64 val = XK == 100 ? te - t0 : XK == 101 ? (ce - cstart) * 100 / (te - tstart) : mx - t0;
        if (val > 20000) val = 20000;
        while (now() - te < val * 100) {}
      }
    }
  } else if (role == 1) {
    stage_B(p, c, S, lam >= 7, wg & 7, lam % 7, ntail);
  } else if (role == 2) {
    stage_D(p, c, S, tt, ntail);
  }
}

extern "C" void kernel_launch(void* const* d_in, const int* in_sizes, int n_in, void* d_out, int out_size, void* d_ws,
                              size_t ws_size, hipStream_t stream) {
  Params p;
  p.x = (const u16*)d_in[0]; p.K = (const u16*)d_in[1]; p.V = (const u16*)d_in[2];
  p.wq = (const u16*)d_in[3]; p.wo = (const u16*)d_in[4];
  p.wg = (const u16*)d_in[6]; p.wu = (const u16*)d_in[7]; p.wd = (const u16*)d_in[8];
  p.c = (const u16*)d_in[9]; p.wmod = (const u16*)d_in[10];
  p.out = (u16*)d_out; p.ws = (char*)d_ws;
  (void)hipFuncSetAttribute((const void*)fused_block, hipFuncAttributeMaxDynamicSharedMemorySize, 163840);
  hipLaunchKernelGGL(fused_block, dim3(256), dim3(512), 163840, stream, p);
}
```

```cpp
#include <hip/hip_runtime.h>
#include <stdint.h>
#include <type_traits>

typedef unsigned short u16;
typedef unsigned u32;
typedef unsigned long long u64;
typedef u32 v4u __attribute__((ext_vector_type(4)));
typedef u32 v2u __attribute__((ext_vector_type(2)));
typedef u32 v32u __attribute__((ext_vector_type(32)));
typedef float v4f __attribute__((ext_vector_type(4)));
typedef float v2f __attribute__((ext_vector_type(2)));
typedef __bf16 v8bf __attribute__((ext_vector_type(8)));
typedef __bf16 v16bf __attribute__((ext_vector_type(16)));
typedef u32 v8u __attribute__((ext_vector_type(8)));
typedef __bf16 v2bf __attribute__((ext_vector_type(2)));
typedef short v4s __attribute__((ext_vector_type(4)));
#define LAS __attribute__((address_space(3)))
#define GAS __attribute__((address_space(1)))
#define DI __device__ __forceinline__
typedef __amdgpu_buffer_rsrc_t rsrc_t;

#define STAMPS 0
#define ROT 128
#define NAP0A 36
#define NAP0B 44
#define NAP1 1
#define NAP2 16
#define NAPR 10
#define NAP3 14
#define NAP4 48
#define PBF 1
#define RR 1
#define SKIP 0
#define NAPCS 127
#define MLE 0
#define NRING 1
#define XA 0
#define XB (XA + 230)
#define PREW 0
#define PREWN 60
#define WSOFF 0
#define NL2 0
#define L2B 2
#define L2D 4
#define L2DEP 24
#define MRS 1
#define PSTR 256
#define PZ 0
#define PF2 0
#define DPACE 16
#define TLBW 0
#define TLBP 0
#define RQ 0
#define NAPQ1 40
#define NAPQ2 30
#define RH 0
#define NAPH1 30
#define NAPH2 120
#define DK 32
#define DV 32
#define NLAST 5
#define KNEE 58
#define KD 0
#define OFFM 0
#define OFFT 290
#define OAUX 17
#define TP 1
#define T1 168
#define T2 152
#define T3 (T2 + 88)
#define T4 (T3 + 98)
#define T5 (T4 + 115)
#define NAP5 22
#define CODEPF 9
#define PACE0 100
#define PUBNAP 0
#define PACE 461
#define DEPTH 24
#define DEPTH3 24
#define DEPTH4 40
#define H2_ 0
#define H2D 2
#define H3D 16
#define H3_ 3
#define DEPTH2 16

constexpr u32 W_EPOCH = 0;
constexpr u32 W_MOD1 = 4096;
constexpr u32 W_Q = W_MOD1 + 256 * 512;
constexpr u32 W_MOD2 = W_Q + 256 * 512;
constexpr u32 W_T0 = 1024;
constexpr u32 W_CS = W_MOD2 + 256 * 512;
constexpr u32 W_X = W_CS + 64 * 128;
constexpr u32 W_ML = W_X + 64 * 128;
constexpr u32 W_GU = W_ML + 32 * 64 * 16;
constexpr u32 W_A = W_GU + 56 * 16 * 512;
constexpr u32 W_D = W_A + 64 * 64 * 128;
constexpr u32 W_P = W_D + 64 * 64 * 128;
constexpr u32 W_MR = W_P + 64 * 64 * 512;
constexpr u32 W_BELL = W_MR + 64 * 64 * 128;
constexpr u32 W_Q2 = W_BELL + 18 * 16 * 128;
constexpr u32 W_H2 = W_Q2 + 2048 * 8;
constexpr u32 W_END = W_H2 + 512 * 8;
#define W_STAMP ((long)(8u << 20) - (long)(WSOFF))

constexpr u32 NH = 18;
constexpr u32 L_CTRL = NH * 8192;
constexpr u32 L_MRG = L_CTRL + 256;
constexpr u32 L_QS = L_MRG + 4608;
constexpr u32 L_HS = L_QS + 2048;
constexpr u32 L_CS = L_HS + 512;
constexpr u32 L_PRIV = L_CS + 4096;
constexpr u32 L_END = L_PRIV + 4096;
static_assert(L_END <= 163840, "LDS");
constexpr u32 C_FULL = 0, C_H = 2, C_Q = 3, C_M = 4, C_CS = 5, C_TAG = 6, C_W = 7, C_T0 = 8, C_L2 = 11, C_MX = 9, C_LS = 10, C_FREE = 16;
constexpr float LOG2E = 1.4426950408889634f;

struct Params {
  const u16 *x, *K, *V, *wq, *wo, *wg, *wu, *wd, *c, *wmod;
  u16* out;
  char* ws;
};

template <class T> DI T ldl(u32 a) { return *(LAS T*)a; }
template <class T> DI void stl(u32 a, T v) { *(LAS T*)a = v; }
DI u32 rfl(u32 v) { return __builtin_amdgcn_readfirstlane(v); }
DI float asf(u32 v) { return __builtin_bit_cast(float, v); }
DI u32 asu(float v) { return __builtin_bit_cast(u32, v); }
DI float lo16(u32 v) { return asf(v << 16); }
DI float hi16(u32 v) { return asf(v & 0xffff0000u); }
DI u32 pk(float a, float b) { v2f t = {a, b}; return __builtin_bit_cast(u32, __builtin_convertvector(t, v2bf)); }
DI float rbf(float a) { return hi16(pk(a, a)); }
#define CBAR() asm volatile("" ::: "memory")
#define LGKM0() asm volatile("s_waitcnt lgkmcnt(0)" ::: "memory")
DI v4f mfma(v4u a, v4u b, v4f c) {
  return __builtin_amdgcn_mfma_f32_16x16x32_bf16(__builtin_bit_cast(v8bf, a), __builtin_bit_cast(v8bf, b), c, 0, 0, 0);
}
DI v2u trd(u32 addr) { return __builtin_bit_cast(v2u, __builtin_amdgcn_ds_read_tr16_b64_v4i16((LAS v4s*)addr)); }
DI v4u cat(v2u a, v2u b) { return v4u{a.x, a.y, b.x, b.y}; }
template <int CTRL> DI float dpp(float x) { return asf(__builtin_amdgcn_update_dpp(0, asu(x), CTRL, 0xf, 0xf, true)); }
constexpr int XOR1 = 0xB1, XOR2 = 0x4E, HMIR = 0x141, ROR8 = 0x128;
DI float fmx(float a, float b) { float r; asm("v_max_f32 %0, %1, %2" : "=v"(r) : "v"(a), "v"(b)); return r; }
DI float fmx3(float a, float b, float c) { float r; asm("v_max3_f32 %0, %1, %2, %3" : "=v"(r) : "v"(a), "v"(b), "v"(c)); return r; }
#define DPPMAX(NAME, CTRL) \
  DI float NAME(float x) { float r; asm("s_nop 1\n\tv_max_f32_dpp %0, %1, %1 " CTRL " row_mask:0xf bank_mask:0xf" : "=v"(r) : "v"(x)); return r; }
DPPMAX(max_x1, "quad_perm:[1,0,3,2]") DPPMAX(max_x2, "quad_perm:[2,3,0,1]") DPPMAX(max_hm, "row_half_mirror") DPPMAX(max_r8, "row_ror:8")
DI float sw16(float& a, float b) { auto s = __builtin_amdgcn_permlane16_swap(asu(a), asu(b), false, false); a = asf(s[0]); return asf(s[1]); }
DI float sw32(float& a, float b) { auto s = __builtin_amdgcn_permlane32_swap(asu(a), asu(b), false, false); a = asf(s[0]); return asf(s[1]); }
DI float sum16(float x) { const float o = sw16(x, x); return x + o; }
DI float sum32(float x) { const float o = sw32(x, x); return x + o; }
DI float max16(float x) { const float o = sw16(x, x); return fmx(x, o); }
DI float max32(float x) { const float o = sw32(x, x); return fmx(x, o); }
DI float sum_lo3(float x) { x += dpp<XOR1>(x); x += dpp<XOR2>(x); return x + dpp<HMIR>(x); }
DI float sum64(float x) { x = sum_lo3(x); x += dpp<ROR8>(x); return sum32(sum16(x)); }
DI float sel(bool c, float a, float b) { asm("" : "+v"(a)); asm("" : "+v"(b)); return c ? a : b; }
DI u32 selu(bool c, u32 a, u32 b) { return asu(sel(c, asf(a), asf(b))); }
DI float ex2(float x) { return __builtin_amdgcn_exp2f(x); }
DI float rcp(float x) { return __builtin_amdgcn_rcpf(x); }
DI u32 silu2(u32 a) { const float x = lo16(a), y = hi16(a); return pk(x / (1.f + ex2(-LOG2E * x)), y / (1.f + ex2(-LOG2E * y))); }
DI u32 opq(u32 v) { asm volatile("" : "+v"(v)); return v; }
DI u32 opqu(u32 v) { asm volatile("" : "+s"(v)); return v; }
DI u64 opqs(u64 v) { asm volatile("" : "+s"(v)); return v; }
DI bool lanes(u64 m) { return __builtin_amdgcn_inverse_ballot_w64(m); }
constexpr u64 M_HI8 = 0xff00ff00ff00ff00ull, M_HI32 = 0xffffffff00000000ull;
constexpr u32 OOB = 0x80000000u;

DI u64 now() { return __builtin_amdgcn_s_memrealtime(); }
#define STAMPV(i, val) do {} while (0)
#define STAMP(i) do { if (STAMPS == 1 || (i) == 0 || (i) == 16) { STAMPV(i, now()); } } while (0)

#define TS(name) do {} while (0)
#define TSW(i, val) do {} while (0)
struct Sync {
  u32 base;
  template <u32 OFF> DI void wait_at(u32 addr, u32 v) {
    u32 t, s, n;
    asm volatile("s_movk_i32 %2, 0x7fff\n1:\n\tds_read_b32 %0, %3 offset:%c5\n\ts_waitcnt lgkmcnt(0)\n\tv_readfirstlane_b32 %1, %0\n\t"
                 "s_cmp_ge_u32 %1, %4\n\ts_cbranch_scc1 2f\n\ts_sleep 1\n\ts_sub_u32 %2, %2, 1\n\ts_cbranch_scc0 1b\n2:"
                 : "=&v"(t), "=&s"(s), "=&s"(n) : "v"(addr), "s"(v), "i"(OFF) : "memory", "scc");
  }
  template <u32 W> DI void wait(u32 v) { wait_at<4 * W>(base, v); }
  template <u32 W, int N> DI void add1() {
    u64 e;
    asm volatile("s_mov_b64 %0, exec\n\ts_mov_b64 exec, %4\n\tds_add_u32 %1, %2 offset:%c3\n\ts_mov_b64 exec, %0"
                 : "=&s"(e) : "v"(base + (N > 1 ? 4 * (threadIdx.x & 63) : 0)), "v"(1u), "i"(4 * W), "s"((u64)((1ull << N) - 1)) : "memory");
  }
  template <u32 OFF> DI void set_at(u32 addr, u32 v) {
    asm volatile("ds_write_b32 %0, %1 offset:%c2" :: "v"(addr), "v"(v), "i"(OFF) : "memory");
  }
  template <u32 W> DI void set(u32 v) { set_at<4 * W>(base, v); }
};
DI v4u gl16(rsrc_t r, u32 off, u32 so = 0) { return __builtin_amdgcn_raw_buffer_load_b128(r, off, so, 16); }
DI v2u gl8(rsrc_t r, u32 off) { return __builtin_amdgcn_raw_buffer_load_b64(r, off, 0, 16); }
DI void gs16(rsrc_t r, u32 off, v4u v) { __builtin_amdgcn_raw_buffer_store_b128(v, r, off, 0, 16); }
DI void gs8(rsrc_t r, u32 off, v2u v) { __builtin_amdgcn_raw_buffer_store_b64(v, r, off, 0, 16); }
template <int K> DI void nap() { if constexpr (K > 0) __builtin_amdgcn_s_sleep(K); }
DI bool all_ok(u32 bad) { return __builtin_amdgcn_ballot_w64(bad != 0u) == 0ull; }
template <int N, int NAP, class F> DI void gather16(Sync& S, rsrc_t r, u32 base, F f, v4u (&v)[N], u32 tag) {
#pragma unroll 1
  for (u32 n = 0;;) {
    CBAR();
#pragma unroll
    for (int i = 0; i < N; ++i) v[i] = gl16(r, base + (f(i) & 4095u), f(i) & ~4095u);
    u32 bad = 0;
#pragma unroll
    for (int i = 0; i < N; ++i) bad |= (v[i].y ^ tag) | (v[i].w ^ tag);
    if (all_ok(opq(bad)) || ++n > 8192u) break;
    nap<NAP>();
  }
  CBAR();
}

template <int NAP> DI void gather1(rsrc_t r, u32 a, v4u (&v)[1], u32 tag) {
  u64 m[1], o;
  u32 n;
  asm volatile(
               "s_movk_i32 %[n], 0x3fff\n\t"
               "buffer_load_dwordx4 v[224:227], %[a], %[r], 0 offen offset:0 sc1\n\t"
               "1:\n\t"
               "s_waitcnt vmcnt(0)\n\t"
               "v_bitop3_b32 v223, v225, v227, %[t] bitop3:0x7e\n\t"
               "v_cmp_ne_u32 %[m0], 0, v223\n\t"
               "s_or_b64 %[o], %[m0], %[m0]\n\t"
               "s_cbranch_scc0 2f\n\t"
               "s_sleep %c[z]\n\t"
               "s_mov_b64 exec, %[m0]\n\t"
               "s_cbranch_execz 3f\n\t"
               "buffer_load_dwordx4 v[224:227], %[a], %[r], 0 offen offset:0 sc1\n\t"
               "3:\n\t"
               "s_mov_b64 exec, -1\n\t"
               "s_waitcnt vmcnt(0)\n\t"
               "s_sub_u32 %[n], %[n], 1\n\t"
               "s_cbranch_scc0 1b\n\t"
               "2:\n\t"
               : "=&{v[224:227]}"(v[0]), [m0] "=&s"(m[0]), [o] "=&s"(o), [n] "=&s"(n)
               : [a] "v"(a), [r] "s"(r), [t] "s"(tag), [z] "i"(NAP) : "memory", "scc", "v223");
}
template <int NAP> DI void gather2(rsrc_t r, u32 a, u32 b, v4u (&v)[2], u32 tag) {
  u64 m[2], o;
  u32 n;
  asm volatile(
               "s_movk_i32 %[n], 0x3fff\n\t"
               "buffer_load_dwordx4 v[224:227], %[a], %[r], 0 offen sc1\n\t"
               "buffer_load_dwordx4 v[228:231], %[b], %[r], 0 offen sc1\n\t"
               "1:\n\t"
               "s_waitcnt vmcnt(1)\n\t"
               "v_bitop3_b32 v223, v225, v227, %[t] bitop3:0x7e\n\t"
               "v_cmp_ne_u32 %[m0], 0, v223\n\t"
               "s_waitcnt vmcnt(0)\n\t"
               "v_bitop3_b32 v223, v229, v231, %[t] bitop3:0x7e\n\t"
               "v_cmp_ne_u32 %[m1], 0, v223\n\t"
               "s_or_b64 %[o], %[m0], %[m1]\n\t"
               "s_cbranch_scc0 2f\n\t"
               "s_sleep %c[z]\n\t"
               "s_mov_b64 exec, %[m0]\n\t"
               "s_cbranch_execz 3f\n\t"
               "buffer_load_dwordx4 v[224:227], %[a], %[r], 0 offen sc1\n\t"
               "3:\n\t"
               "s_mov_b64 exec, %[m1]\n\t"
               "s_cbranch_execz 3f\n\t"
               "buffer_load_dwordx4 v[228:231], %[b], %[r], 0 offen sc1\n\t"
               "3:\n\t"
               "s_mov_b64 exec, -1\n\t"
               "s_waitcnt vmcnt(0)\n\t"
               "s_sub_u32 %[n], %[n], 1\n\t"
               "s_cbranch_scc0 1b\n\t"
               "2:\n\t"
               : "=&{v[224:227]}"(v[0]), "=&{v[228:231]}"(v[1]), [m0] "=&s"(m[0]), [m1] "=&s"(m[1]), [o] "=&s"(o), [n] "=&s"(n)
               : [a] "v"(a), [b] "v"(b), [r] "s"(r), [t] "s"(tag), [z] "i"(NAP) : "memory", "scc", "v223");
}
template <int NAP> DI void gather4(rsrc_t r, u32 a, v4u (&v)[4], u32 tag) {
  u64 m[4], o;
  u32 n;
  asm volatile(
               "s_movk_i32 %[n], 0x3fff\n\t"
               "buffer_load_dwordx4 v[224:227], %[a], %[r], 0 offen offset:0 sc1\n\t"
               "buffer_load_dwordx4 v[228:231], %[a], %[r], 0 offen offset:128 sc1\n\t"
               "buffer_load_dwordx4 v[232:235], %[a], %[r], 0 offen offset:256 sc1\n\t"
               "buffer_load_dwordx4 v[236:239], %[a], %[r], 0 offen offset:384 sc1\n\t"
               "1:\n\t"
               "s_waitcnt vmcnt(3)\n\t"
               "v_bitop3_b32 v223, v225, v227, %[t] bitop3:0x7e\n\t"
               "v_cmp_ne_u32 %[m0], 0, v223\n\t"
               "s_waitcnt vmcnt(2)\n\t"
               "v_bitop3_b32 v223, v229, v231, %[t] bitop3:0x7e\n\t"
               "v_cmp_ne_u32 %[m1], 0, v223\n\t"
               "s_or_b64 %[o], %[m0], %[m1]\n\t"
               "s_waitcnt vmcnt(1)\n\t"
               "v_bitop3_b32 v223, v233, v235, %[t] bitop3:0x7e\n\t"
               "v_cmp_ne_u32 %[m2], 0, v223\n\t"
               "s_or_b64 %[o], %[o], %[m2]\n\t"
               "s_waitcnt vmcnt(0)\n\t"
               "v_bitop3_b32 v223, v237, v239, %[t] bitop3:0x7e\n\t"
               "v_cmp_ne_u32 %[m3], 0, v223\n\t"
               "s_or_b64 %[o], %[o], %[m3]\n\t"
               "s_cbranch_scc0 2f\n\t"
               "s_sleep %c[z]\n\t"
               "s_mov_b64 exec, %[m0]\n\t"
               "s_cbranch_execz 3f\n\t"
               "buffer_load_dwordx4 v[224:227], %[a], %[r], 0 offen offset:0 sc1\n\t"
               "3:\n\t"
               "s_mov_b64 exec, %[m1]\n\t"
               "s_cbranch_execz 3f\n\t"
               "buffer_load_dwordx4 v[228:231], %[a], %[r], 0 offen offset:128 sc1\n\t"
               "3:\n\t"
               "s_mov_b64 exec, %[m2]\n\t"
               "s_cbranch_execz 3f\n\t"
               "buffer_load_dwordx4 v[232:235], %[a], %[r], 0 offen offset:256 sc1\n\t"
               "3:\n\t"
               "s_mov_b64 exec, %[m3]\n\t"
               "s_cbranch_execz 3f\n\t"
               "buffer_load_dwordx4 v[236:239], %[a], %[r], 0 offen offset:384 sc1\n\t"
               "3:\n\t"
               "s_mov_b64 exec, -1\n\t"
               "s_waitcnt vmcnt(0)\n\t"
               "s_sub_u32 %[n], %[n], 1\n\t"
               "s_cbranch_scc0 1b\n\t"
               "2:\n\t"
               : "=&{v[224:227]}"(v[0]), "=&{v[228:231]}"(v[1]), "=&{v[232:235]}"(v[2]), "=&{v[236:239]}"(v[3]), [m0] "=&s"(m[0]), [m1] "=&s"(m[1]), [m2] "=&s"(m[2]), [m3] "=&s"(m[3]), [o] "=&s"(o), [n] "=&s"(n)
               : [a] "v"(a), [r] "s"(r), [t] "s"(tag), [z] "i"(NAP) : "memory", "scc", "v223");
}
template <int NAP> DI void gather7(rsrc_t r, u32 a, v4u (&v)[7], u32 tag) {
  u64 m[7], o;
  u32 n;
  asm volatile(
               "s_movk_i32 %[n], 0x3fff\n\t"
               "buffer_load_dwordx4 v[224:227], %[a], %[r], 0 offen offset:0 sc1\n\t"
               "buffer_load_dwordx4 v[228:231], %[a], %[r], 0 offen offset:1024 sc1\n\t"
               "buffer_load_dwordx4 v[232:235], %[a], %[r], 0 offen offset:2048 sc1\n\t"
               "buffer_load_dwordx4 v[236:239], %[a], %[r], 0 offen offset:3072 sc1\n\t"
               "buffer_load_dwordx4 v[240:243], %[a], %[r], %[hi] offen offset:0 sc1\n\t"
               "buffer_load_dwordx4 v[244:247], %[a], %[r], %[hi] offen offset:1024 sc1\n\t"
               "buffer_load_dwordx4 v[248:251], %[a], %[r], %[hi] offen offset:2048 sc1\n\t"
               "1:\n\t"
               "s_waitcnt vmcnt(6)\n\t"
               "v_bitop3_b32 v223, v225, v227, %[t] bitop3:0x7e\n\t"
               "v_cmp_ne_u32 %[m0], 0, v223\n\t"
               "s_waitcnt vmcnt(5)\n\t"
               "v_bitop3_b32 v223, v229, v231, %[t] bitop3:0x7e\n\t"
               "v_cmp_ne_u32 %[m1], 0, v223\n\t"
               "s_or_b64 %[o], %[m0], %[m1]\n\t"
               "s_waitcnt vmcnt(4)\n\t"
               "v_bitop3_b32 v223, v233, v235, %[t] bitop3:0x7e\n\t"
               "v_cmp_ne_u32 %[m2], 0, v223\n\t"
               "s_or_b64 %[o], %[o], %[m2]\n\t"
               "s_waitcnt vmcnt(3)\n\t"
               "v_bitop3_b32 v223, v237, v239, %[t] bitop3:0x7e\n\t"
               "v_cmp_ne_u32 %[m3], 0, v223\n\t"
               "s_or_b64 %[o], %[o], %[m3]\n\t"
               "s_waitcnt vmcnt(2)\n\t"
               "v_bitop3_b32 v223, v241, v243, %[t] bitop3:0x7e\n\t"
               "v_cmp_ne_u32 %[m4], 0, v223\n\t"
               "s_or_b64 %[o], %[o], %[m4]\n\t"
               "s_waitcnt vmcnt(1)\n\t"
               "v_bitop3_b32 v223, v245, v247, %[t] bitop3:0x7e\n\t"
               "v_cmp_ne_u32 %[m5], 0, v223\n\t"
               "s_or_b64 %[o], %[o], %[m5]\n\t"
               "s_waitcnt vmcnt(0)\n\t"
               "v_bitop3_b32 v223, v249, v251, %[t] bitop3:0x7e\n\t"
               "v_cmp_ne_u32 %[m6], 0, v223\n\t"
               "s_or_b64 %[o], %[o], %[m6]\n\t"
               "s_cbranch_scc0 2f\n\t"
               "s_sleep %c[z]\n\t"
               "s_mov_b64 exec, %[m0]\n\t"
               "s_cbranch_execz 3f\n\t"
               "buffer_load_dwordx4 v[224:227], %[a], %[r], 0 offen offset:0 sc1\n\t"
               "3:\n\t"
               "s_mov_b64 exec, %[m1]\n\t"
               "s_cbranch_execz 3f\n\t"
               "buffer_load_dwordx4 v[228:231], %[a], %[r], 0 offen offset:1024 sc1\n\t"
               "3:\n\t"
               "s_mov_b64 exec, %[m2]\n\t"
               "s_cbranch_execz 3f\n\t"
               "buffer_load_dwordx4 v[232:235], %[a], %[r], 0 offen offset:2048 sc1\n\t"
               "3:\n\t"
               "s_mov_b64 exec, %[m3]\n\t"
               "s_cbranch_execz 3f\n\t"
               "buffer_load_dwordx4 v[236:239], %[a], %[r], 0 offen offset:3072 sc1\n\t"
               "3:\n\t"
               "s_mov_b64 exec, %[m4]\n\t"
               "s_cbranch_execz 3f\n\t"
               "buffer_load_dwordx4 v[240:243], %[a], %[r], %[hi] offen offset:0 sc1\n\t"
               "3:\n\t"
               "s_mov_b64 exec, %[m5]\n\t"
               "s_cbranch_execz 3f\n\t"
               "buffer_load_dwordx4 v[244:247], %[a], %[r], %[hi] offen offset:1024 sc1\n\t"
               "3:\n\t"
               "s_mov_b64 exec, %[m6]\n\t"
               "s_cbranch_execz 3f\n\t"
               "buffer_load_dwordx4 v[248:251], %[a], %[r], %[hi] offen offset:2048 sc1\n\t"
               "3:\n\t"
               "s_mov_b64 exec, -1\n\t"
               "s_waitcnt vmcnt(0)\n\t"
               "s_sub_u32 %[n], %[n], 1\n\t"
               "s_cbranch_scc0 1b\n\t"
               "2:\n\t"
               : "=&{v[224:227]}"(v[0]), "=&{v[228:231]}"(v[1]), "=&{v[232:235]}"(v[2]), "=&{v[236:239]}"(v[3]), "=&{v[240:243]}"(v[4]), "=&{v[244:247]}"(v[5]), "=&{v[248:251]}"(v[6]), [m0] "=&s"(m[0]), [m1] "=&s"(m[1]), [m2] "=&s"(m[2]), [m3] "=&s"(m[3]), [m4] "=&s"(m[4]), [m5] "=&s"(m[5]), [m6] "=&s"(m[6]), [o] "=&s"(o), [n] "=&s"(n)
               : [a] "v"(a), [r] "s"(r), [t] "s"(tag), [z] "i"(NAP), [hi] "s"(4096u) : "memory", "scc", "v223");
}
template <int NAP> DI void gather8(rsrc_t r, u32 a, v4u (&v)[8], u32 tag) {
  u64 m[8], o;
  u32 n;
  asm volatile(
               "s_movk_i32 %[n], 0x3fff\n\t"
               "buffer_load_dwordx4 v[224:227], %[a], %[r], 0 offen offset:0 sc1\n\t"
               "buffer_load_dwordx4 v[228:231], %[a], %[r], 0 offen offset:1024 sc1\n\t"
               "buffer_load_dwordx4 v[232:235], %[a], %[r], 0 offen offset:2048 sc1\n\t"
               "buffer_load_dwordx4 v[236:239], %[a], %[r], 0 offen offset:3072 sc1\n\t"
               "buffer_load_dwordx4 v[240:243], %[a], %[r], %[hi] offen offset:0 sc1\n\t"
               "buffer_load_dwordx4 v[244:247], %[a], %[r], %[hi] offen offset:1024 sc1\n\t"
               "buffer_load_dwordx4 v[248:251], %[a], %[r], %[hi] offen offset:2048 sc1\n\t"
               "buffer_load_dwordx4 v[252:255], %[a], %[r], %[hi] offen offset:3072 sc1\n\t"
               "1:\n\t"
               "s_waitcnt vmcnt(7)\n\t"
               "v_bitop3_b32 v223, v225, v227, %[t] bitop3:0x7e\n\t"
               "v_cmp_ne_u32 %[m0], 0, v223\n\t"
               "s_waitcnt vmcnt(6)\n\t"
               "v_bitop3_b32 v223, v229, v231, %[t] bitop3:0x7e\n\t"
               "v_cmp_ne_u32 %[m1], 0, v223\n\t"
               "s_or_b64 %[o], %[m0], %[m1]\n\t"
               "s_waitcnt vmcnt(5)\n\t"
               "v_bitop3_b32 v223, v233, v235, %[t] bitop3:0x7e\n\t"
               "v_cmp_ne_u32 %[m2], 0, v223\n\t"
               "s_or_b64 %[o], %[o], %[m2]\n\t"
               "s_waitcnt vmcnt(4)\n\t"
               "v_bitop3_b32 v223, v237, v239, %[t] bitop3:0x7e\n\t"
               "v_cmp_ne_u32 %[m3], 0, v223\n\t"
               "s_or_b64 %[o], %[o], %[m3]\n\t"
               "s_waitcnt vmcnt(3)\n\t"
               "v_bitop3_b32 v223, v241, v243, %[t] bitop3:0x7e\n\t"
               "v_cmp_ne_u32 %[m4], 0, v223\n\t"
               "s_or_b64 %[o], %[o], %[m4]\n\t"
               "s_waitcnt vmcnt(2)\n\t"
               "v_bitop3_b32 v223, v245, v247, %[t] bitop3:0x7e\n\t"
               "v_cmp_ne_u32 %[m5], 0, v223\n\t"
               "s_or_b64 %[o], %[o], %[m5]\n\t"
               "s_waitcnt vmcnt(1)\n\t"
               "v_bitop3_b32 v223, v249, v251, %[t] bitop3:0x7e\n\t"
               "v_cmp_ne_u32 %[m6], 0, v223\n\t"
               "s_or_b64 %[o], %[o], %[m6]\n\t"
               "s_waitcnt vmcnt(0)\n\t"
               "v_bitop3_b32 v223, v253, v255, %[t] bitop3:0x7e\n\t"
               "v_cmp_ne_u32 %[m7], 0, v223\n\t"
               "s_or_b64 %[o], %[o], %[m7]\n\t"
               "s_cbranch_scc0 2f\n\t"
               "s_sleep %c[z]\n\t"
               "s_mov_b64 exec, %[m0]\n\t"
               "s_cbranch_execz 3f\n\t"
               "buffer_load_dwordx4 v[224:227], %[a], %[r], 0 offen offset:0 sc1\n\t"
               "3:\n\t"
               "s_mov_b64 exec, %[m1]\n\t"
               "s_cbranch_execz 3f\n\t"
               "buffer_load_dwordx4 v[228:231], %[a], %[r], 0 offen offset:1024 sc1\n\t"
               "3:\n\t"
               "s_mov_b64 exec, %[m2]\n\t"
               "s_cbranch_execz 3f\n\t"
               "buffer_load_dwordx4 v[232:235], %[a], %[r], 0 offen offset:2048 sc1\n\t"
               "3:\n\t"
               "s_mov_b64 exec, %[m3]\n\t"
               "s_cbranch_execz 3f\n\t"
               "buffer_load_dwordx4 v[236:239], %[a], %[r], 0 offen offset:3072 sc1\n\t"
               "3:\n\t"
               "s_mov_b64 exec, %[m4]\n\t"
               "s_cbranch_execz 3f\n\t"
               "buffer_load_dwordx4 v[240:243], %[a], %[r], %[hi] offen offset:0 sc1\n\t"
               "3:\n\t"
               "s_mov_b64 exec, %[m5]\n\t"
               "s_cbranch_execz 3f\n\t"
               "buffer_load_dwordx4 v[244:247], %[a], %[r], %[hi] offen offset:1024 sc1\n\t"
               "3:\n\t"
               "s_mov_b64 exec, %[m6]\n\t"
               "s_cbranch_execz 3f\n\t"
               "buffer_load_dwordx4 v[248:251], %[a], %[r], %[hi] offen offset:2048 sc1\n\t"
               "3:\n\t"
               "s_mov_b64 exec, %[m7]\n\t"
               "s_cbranch_execz 3f\n\t"
               "buffer_load_dwordx4 v[252:255], %[a], %[r], %[hi] offen offset:3072 sc1\n\t"
               "3:\n\t"
               "s_mov_b64 exec, -1\n\t"
               "s_waitcnt vmcnt(0)\n\t"
               "s_sub_u32 %[n], %[n], 1\n\t"
               "s_cbranch_scc0 1b\n\t"
               "2:\n\t"
               : "=&{v[224:227]}"(v[0]), "=&{v[228:231]}"(v[1]), "=&{v[232:235]}"(v[2]), "=&{v[236:239]}"(v[3]), "=&{v[240:243]}"(v[4]), "=&{v[244:247]}"(v[5]), "=&{v[248:251]}"(v[6]), "=&{v[252:255]}"(v[7]), [m0] "=&s"(m[0]), [m1] "=&s"(m[1]), [m2] "=&s"(m[2]), [m3] "=&s"(m[3]), [m4] "=&s"(m[4]), [m5] "=&s"(m[5]), [m6] "=&s"(m[6]), [m7] "=&s"(m[7]), [o] "=&s"(o), [n] "=&s"(n)
               : [a] "v"(a), [r] "s"(r), [t] "s"(tag), [z] "i"(NAP), [hi] "s"(4096u) : "memory", "scc", "v223");
}

#define ML_FETCH \
      "buffer_load_dwordx4 v[228:231], %[b], %[r], 0 offen sc1\n\t" \
      "buffer_load_dwordx4 v[232:235], %[b], %[r], 0 offen offset:128 sc1\n"
#define ML_CHECK \
      "v_bitop3_b32 v223, v229, v231, %[t] bitop3:0x7e\n\t" \
      "v_bitop3_b32 v222, v233, v235, %[t] bitop3:0x7e\n\t" \
      "v_or_b32 v223, v223, v222\n\t" \
      "v_cmp_ne_u32 vcc, 0, v223\n\t"
DI void gather_ml1(rsrc_t r, u32 a, u32 b, v4u& ml, v4u (&v)[4], u32 tag) {
  u64 P, N, x;
  u32 n;
  asm volatile(
      "s_movk_i32 %[n], 0x3fff\n\ts_mov_b64 %[P], -1\n\ts_mov_b64 %[N], 0\n"
      "1:\n\t"
      "s_mov_b64 exec, %[P]\n\t"
      "buffer_load_dwordx4 v[224:227], %[a], %[r], 0 offen sc1\n\t"
      "s_mov_b64 exec, -1\n\ts_waitcnt vmcnt(0)\n\t"
      ML_CHECK
      "v_bitop3_b32 v221, v225, v227, %[t] bitop3:0x7e\n\t"
      "s_and_b64 %[N], %[N], vcc\n\t"
      "v_cmp_ne_u32 %[x], 0, v221\n\t"
      "s_andn2_b64 vcc, %[P], %[x]\n\t"
      "s_or_b64 %[N], %[N], vcc\n\t"
      "s_mov_b64 exec, %[N]\n\ts_cbranch_execz 3f\n\t"
      ML_FETCH
      "3:\n\t"
      "s_and_b64 %[P], %[P], %[x]\n\t"
      "s_cbranch_scc0 2f\n\t"
      "s_sub_u32 %[n], %[n], 1\n\ts_cbranch_scc0 1b\n"
      "2:\n\ts_mov_b64 exec, -1"
      : "=&{v[224:227]}"(ml), "=&{v[228:231]}"(v[0]), "=&{v[232:235]}"(v[1]), "=&{v[236:239]}"(v[2]), "=&{v[240:243]}"(v[3]),
        [P] "=&s"(P), [N] "=&s"(N), [x] "=&s"(x), [n] "=&s"(n)
      : [a] "v"(a), [b] "v"(b), [r] "s"(r), [t] "s"(tag) : "memory", "scc", "vcc", "v221", "v222", "v223");
}
DI void gather_ml2(rsrc_t r, u32 b, v4u (&v)[4], u32 tag) {
  u32 n;
  asm volatile(
      "s_movk_i32 %[n], 0x3fff\n"
      "1:\n\ts_waitcnt vmcnt(0)\n\t"
      ML_CHECK
      "s_cbranch_vccz 2f\n\t"
      "s_mov_b64 exec, vcc\n\t"
      ML_FETCH
      "\ts_mov_b64 exec, -1\n\ts_sub_u32 %[n], %[n], 1\n\ts_cbranch_scc0 1b\n\ts_waitcnt vmcnt(0)\n"
      "2:"
      : "+{v[228:231]}"(v[0]), "+{v[232:235]}"(v[1]), "+{v[236:239]}"(v[2]), "+{v[240:243]}"(v[3]), [n] "=&s"(n)
      : [b] "v"(b), [r] "s"(r), [t] "s"(tag) : "memory", "scc", "vcc", "v221", "v222", "v223");
}

DI u32 hear(rsrc_t r, u32 line, u32 tag) {
  v2u t;
  u32 n;
  asm volatile("s_movk_i32 %1, 0x3fff\n1:\n\tbuffer_load_dwordx2 v[224:225], %2, %3, 0 offen sc1\n\ts_waitcnt vmcnt(0)\n\t"
               "v_cmp_ne_u32 vcc, %4, v225\n\ts_cbranch_vccz 2f\n\ts_sub_u32 %1, %1, 1\n\ts_cbranch_scc0 1b\n2:"
               : "=&{v[224:225]}"(t), "=&s"(n) : "v"(W_BELL + line * 128), "s"(r), "s"(tag) : "memory", "scc", "vcc");
  return t.x;
}
DI u32 hear4(rsrc_t r, u32 copy, int lane, u32 tag) {
  const u32 t = hear(r, (2 + (lane & (4 * NRING - 1))) * 16 + copy, tag), t0 = rfl(t);
  float d = max_x2(max_x1((float)(int)(t - t0)));
  if (NRING > 1) d = max_hm(d);
  if (NRING > 2) d = max_r8(d);
  return t0 + (int)asf(rfl(asu(d)));
}
DI void ring(rsrc_t r, u32 bell, int lane, u32 time, u32 tag) { gs8(r, lane < 16 ? W_BELL + (bell * 16 + lane) * 128 : OOB, v2u{time, tag}); }
DI void until(u32 t, bool low28 = false) {
  int d;
  do { d = low28 ? (int)((t - (u32)now()) << 4) >> 4 : (int)(t - (u32)now()); } while (d > 0 && d < 1000);
}

DI void at_time(Sync& S, u32 x) {
  S.wait<C_T0>(1);
  until((rfl(*(volatile LAS u32*)(S.base + 4 * C_T0)) >> 4) - PACE0 + x, true);
}

DI u32 late_margin(Sync& S, u32 t) {
  return t;
}

#define SB() __builtin_amdgcn_sched_barrier(0)
template <int D, int IMM> DI void dma(u32 v, rsrc_t r, u32 so) {
  asm volatile("buffer_load_dwordx4 %0, %1, %2 offen offset:%c3 nt lds\n\ts_waitcnt vmcnt(%c4)" :: "v"(v), "s"(r), "s"(so), "i"(IMM), "i"(D) : "memory");
  SB();
}
DI void set_m0(u32 ld) { asm volatile("s_mov_b32 m0, %0\n\ts_nop 0" :: "s"(ld) : "memory"); }
template <int D> DI void vmwait() { asm volatile("s_waitcnt vmcnt(%c0)" :: "i"(D) : "memory"); }
struct Ring {
  Sync S;
  u32 n, pos, need, full;
  bool ok;
  u32 due, lim;
  u64 stall, first;
  DI void publish(u32 f) { if ((int)(f - full) > 0) { full = f; S.set<C_FULL>(f); } }
  template <int D, bool LIN, class F> DI void fill(const u32 (&v)[8], rsrc_t r, u32 so, u32 step, F between) {
    if (!ok) {
      const u32 fa = S.base + 4 * pos;
      if (rfl(*(volatile LAS u32*)(fa + 4 * C_FREE)) < need) {
        vmwait<0>();
        publish(n);
        S.wait_at<4 * C_FREE>(fa, need);
      }
    }
    const u32 ld = pos * 8192u;
    u32 fr;
    set_m0(ld);
    dma<D, 0>(v[0], r, so);
    ++n; ++pos;
    if (pos == NH) { pos = 0; need += 4; }
    asm volatile("ds_read_b32 %0, %1 offset:%c2" : "=v"(fr) : "v"(S.base + 4 * pos), "i"(4 * C_FREE) : "memory");
    u32 t0v;
    asm volatile("ds_read_b32 %0, %1 offset:%c2" : "=v"(t0v) : "v"(S.base), "i"(4 * C_T0) : "memory");
    SB();
    dma<D, 1024>(v[1], r, LIN ? so : so + step - 1024);
    between();
    SB();
    dma<D, 2048>(v[2], r, LIN ? so : so + 2 * step - 2048);
    dma<D, 3072>(v[3], r, LIN ? so : so + 3 * step - 3072);
    set_m0(ld + 4096);
    dma<D, 0>(v[4], r, so + 4 * step);
    asm volatile("s_waitcnt lgkmcnt(0)" : "+v"(fr) :: "memory");
    ok = rfl(fr) >= need;
    asm volatile("" : "+v"(t0v));
    const u32 t0s = rfl(t0v);
    due = (t0s && n < lim && n >= 12) ? t0s + n * PACE - (n > KNEE ? ((n < 78 ? n : 78) - KNEE) * KD : 0u) : 0u;
    SB();
    dma<D, 1024>(v[5], r, LIN ? so + 4 * step : so + 5 * step - 1024);
    const u32 f = n - (D + 7) / 8;
    const bool pub = (int)(f - full) > 0;
    full = pub ? f : full;
    const u32 fv = opq(full);
    SB();
    dma<D, 2048>(v[6], r, LIN ? so + 4 * step : so + 6 * step - 2048);
    dma<D, 3072>(v[7], r, LIN ? so + 4 * step : so + 7 * step - 3072);
    S.set<C_FULL>(fv);
    if (due) { int d; do { d = (int)(due - ((u32)now() << 4)); } while (d > 0 && d < 16000); }
  }
};
DI rsrc_t mk(const void* p, u32 bytes) { return __builtin_amdgcn_make_buffer_rsrc((void*)p, 0, bytes, 0x00020000); }
template <int D> DI void ld_slab(Ring& R, rsrc_t r, u32 base, u32 pitch, int nh, int lane) {
  const u32 row = lane >> 3, v0 = row * pitch + (((lane & 7) ^ (((row >> 1) & 3) << 1)) << 4);
  const u32 v[8] = {v0, v0, v0, v0, v0, v0, v0, v0};
#pragma unroll 1
  for (int h = 0; h < nh; ++h) { R.fill<D, false>(v, r, base, 8 * pitch, [] {}); base += 64 * pitch; }
}
template <int D, int D2, int D3> DI void ld_rows(Ring& R, rsrc_t r, u32 base, u32 pitch, int sh, int lane, int H2, int H3, int HE = 16) {
  u32 v[8];
#pragma unroll
  for (int i = 0; i < 8; ++i) v[i] = ((lane ^ (i << 1)) << 4) + i * pitch - (i & 3) * 1024;
  int h = 0;
  u32 so = base;
  auto run = [&](auto dd, int end) {
#pragma unroll 1
    while (h < end) {
      const u32 cur = so;
      R.template fill<decltype(dd)::value, true>(v, r, cur, 0, [&] { ++h; so = base + (h >> sh) * 8 * pitch + (h & sh) * 1024; });
    }
  };
  run(std::integral_constant<int, D>{}, H2);
  if constexpr (D2 > 0) run(std::integral_constant<int, D2>{}, H3);
  if constexpr (D3 > 0) run(std::integral_constant<int, D3>{}, HE);
}

DI v4u xfrag(u32 addr, int k) { return cat(ldl<v2u>(addr + k * 64), ldl<v2u>(addr + k * 64 + 32)); }
template <int NK> DI v4f gemv_cols(u32 n0, u32 xaddr, int c, int lane) {
  const u32 g = lane >> 4, r16 = 4 * g + ((lane & 15) >> 2);
  const u32 a = opq(n0 * 8192 + r16 * 128 + ((c ^ ((r16 >> 1) & 3)) << 5) + (lane & 3) * 8), xa = opq(xaddr + g * 8);
  v4f acc = {0.f, 0.f, 0.f, 0.f};
#pragma unroll
  for (int k = 0; k < NK; ++k) acc = mfma(xfrag(xa, k), cat(trd(a + k * 4096), trd(a + k * 4096 + 2048)), acc);
  return acc;
}
DI void put_tile(rsrc_t ws, u32 off, v4f acc, u32 tag, int lane) {
  gs8(ws, lane < 16 ? off + lane * 8 : OOB, v2u{asu(acc.x), tag});
}
DI u32 hs_addr(u32 n) { return (n % NH) * 8192u; }
template <int SH, int O = 0, int NW = 8, int KONLY = -1> DI void read_rows(u32 n0, int v, int lane, v8u (&wf)[NW]) {
  const u32 g = lane >> 4, r8 = (4 * g + ((lane & 15) >> 2)) & 7;
  const u32 lo = r8 * 1024 + ((v ^ r8) << 5) + (lane & 3) * 8;
#pragma unroll
  for (int q = 0; q < 4; ++q) {
    const int half = SH ? q >> 1 : 0, k = SH ? q & 1 : q;
    if (KONLY >= 0 && k != KONLY) continue;
#pragma unroll
    for (int t = 0; t < 2; ++t) {
      const u32 h0 = opqu(hs_addr(n0 + ((4 * k + 2 * t) << SH) + half)), h1 = opqu(hs_addr(n0 + ((4 * k + 2 * t + 1) << SH) + half));
      const u32 a = lo + (lanes(M_HI32) ? h1 : h0);
#pragma unroll
      for (int u = 0; u < 4; ++u) {
        const v2u f = trd(a + u * 256);
        v8u& d = wf[O + 4 * (q >> 1) + u];
        d[4 * (k & 1) + 2 * t] = f.x; d[4 * (k & 1) + 2 * t + 1] = f.y;
      }
    }
  }
}
DI float pick4(float a0, float a1, float a2, float a3) {
  const bool odd = lanes(0xffff0000ffff0000ull);
  return sel(lanes(M_HI32), sel(odd, a3, a2), sel(odd, a1, a0));
}
DI v4f smfma(v4u x, v8u w, v4f c, u32 idx) {
  return __builtin_amdgcn_smfmac_f32_16x16x64_bf16(__builtin_bit_cast(v8bf, x), __builtin_bit_cast(v16bf, w), c, (int)idx, 0, 0);
}
DI u32 xs_addr(u32 base, int lane) { return opq(base + (lane >> 5) * 64 + ((lane >> 4) & 1) * 16 + (lane & 1) * 4); }
DI u32 xs_idx(int lane) { return opq((lane & 1) ? 0xeeeeu : 0x4444u); }
DI v4u xs_frag(u32 a) {
  v2u p, q;
  asm volatile("ds_read2_b32 %0, %2 offset1:8\n\tds_read2_b32 %1, %2 offset0:2 offset1:10\n\ts_waitcnt lgkmcnt(0)" : "=&v"(p), "=&v"(q) : "v"(a) : "memory");
  return cat(p, q);
}
template <int N> DI void zero_acc(v4f (&acc)[N]) {
#pragma unroll
  for (int i = 0; i < N; ++i) { acc[i] = v4f{0.f, 0.f, 0.f, 0.f}; if (i < 4) asm volatile("" : "+v"(acc[i])); }
}
template <int N> DI void put_tiles(rsrc_t ws, const u32 (&off)[N / 4], const v8u (&wf)[16], v4f (&acc)[N], v4u x, u32 idx, u32 tag) {
  auto put = [&](int q) {
    const int i = 4 * q;
    gs8(ws, off[q], v2u{asu(pick4(acc[i].x, acc[i + 1].x, acc[i + 2].x, acc[i + 3].x) + pick4(acc[i].y, acc[i + 1].y, acc[i + 2].y, acc[i + 3].y)), tag});
  };
#pragma unroll
  for (int i = 0; i < N; ++i) {
    acc[i] = smfma(x, wf[i], acc[i], idx);
    if (i + 4 < N) asm volatile("" : "+v"(acc[(i + 4) % N]));
    if ((i & 3) == 2 && i > 4) put(i / 4 - 1);
    __builtin_amdgcn_sched_barrier(0);
  }
  put(N / 4 - 1);
}

DI u32 next_tag(u32 e) { return e + 1u ? e + 1u : 1u; }
struct Ctx { rsrc_t ws; u32 tag; int lane, w, wg, grp, s; };

struct XIn { v4u xa, xb; u32 xp; };
DI XIn load_x(const Params& p, const Ctx& c) {
  return XIn{*(const GAS v4u*)(p.x + c.lane * 16), *(const GAS v4u*)(p.x + c.lane * 16 + 8),
             *(const GAS u32*)(p.x + (RH ? 16 * (c.wg >> 2) + 2 * (c.lane & 7) : 256 * (c.wg >> 6) + 64 * c.w + 2 * (c.lane & 31)))};
}
DI float make_ln(const XIn& X) {
  const v4u xa = X.xa, xb = X.xb;
  float sm = 0.f;
#pragma unroll
  for (int i = 0; i < 4; ++i) sm += (lo16(xa[i]) + hi16(xa[i])) + (lo16(xb[i]) + hi16(xb[i]));
  const float mean = sum64(sm) * (1.f / 1024.f);
  float q = 0.f;
#pragma unroll
  for (int i = 0; i < 4; ++i) {
    const float a = lo16(xa[i]) - mean, b = hi16(xa[i]) - mean, d = lo16(xb[i]) - mean, e = hi16(xb[i]) - mean;
    q += (a * a + b * b) + (d * d + e * e);
  }
  const float var = sum64(q) * (1.f / 1024.f);
  const float mb = rbf(mean), den = rbf(sqrtf(rbf(rbf(var) + 1e-5f)));
  return asf(opq(asu(rbf(rbf((lanes(M_HI32) ? hi16(X.xp) : lo16(X.xp)) - mb) / den))));
}
DI void make_h(const Params& p, const Ctx& c, Sync& S, float ln) {
  const int lane = c.lane, kq = c.wg >> 6;
  const u32 base = opq(W_MOD1 + (32 * (lane >> 5) + 4 * kq + c.w) * 512 + (lane & 31) * 16);
  const u32 ha = opq(L_HS + (64 * c.w + 2 * (lane & 31) + (lane >> 5)) * 2);
  v4u v[8];
  nap<NAP0A>();
  gather16<8, 4>(S, c.ws, base, [](int i) { return (u32)(64 * (i & 3) + 16 * (i >> 2)) * 512; }, v, c.tag);
  if (c.w == 0) { STAMP(17); }
  float s0 = (asf(v[0].x) + asf(v[1].x)) + (asf(v[2].x) + asf(v[3].x)), c0 = (asf(v[4].x) + asf(v[5].x)) + (asf(v[6].x) + asf(v[7].x));
  s0 += sw32(s0, (asf(v[0].z) + asf(v[1].z)) + (asf(v[2].z) + asf(v[3].z)));
  c0 += sw32(c0, (asf(v[4].z) + asf(v[5].z)) + (asf(v[6].z) + asf(v[7].z)));
  const float h = rbf(ln * rbf(1.f + rbf(c0))) + rbf(s0);
  stl<u16>(ha, (u16)pk(h, h));
  S.add1<C_H, 1>();
}
DI void make_q(const Params& p, const Ctx& c, Sync& S) {
  const int lane = c.lane;
  v4u v[8];
  const u32 base = W_Q + (16 * c.grp + 4 * c.w + (lane >> 5)) * 512 + (lane & 31) * 16;
  gather16<8, 4>(S, c.ws, base, [](int i) { return (u32)(64 * (i & 3) + 2 * (i >> 2)) * 512; }, v, c.tag);
#pragma unroll
  for (int i = 0; i < 8; i += 4)
    stl<u32>(L_QS + (128 * c.w + 16 * i + lane) * 4,
             pk((asf(v[i].x) + asf(v[i + 1].x)) + (asf(v[i + 2].x) + asf(v[i + 3].x)), (asf(v[i].z) + asf(v[i + 1].z)) + (asf(v[i + 2].z) + asf(v[i + 3].z))));
  S.add1<C_Q, 1>();
}

DI void combine_part(const Params& p, const Ctx& c, Sync& S) {
  const int lane = c.lane, a = 16 * c.grp + c.s, sl = 8 * c.w + (lane >> 3);
  const u32 base = opq(W_P + (a * 64 + sl) * PSTR + (lane & 7) * 16), mla = opq(MRS ? W_ML + (c.grp * 64 + sl) * 128 + (c.s >> 1) * 16 : W_MR + (a * 64 + sl) * 128);
  const int dim = 2 * (lane & 7) + 32 * (lane >> 5) + 16 * ((lane >> 4) & 1) + ((lane >> 3) & 1);
  const u32 ra = opq(L_QS + (c.w * 64 + dim) * 4), wa = opq(L_CS + sl * 8), wb = opq(L_CS + lane * 8);
  v4u ml, v[4];
  {
    const u32 t0 = rfl(*(volatile LAS u32*)(S.base + 4 * C_T0));
    if (t0) until(((t0 + 77 * PACE - (77 - KNEE) * KD) >> 4) + T1, true);
  }
  gather_ml1(c.ws, mla, base, ml, v, c.tag);
  stl<v2u>(wa, v2u{ml.x, ml.z});
  LGKM0();
  __builtin_amdgcn_s_barrier();
  CBAR();
  const u32 tb = (u32)now();
  const v2f all = ldl<v2f>(wb);
  const float M = max32(max16(max_r8(max_hm(max_x2(max_x1(all.x))))));
  const float wt = asf(opq(asu(ex2((asf(ml.x) - M) * LOG2E) * rcp(sum64(ex2((all.x - M) * LOG2E) * all.y)))));
  gather_ml2(c.ws, base, v, c.tag);
  TS(t8);
  if (c.w == 1 && (c.s & (16 / NRING - 1)) == 0) ring(c.ws, 2 + c.grp * NRING + c.s / (16 / NRING), lane, tb, c.tag);
  STAMP(8);
  float z0 = lo16(v[0].x) * wt, z1 = hi16(v[0].x) * wt, z2 = lo16(v[0].z) * wt, z3 = hi16(v[0].z) * wt;
  const float y0 = lo16(v[1].x) * wt, y1 = hi16(v[1].x) * wt, y2 = lo16(v[1].z) * wt, y3 = hi16(v[1].z) * wt;
  z0 += sw32(z0, y0); z1 += sw32(z1, y1); z2 += sw32(z2, y2); z3 += sw32(z3, y3);
  z0 += sw16(z0, z2); z1 += sw16(z1, z3);
  const bool up = lanes(M_HI8);
  stl<float>(ra, sel(up, z1, z0) + dpp<ROR8>(sel(up, z0, z1)));
  LGKM0();
  __builtin_amdgcn_s_barrier();
  CBAR();
}
DI float sum8(u32 a) {
  float r[8];
#pragma unroll
  for (int i = 0; i < 8; ++i) r[i] = ldl<float>(a + i * 256);
  return ((r[0] + r[1]) + (r[2] + r[3])) + ((r[4] + r[5]) + (r[6] + r[7]));
}

DI void stage_B(const Params& p, const Ctx& c, Sync& S, int m, int kb, int cb, u32 n0, v8u (&wf)[16]) {
  const int lane = c.lane, w = c.w;
  S.wait<C_FULL>(n0 + 8);
  read_rows<0, 0, 16, 0>(n0, w, lane, wf);
  read_rows<0, 0, 16, 1>(n0, w, lane, wf);
  S.wait<C_FULL>(n0 + 16 - NL2);
  if (NL2) S.wait<C_L2>(1);
  read_rows<0, 0, 16, 2>(n0, w, lane, wf);
  read_rows<0, 0, 16, 3>(n0, w, lane, wf);
  const int tile = w + 8 * (lane >> 4);
  const u32 xa = xs_addr(L_PRIV, lane), ix = xs_idx(lane);
  const u32 oo = opq(W_GU + ((8 * cb + (tile >> 2)) * 16 + 2 * kb + m) * 512 + (tile & 3) * 128 + (lane & 15) * 8);
  v4f acc[4];
  zero_acc(acc);
  if (w == 4) {
    const v2u cs = ldl<v2u>(L_CS + lane * 8);
    const int P = 64 * kb + lane;
    const u32 pa = opq(L_PRIV + lane * 4);
    const u32 o0 = opq(W_X + (P & ~1) * 8), o1 = opq(W_X + 4096 + lane * 16);
    LGKM0();
    TS(t12);
    STAMP(12);
    until(rfl(hear(c.ws, (c.wg >> 3) & 15, c.tag)) + T3);
    v4u q[2];
    gather2<1>(c.ws, o1, o0, q, c.tag);
    TS(t13);
    STAMP(13);
    const u32 xg = asu(sel(lanes(0xaaaaaaaaaaaaaaaaull), asf(q[1].z), asf(q[1].x)));
    const float mu0 = asf(rfl(q[0].x));
    float d = asf(q[0].x) - mu0;
    d += sw32(d, asf(q[0].z) + 16.f * d * d);
    d = sum_lo3(d); d += dpp<ROR8>(d); d = sum16(d);
    const float e = sw32(d, d) * (1.f / 1024.f);
    d *= 1.f / 64.f;
    const float mb = rbf(mu0 + d), inv = rcp(rbf(__builtin_amdgcn_sqrtf(rbf(rbf(fmaxf(e - d * d, 0.f)) + 1e-5f))));
    const u32 xm = pk(lo16(xg) - mb, hi16(xg) - mb), ln = pk(lo16(xm) * inv, hi16(xm) * inv);
    const u32 sc = pk(lo16(ln) * lo16(cs.x), hi16(ln) * lo16(cs.y));
    stl<u32>(pa, pk(lo16(sc) + hi16(cs.x), hi16(sc) + hi16(cs.y)));
  }
  LGKM0();
  __builtin_amdgcn_s_barrier();
  CBAR();
  const v4u x0 = xs_frag(xa), x1 = xs_frag(xa + 128);
#pragma unroll
  for (int u = 0; u < 4; ++u) acc[u] = smfma(x0, wf[u], acc[u], ix);
#pragma unroll
  for (int u = 0; u < 4; ++u) acc[u] = smfma(x1, wf[4 + u], acc[u], ix);
  gs8(c.ws, oo, v2u{asu(pick4(acc[0].x, acc[1].x, acc[2].x, acc[3].x) + pick4(acc[0].y, acc[1].y, acc[2].y, acc[3].y)), c.tag});
  TS(t14);
  STAMP(14);
}

DI void stage_D(const Params& p, const Ctx& c, Sync& S, int t, u32 n0, v8u (&wf)[16]) {
  const int lane = c.lane, w = c.w;
  S.wait<C_FULL>(n0 + 8);
  read_rows<1, 0, 16, 0>(n0, w, lane, wf);
  S.wait<C_FULL>(n0 + 16 - NL2);
  if (NL2) S.wait<C_L2>(1);
  read_rows<1, 0, 16, 1>(n0, w, lane, wf);
  const u32 pa = opq(L_PRIV + w * 512 + lane * 2), xa = xs_addr(L_PRIV + w * 512, lane), ix = xs_idx(lane);
  const u32 oi = opq(W_GU + (t * 16 + 2 * w) * 512 + lane * 16);
  const u32 wa = opq(L_CS + w * 512 + (lane & 31) * 16 + (lane >> 5) * 4), ra = opq(L_CS + lane * 8);
  const u32 oo = opq(W_D + ((w + 8 * (lane >> 4)) * 56 + t) * 128 + (lane & 15) * 8);
  const u32 of[2] = {oo, opq(oo + 32 * 56 * 128)};
  v4f acc[8];
  zero_acc(acc);
  LGKM0();
  TS(t27);
  STAMP(27);
  if (w == 4) until(rfl(hear(c.ws, 16 + (t & 15), c.tag)) + T4);
  __builtin_amdgcn_s_barrier();
  v4u q[1];
  gather1<2>(c.ws, oi, q, c.tag);
  TS(t28);
  STAMP(28);
  stl<u32>(wa, q[0].x);
  stl<u32>(wa + 8, q[0].z);
  LGKM0();
  __builtin_amdgcn_s_barrier();
  CBAR();
  v2f r[8];
#pragma unroll
  for (int i = 0; i < 8; ++i) r[i] = ldl<v2f>(ra + i * 512);
  const v2f y = ((r[0] + r[1]) + (r[2] + r[3])) + ((r[4] + r[5]) + (r[6] + r[7]));
  const u32 gu = pk(y.x, y.y);
  const float g = lo16(gu);
  const float z = (-2.f * LOG2E * 0.7978845608028654f) * (g + 0.044715f * g * g * g);
  const float h = rbf(g * rcp(1.f + ex2(z))) * hi16(gu);
  stl<u16>(pa, (u16)pk(h, h));
  CBAR();
  put_tiles<8>(c.ws, of, wf, acc, xs_frag(xa), ix, c.tag);
  TS(t29);
  STAMP(29);
}

struct Att {
  v4u qf[4];
  v4f acc[8];
  float m, l;
  u32 kb, vb, ma, xa;
  DI void init(int lane) {
    const u32 g = lane >> 4, j = lane & 15, r16 = 4 * g + (j >> 2);
    kb = j * 256 + ((g ^ j) << 4);
    vb = r16 * 256 + ((r16 & 7) << 5) + (lane & 3) * 8;
    ma = opq(L_MRG + lane * 16); xa = opq(L_MRG + 4096 + (lane & 7) * 32);
    m = -INFINITY; l = 0.f;
  }
  DI void load_q(int lane) {
#pragma unroll
    for (int k = 0; k < 4; ++k) qf[k] = ldl<v4u>(L_QS + (lane & 7) * 256 + k * 64 + (lane >> 4) * 16);
  }
  DI static void release(Sync& S, u32 n) { S.set_at<4 * C_FREE>(S.base + 4 * (n % NH), n / NH * 4 + 4); }
  DI void read_k(Sync& S, u32 n, v4u (&kf)[8]) {
    const u32 ka = n % NH * 8192;
    S.wait<C_FULL>(n + 1);
#pragma unroll
    for (int k = 0; k < 4; ++k) { const u32 a = ka + (kb ^ (k << 6)); kf[k] = ldl<v4u>(a); kf[4 + k] = ldl<v4u>(a + 4096); }
    release(S, n);
  }
  DI v4u scores(const v4u (&kf)[8]) {
    v4f s0 = {0.f, 0.f, 0.f, 0.f}, s1 = s0;
#pragma unroll
    for (int k = 0; k < 4; ++k) { s0 = mfma(kf[k], qf[k], s0); s1 = mfma(kf[4 + k], qf[k], s1); }
    v4u r;
#pragma unroll
    for (int i = 0; i < 4; ++i) {
      const u32 a = i < 2 ? pk(s0[2 * i], s0[2 * i + 1]) : pk(s1[2 * i - 4], s1[2 * i - 3]);
      r[i] = pk(lo16(a) * 0.08838834764831845f, hi16(a) * 0.08838834764831845f);
      m = fmx3(m, lo16(r[i]), hi16(r[i]));
    }
    return r;
  }
  DI void share_max(Sync& S, u32 slot, u32 spare, u32 nw) {
    m = max32(max16(m));
    stl<float>(xa + 4 * spare, m);
    stl<float>(xa + 4 * slot, m);
    S.add1<C_MX, 1>();
    S.wait<C_MX>(nw);
    const v4f a = ldl<v4f>(xa), b = ldl<v4f>(xa + 16);
    m = fmx3(fmx3(a.x, a.y, a.z), fmx3(a.w, b.x, b.y), fmx(b.z, b.w));
  }
  DI v4u probs(v4u sc) {
    float e[8];
#pragma unroll
    for (int i = 0; i < 4; ++i) { e[2 * i] = ex2((lo16(sc[i]) - m) * LOG2E); e[2 * i + 1] = ex2((hi16(sc[i]) - m) * LOG2E); }
    l += ((e[0] + e[1]) + (e[2] + e[3])) + ((e[4] + e[5]) + (e[6] + e[7]));
    return v4u{pk(e[0], e[1]), pk(e[2], e[3]), pk(e[4], e[5]), pk(e[6], e[7])};
  }
  DI void share_sum(Sync& S, u32 slot, u32 spare) {
    stl<float>(xa + 256 + 4 * spare, 0.f);
    stl<float>(xa + 256 + 4 * slot, sum32(sum16(l)));
    S.add1<C_LS, 1>();
#pragma unroll
    for (int b = 0; b < 8; ++b) acc[b] = v4f{0.f, 0.f, 0.f, 0.f};
  }
  DI void values(Sync& S, u32 n, v4u pf) {
    u32 a[8];
#pragma unroll
    for (int b = 0; b < 8; ++b) a[b] = opq(n % NH * 8192 + (vb ^ (b << 5)));
    S.wait<C_FULL>(n + 1);
    v4u vf[8];
#pragma unroll
    for (int b = 0; b < 8; ++b) vf[b] = cat(trd(a[b]), trd(a[b] + 4096));
    release(S, n);
#pragma unroll
    for (int b = 0; b < 8; ++b) acc[b] = mfma(vf[b], pf, acc[b]);
  }
  DI void hand_on(Sync& S, u32 order) {
    const bool hi = lanes(M_HI8);
    v4f o[4];
#pragma unroll
    for (int b2 = 0; b2 < 4; ++b2) {
      const v4f a = acc[2 * b2], b = acc[2 * b2 + 1];
      o[b2] = v4f{sel(hi, a.z, a.x), sel(hi, a.w, a.y), sel(hi, b.z, b.x), sel(hi, b.w, b.y)};
    }
    if (order > 0) {
      S.wait<C_M>(order);
#pragma unroll
      for (int b2 = 0; b2 < 4; ++b2) o[b2] += ldl<v4f>(ma + b2 * 1024);
    }
#pragma unroll
    for (int b2 = 0; b2 < 4; ++b2) stl<v4f>(ma + b2 * 1024, o[b2]);
    S.set<C_M>(order + 1);
  }
};

template <int N> DI float col_total(const v4u (&q)[N]) {
  float s0 = asf(q[0].x), s1 = asf(q[0].z);
#pragma unroll
  for (int i = 1; i < N; ++i) { s0 += asf(q[i].x); s1 += asf(q[i].z); }
  s0 += sw32(s0, s1);
  s0 = sum16(s0);
  return s0 + dpp<ROR8>(s0);
}

__global__ void __launch_bounds__(512) fused_block(Params p) {
  const int tid = threadIdx.x, lane = tid & 63, w = rfl(tid >> 6), wg = blockIdx.x;
  if (w == 6) {
    u64 pc;
    u32 len, t;
    asm volatile("s_getpc_b64 %0\npf_here:\n\ts_mov_b32 %1, pf_end-pf_here" : "=s"(pc), "=s"(len));
    len = len < 65536u ? len : 0u;
    asm volatile("buffer_load_dword %0, %1, %2, 0 offen" : "=&v"(t) : "v"(lane < CODEPF ? ((wg >> 3) * CODEPF + lane) * 128u : OOB), "s"(mk((const void*)pc, len)) : "memory");
    vmwait<0>();
  }
  const int grp = (wg >> 3) & 3, s = (wg & 7) + ((wg >> 5) << 3);
  const bool isA = s < 16;
  const int lam = (wg >> 3) - 8;
  const int role = isA ? 0 : lam < 14 ? 1 : lam < 21 ? 2 : 3;
  const int tt = (wg & 7) + 8 * (lam % 7), r = 16 * grp + s, v = w & 3;
  const u32 nkv = isA ? 26 : 10, NT = isA ? 26 : 34, ntail = nkv + 2 * NT;
  const u32 tstart = (u32)now();
  Sync S{opq(L_CTRL)};
  if (w == 0) {
    S.set_at<0>(S.base + 4 * lane, 0u); }
  Ctx c;
  c.ws = mk(p.ws, W_END);
  c.lane = lane; c.w = w; c.wg = wg; c.grp = grp; c.s = s;
  v8u wa[16];
  auto take_wo0 = [&]() {
    read_rows<1, 0, 16, 0>(10, v, lane, wa);
    read_rows<1, 8, 16, 0>(10, v + 4, lane, wa);
    S.add1<C_FREE + 10, 8>();
  };
  auto take_wo1 = [&]() {
    read_rows<1, 0, 16, 1>(10, v, lane, wa);
    read_rows<1, 8, 16, 1>(10, v + 4, lane, wa);
    S.add1<C_FREE, 8>();
  };

  if (w == 7) {
    __builtin_amdgcn_s_setprio(3);
    __builtin_amdgcn_s_waitcnt(0x0F70);
    Ring R{S, 0, 0, 0, 0, true, 0, ntail + (role == 2 ? DPACE : 0)};
    const rsrc_t rm = mk(p.wmod, 1024 * 6144 * 2);
    ld_slab<DEPTH>(R, rm, (wg >> 5) * 128 * 12288 + (wg & 31) * 128, 12288, 2, lane);
    __builtin_amdgcn_s_barrier();
    ld_slab<DEPTH>(R, mk(p.wq, 1024 * 4096 * 2), (wg >> 6) * 256 * 8192 + (wg & 63) * 128, 8192, 4, lane);
    ld_slab<DEPTH>(R, rm, (wg >> 6) * 256 * 12288 + 4096 + (wg & 63) * 128, 12288, 4, lane);
    if (isA) ld_rows<DEPTH, 0, 0>(R, mk(p.wo, 4096 * 1024 * 2), r * 64 * 2048, 2048, 1, lane, 16, 16);
    const rsrc_t rk = mk(p.K, 1u << 26), rv = mk(p.V, 1u << 26);
    const u32 rw = lane >> 4, ps = lane & 15;
    u32 vk[8], vv[8];
#pragma unroll
    for (int i = 0; i < 8; ++i) { vk[i] = rw * 256 + ((ps ^ (4 * (i & 3) + rw)) << 4); vv[i] = rw * 256 + ((ps ^ ((4 * (i & 1) + rw) << 1)) << 4); }
    const u32 tab = ((u32)grp << 24) + ((selu(lane < 8, lane * 48 + (u32)(s + 32 + RR * lane) % 48, 384 + (lane - 8) * 64 + ((s + RR * lane) & 63)) + ROT * grp) & 2047) * 8192;
    int tau = isA ? 8 : 0;
    u32 so = __builtin_amdgcn_readlane(tab, tau);
    asm volatile("s_nop 4" : "+s"(so));
    const int tau0 = tau;
    auto ldk = [&]() { const u32 cur = so; R.fill<DK, true>(vk, rk, cur, 1024, [&] { ++tau; so = __builtin_amdgcn_readlane(tab, tau < 34 ? tau : tau0); }); };
    if (isA) {
      static_assert(DEPTH <= 32 && DK <= 32, "landed by then");
      take_wo0();
      ldk(); ldk(); ldk(); ldk();
      take_wo1();
    }
#pragma unroll 1
    while (tau < 34) ldk();
    tau = tau0;
#pragma unroll 1
    while (DV != DEPTH && tau < 34 - NLAST) { const u32 cur = so; R.fill<DV, true>(vv, rv, cur, 1024, [&] { ++tau; so = __builtin_amdgcn_readlane(tab, tau); }); }
#pragma unroll 1
    while (tau < 34) { const u32 cur = so; R.fill<DEPTH, true>(vv, rv, cur, 1024, [&] { ++tau; so = __builtin_amdgcn_readlane(tab, tau); }); }
    STAMP(3);
    if (role == 1 || role == 2) {
      const bool gu = role == 1;
      ld_rows<DEPTH2, DEPTH3, DEPTH4>(R, mk(gu ? (lam < 7 ? p.wg : p.wu) : p.wd, 1024 * 3584 * 2),
                                                gu ? (wg & 7) * 128 * 7168 + (lam % 7) * 1024 : tt * 64 * 2048, gu ? 7168 : 2048, gu ? 0 : 1, lane, gu ? H2_ : H2D, gu ? H3_ : H3D, 16 - NL2);
    }
    vmwait<32>(); R.publish(R.n - 4);
    vmwait<24>(); R.publish(R.n - 3);
    vmwait<16>(); R.publish(R.n - 2);
    vmwait<8>(); R.publish(R.n - 1);
    vmwait<0>(); R.publish(R.n);
    STAMP(4);
    __builtin_amdgcn_s_setprio(0);
    c.tag = rfl(*(volatile LAS u32*)(S.base + 4 * C_TAG));
  } else if (w < 4) {
    const u32 ep = __hip_atomic_load((GAS u32*)(p.ws + W_EPOCH), __ATOMIC_RELAXED, __HIP_MEMORY_SCOPE_AGENT);
    const u32 c1 = *(const GAS u32*)(p.c + (wg >> 5) * 128 + 2 * lane);
    const XIn X = load_x(p, c);
    const u32 pa = L_PRIV + w * 256;
    LGKM0();
    __builtin_amdgcn_s_barrier();
    CBAR();
    stl<u32>(pa + lane * 4, silu2(c1));
    c.tag = next_tag(rfl(ep));
    S.set<C_TAG>(c.tag);
    if (w == 3) gs16(c.ws, (wg < 64 && lane == 0) ? W_T0 + wg * 16 : OOB, v4u{tstart, c.tag, 0u, c.tag});
    S.wait<C_FULL>(2);
    put_tile(c.ws, W_MOD1 + wg * 512 + w * 128, gemv_cols<4>(0, pa, w, lane), c.tag, lane);
    S.add1<C_FREE, 2>();
    if (w == 0) { STAMP(1); }
    make_h(p, c, S, make_ln(X));
    S.wait<C_FULL>(6);
    S.wait<C_H>(4);
    if (w == 0) { STAMP(18); }
    put_tile(c.ws, W_Q + wg * 512 + w * 128, gemv_cols<8>(2, L_HS, w, lane), c.tag, lane);
    S.add1<C_FREE + 2, 4>();
    if (w == 0) { STAMP(19); }
    nap<NAP0B>();
    make_q(p, c, S);
    S.wait<C_Q>(4);
    if (w == 0) { STAMP(2); }
    Att A;
    A.init(lane);
    A.load_q(lane);
    const u32 it0 = isA ? 0 : 12, ord0 = isA ? 0 : 3, order = (w + it0 - NT + 1) & 3;
    const u32 cnt = (NT - 2 - it0 - w) / 4 + 1 + (order == 3);
    v32u sc;
#pragma unroll 1
    for (u32 k = 0; k < cnt; ++k) {
      v4u f[8];
      A.read_k(S, nkv + min(it0 + w + 4 * k, NT - 1), f);
      const v4u x = A.scores(f);
      sc[4 * k] = x.x; sc[4 * k + 1] = x.y; sc[4 * k + 2] = x.z; sc[4 * k + 3] = x.w;
    }
    A.share_max(S, w, isA ? w + 4 : w, ord0 + 4);
#pragma unroll 1
    for (u32 k = 0; k < cnt; ++k) {
      const v4u x = A.probs(v4u{sc[4 * k], sc[4 * k + 1], sc[4 * k + 2], sc[4 * k + 3]});
      sc[4 * k] = x.x; sc[4 * k + 1] = x.y; sc[4 * k + 2] = x.z; sc[4 * k + 3] = x.w;
    }
    A.share_sum(S, w, isA ? w + 4 : w);
    const u32 nv = cnt - (order == 3);
#pragma unroll 1
    for (u32 k = 0; k < nv; ++k) A.values(S, nkv + NT + it0 + w + 4 * k, v4u{sc[4 * k], sc[4 * k + 1], sc[4 * k + 2], sc[4 * k + 3]});
    if (order < 3) {
      A.hand_on(S, ord0 + order);
    } else {
      const u32 g = lane >> 4, j = lane & 15;
      const bool hi = lanes(M_HI8);
      const v4u p1 = {sc[4 * nv], sc[4 * nv + 1], sc[4 * nv + 2], sc[4 * nv + 3]};
      const u32 o = opq(W_P + ((16 * grp + 2 * (j & 7)) * 64 + s) * PSTR + g * 32 + (j & 8) * 2), o2 = opq(o + 64 * PSTR);
      const u32 om = opq(MRS ? (lane < 8 ? W_ML + (64 * grp + s) * 128 + lane * 16 : OOB) : lane < 16 ? W_MR + ((16 * grp + 2 * (lane & 7) + (lane >> 3)) * 64 + s) * 128 : OOB);
      TS(t24);
      S.wait<C_LS>(ord0 + 4);
      const v4f la = ldl<v4f>(A.xa + 256), lb = ldl<v4f>(A.xa + 272);
      S.wait<C_M>(ord0 + 3);
      v4f y[4];
#pragma unroll
      for (int b2 = 0; b2 < 4; ++b2) { y[b2] = ldl<v4f>(A.ma + b2 * 1024); asm volatile("" : "+v"(y[b2])); }
      TS(t25);
      gs16(c.ws, om, v4u{asu(A.m), c.tag, asu(((la.x + la.y) + (la.z + la.w)) + ((lb.x + lb.y) + (lb.z + lb.w))), c.tag});
      STAMP(24);
      STAMP(25);
      A.values(S, ntail - 1, p1);
      STAMP(5);
#pragma unroll
      for (int b = 0; b < 8; b += 2)
        gs16(c.ws, (b < 4 ? o : o2) + (b & 2) * 64,
             v4u{pk(sel(hi, A.acc[b].z, A.acc[b].x) + y[b >> 1].x, sel(hi, A.acc[b].w, A.acc[b].y) + y[b >> 1].y), c.tag,
                 pk(sel(hi, A.acc[b + 1].z, A.acc[b + 1].x) + y[b >> 1].z, sel(hi, A.acc[b + 1].w, A.acc[b + 1].y) + y[b >> 1].w), c.tag});
      TS(t6);
      TSW(25, t25); TSW(6, t6); TSW(5, A.tv); TSW(24, t24);
      STAMP(6);
    }
  } else {
    const u32 ep = __hip_atomic_load((GAS u32*)(p.ws + W_EPOCH), __ATOMIC_RELAXED, __HIP_MEMORY_SCOPE_AGENT);
    const v2u c2 = *(const GAS v2u*)(p.c + (wg >> 6) * 256 + 4 * lane);
    const u32 pa = L_PRIV + 1024 + v * 512;
    stl<v2u>(pa + lane * 8, v2u{silu2(c2.x), silu2(c2.y)});
    __builtin_amdgcn_s_barrier();
    c.tag = next_tag(rfl(ep));
    if (w == 4 && lam == 21) {
      const u32 o = (lane & 31) * 0x200000u + (wg & 7) * 4096 + 2048, o2 = (lane & 3) * 0x200000u + (wg & 7) * 4096 + 2048;
      u32 t0, t1, t2, t3, t4, t5;
      asm volatile("buffer_load_dword %0, %6, %8, 0 offen nt\n\tbuffer_load_dword %1, %6, %9, 0 offen nt\n\t"
                   "buffer_load_dword %2, %7, %10, 0 offen nt\n\tbuffer_load_dword %3, %7, %11, 0 offen nt\n\t"
                   "buffer_load_dword %4, %7, %12, 0 offen nt\n\tbuffer_load_dword %5, %7, %13, 0 offen nt\n\ts_waitcnt vmcnt(0)"
                   : "=&v"(t0), "=&v"(t1), "=&v"(t2), "=&v"(t3), "=&v"(t4), "=&v"(t5)
                   : "v"(o), "v"(o2), "s"(mk(p.K, 1u << 26)), "s"(mk(p.V, 1u << 26)), "s"(mk(p.wo, 4096 * 1024 * 2)),
                     "s"(mk(p.wg, 1024 * 3584 * 2)), "s"(mk(p.wu, 1024 * 3584 * 2)), "s"(mk(p.wd, 1024 * 3584 * 2)) : "memory");
    }
    if (w == 5) {
      v4u q[1];
      S.wait<C_FULL>(4);
      gather16<1, 8>(S, c.ws, W_T0 + lane * 16, [](int) { return 0u; }, q, c.tag);
      const float d = sum64((float)(int)(q[0].x - tstart)) * (1.f / 64.f);
      S.set<C_T0>(((tstart + (int)d + PACE0) << 4) | 1u);
    }
    S.wait<C_FULL>(10);
    put_tile(c.ws, W_MOD2 + wg * 512 + v * 128, gemv_cols<8>(6, pa, v, lane), c.tag, lane);
    if (v == 0) { put_tile(c.ws, W_MOD2 + wg * 512 + 3 * 128, gemv_cols<8>(6, pa, 3, lane), c.tag, lane); S.add1<C_FREE + 6, 4>(); }
    S.add1<C_FREE + 6, 4>();
    if (role == 0) {
      S.wait<C_FULL>(18);
      take_wo0();
      S.wait<C_FULL>(26);
      take_wo1();
      S.wait<C_Q>(4);
    } else {
      Att A;
      A.init(lane);
      v4u kf[4][8], sc[4];
#pragma unroll
      for (int i = 0; i < 4; ++i) A.read_k(S, 10 + v + 3 * i, kf[i]);
      S.wait<C_Q>(4);
      A.load_q(lane);
#pragma unroll
      for (int i = 0; i < 4; ++i) sc[i] = A.scores(kf[i]);
      A.share_max(S, w, v == 2 ? 7 : w, 7);
#pragma unroll
      for (int i = 0; i < 4; ++i) sc[i] = A.probs(sc[i]);
      A.share_sum(S, w, v == 2 ? 7 : w);
#pragma unroll 1
      for (int i = 0; i < 4; ++i) {
        A.values(S, 44 + v + 3 * i, sc[0]);
        sc[0] = sc[1]; sc[1] = sc[2]; sc[2] = sc[3];
      }
      A.hand_on(S, v);
    }
    if (role == 0) {
      if (w == 4) {
      v4u q[2];
      const u32 xp = *(const GAS u32*)(p.x + 16 * r + 2 * (lane & 7));
      gather16<2, 8>(S, c.ws, W_MOD2 + (64 * ((lane >> 3) & 3) + 32 * (lane >> 5) + (r >> 2)) * 512 + ((r & 3) * 16 + 2 * (lane & 7)) * 8,
                     [](int i) { return (u32)i * 16 * 512; }, q, c.tag);
      float y[4] = {asf(q[0].x), asf(q[0].z), asf(q[1].x), asf(q[1].z)}, o[4];
#pragma unroll
      for (int i = 0; i < 4; ++i) { y[i] += dpp<ROR8>(y[i]); y[i] = rbf(sum16(y[i])); o[i] = sw32(y[i], y[i]); }
      stl<v4f>(L_HS + (lane & 7) * 16, v4f{y[0], o[2], lo16(xp), 0.f});
      stl<v4f>(L_HS + 128 + (lane & 7) * 16, v4f{y[1], o[3], hi16(xp), 0.f});
      gs16(c.ws, lane < 8 ? W_CS + r * 128 + lane * 16 : OOB, v4u{pk(1.f + o[0], y[2]), c.tag, pk(1.f + o[1], y[3]), c.tag});
      }
    } else if (role == 1 && w == 4) {
      v4u q[1];
      nap<NAPCS>(); nap<NAPCS>();
      gather16<1, 32>(S, c.ws, W_CS + (128 * (wg & 7) + 2 * lane) * 8, [](int) { return 0u; }, q, c.tag);
      stl<v2u>(L_CS + lane * 8, v2u{q[0].x, q[0].z});
    }
  }

  if (role != 0) {
    if (role == 1) stage_B(p, c, S, lam >= 7, wg & 7, lam % 7, ntail, wa);
    else if (role == 2) stage_D(p, c, S, tt, ntail, wa);
  } else {
    const u32 pa = L_PRIV + w * 512;
    const u32 xa = xs_addr(pa, lane), ix = xs_idx(lane), oo = opq(W_A + ((v + 8 * (lane >> 4)) * 64 + r) * 128 + (lane & 15) * 8);
    const u32 o1 = opq(oo + 32 * 8192), o2 = opq(oo + 4 * 8192), o3 = opq(oo + 36 * 8192);
    const u32 ra = opq(L_QS + lane * 4), wr = opq(pa + lane * 2);
    v4f acc[16];
    zero_acc(acc);
    S.wait<C_FULL>(ntail);
    if (w == 4) { STAMP(7); }
    combine_part(p, c, S);
    if (w >= 4) {
      const float o = sum8(ra);
      stl<u16>(wr, (u16)pk(o, o));
      CBAR();
      const u32 of[4] = {oo, o1, o2, o3};
      put_tiles<16>(c.ws, of, wa, acc, xs_frag(xa), ix, c.tag);
      TS(t9); if (w == 7) TSW(9, t9);
      STAMP(9);
    } else if (w == 2) {
      if (r < 2) ring(c.ws, r, lane, late_margin(S, hear4(c.ws, 0, lane, c.tag)), c.tag);
    } else if (w == 3) {
      const v4f kp = ldl<v4f>(L_HS + (lane >> 5) * 128 + (lane & 7) * 16);
      const u32 ox = opq(selu(lane < 8, W_X + r * 64 + lane * 8, selu(lane < 10, W_X + 4096 + r * 16 + (lane - 8) * 8, OOB)));
      const u32 ia = opq(W_A + (r * 64 + (lane >> 3)) * 128 + (lane & 7) * 16);
      const u32 t0 = late_margin(S, hear4(c.ws, r >> 2, lane, c.tag));
      TS(t21);
      until(t0 + T2);
      TS(t20);
      v4u q[8];
      gather8<1>(c.ws, ia, q, c.tag);
      TS(t10);
      STAMP(10);
      const float a = rbf(kp.z + rbf(kp.x * rbf(col_total(q))));
      float a0 = a;
      const float a1 = sw32(a0, a0);
      const float mu = sum_lo3(a0 + a1) * (1.f / 16.f);
      const float m2 = sum_lo3((a0 - mu) * (a0 - mu) + (a1 - mu) * (a1 - mu));
      gs8(c.ws, ox, v2u{asu(sel(lanes(0xff), asf(pk(a0, a1)), sel(lanes(0x100), mu, m2))), c.tag});
      TS(t11);
      STAMP(11);
      __builtin_amdgcn_raw_buffer_store_b32(c.tag, c.ws, (r | lane) ? OOB : W_EPOCH, 0, 16);
      const u32 id = opq(W_D + (r * 56 + (lane >> 3)) * 128 + (lane & 7) * 16);
      const u32 oy = opq(lane < 8 ? 32 * r + 4 * lane : OOB);
      until(t0 + T5);
      v4u q7[7];
      gather7<1>(c.ws, id, q7, c.tag);
      TS(t15);
      STAMP(15);
      float y0 = a + rbf(kp.y * rbf(col_total(q7)));
      const float y1 = sw32(y0, y0);
      __builtin_amdgcn_raw_buffer_store_b32(pk(y0, y1), mk(p.out, 2048), oy, 0, OAUX);
      STAMP(16);
      TSW(21, t21); TSW(20, t20); TSW(10, t10); TSW(11, t11); TSW(15, t15); TSW(30, (t20 & ~0xffffffffull) | t0);
    }
  }
}

__global__ void code_end_marker() { asm volatile("pf_end:"); }

extern "C" void kernel_launch(void* const* d_in, const int* in_sizes, int n_in, void* d_out, int out_size, void* d_ws,
                              size_t ws_size, hipStream_t stream) {
  Params p;
  p.x = (const u16*)d_in[0]; p.K = (const u16*)d_in[1]; p.V = (const u16*)d_in[2];
  p.wq = (const u16*)d_in[3]; p.wo = (const u16*)d_in[4];
  p.wg = (const u16*)d_in[6]; p.wu = (const u16*)d_in[7]; p.wd = (const u16*)d_in[8];
  p.c = (const u16*)d_in[9]; p.wmod = (const u16*)d_in[10];
  p.out = (u16*)d_out; p.ws = (char*)d_ws + WSOFF;
  (void)hipFuncSetAttribute((const void*)fused_block, hipFuncAttributeMaxDynamicSharedMemorySize, 163840);
  hipLaunchKernelGGL(fused_block, dim3(256), dim3(512), 163840, stream, p);
}
```
